```python
import torch

def _softmax(x, axis=-1):
    return torch.softmax(x, dim=axis)
def _gelu(x):
    return torch.nn.functional.gelu(x, approximate="tanh")
def _silu(x):
    return torch.nn.functional.silu(x)
def _sigmoid(x):
    return torch.sigmoid(x)
def _layernorm(x, axis=-1, eps=1e-5):
    m = x.mean(dim=axis, keepdim=True)
    v = x.var(dim=axis, keepdim=True, unbiased=False)
    return (x - m) / torch.sqrt(v + eps)
def _scatter_add(tgt, idx, src, axis=0):
    return tgt.clone().index_add_(axis, idx, src)
def _segment_sum(src, seg, n):
    out = torch.zeros((n,) + src.shape[1:], dtype=src.dtype)
    return out.index_add_(0, seg, src)
def _pad(x, pw):
    flat = tuple(v for pair in reversed(pw) for v in pair)
    return torch.nn.functional.pad(x, flat)

kernel_name = 'sequential_preLN__linear_attn_block__mixer_channel_mlp__prompt'

def setup_inputs():

    _w = (lambda a: (lambda t: t.to(torch.bfloat16) if t.is_floating_point() else t)(torch.as_tensor(a)))
    def _A_setup():
        B, S, D = 1, 2048, 640
        return {
            "x": _w((lambda *s: torch.randn(*s, dtype=torch.bfloat16) * 0.05)(B, S, D)),
            "wqkv": _w((lambda *s: torch.randn(*s, dtype=torch.bfloat16) * 0.1)(D, 3 * D)),
            "wo": _w((lambda *s: torch.randn(*s, dtype=torch.bfloat16) * 0.05)(D, D)),
        }
    _A_in = _A_setup()

    def _B_setup():
        B, S, D, F = 1, 2048, 640, 1728
        return {
            "x": _w((lambda *s: torch.randn(*s, dtype=torch.bfloat16) * 0.05)(B, S, D)),
            "w1": _w((lambda *s: torch.randn(*s, dtype=torch.bfloat16) * 3.2)(D, F) * (D ** -0.5)),
            "b1": _w((lambda *s: torch.randn(*s, dtype=torch.bfloat16) * 0.8)(F)),
            "w2": _w((lambda *s: torch.randn(*s, dtype=torch.bfloat16) * 0.05)(F, D) * (F ** -0.5)),
            "b2": _w((lambda *s: torch.randn(*s, dtype=torch.bfloat16) * 0.05)(D)),
        }
    _B_in = _B_setup()

    return {
        "A_x": _A_in["x"],
        "A_wqkv": _A_in["wqkv"],
        "A_wo": _A_in["wo"],
        "B_x": _B_in["x"],
        "B_w1": _B_in["w1"],
        "B_b1": _B_in["b1"],
        "B_w2": _B_in["w2"],
        "B_b2": _B_in["b2"],
    }

def reference(A_x, A_wqkv, A_wo, B_x, B_w1, B_b1, B_w2, B_b2):
    def _A_ref(x, wqkv, wo):
        B, S, D = x.shape
        H = 32; Dh = D // H
        qkv = (lambda x, *d: x.permute(*d))((x @ wqkv).reshape(B, S, 3, H, Dh), 2, 0, 3, 1, 4)
        q, k, v = qkv[0], qkv[1], qkv[2]
        phi = lambda t: torch.where(t > 0, t + 1, torch.exp(t)) + 1e-6
        pq, pk = phi(q), phi(k)
        kv = torch.einsum("bhsd,bhse->bhde", pk, v)
        z = torch.einsum("bhsd->bhd", pk)
        num = torch.einsum("bhqd,bhde->bhqe", pq, kv)
        den = torch.einsum("bhqd,bhd->bhq", pq, z)[..., None] + 1e-6
        o = num / den
        return (lambda x, *d: x.permute(*d))(o, 0, 2, 1, 3).reshape(B, S, D) @ wo

    def _B_ref(x, w1, b1, w2, b2):
        y = _layernorm(x, axis=-1)
        h = _gelu(torch.einsum("bsd,df->bsf", y, w1) + b1)
        return torch.einsum("bsf,fd->bsd", h, w2) + b2

    x = A_x
    h = x + _A_ref(_layernorm(x, axis=-1), A_wqkv, A_wo)
    h2 = h + _B_ref(_layernorm(h, axis=-1), B_w1, B_b1, B_w2, B_b2)
    return h2
```

```cpp
#include <hip/hip_runtime.h>
#include <hip/hip_bf16.h>
typedef unsigned short u16;
constexpr int S=2048, D=640, H=32, DH=20, F=1728;
__device__ __forceinline__ float b2f(u16 b){ return __uint_as_float(((unsigned)b)<<16); }
__device__ __forceinline__ u16 f2b(float f){ unsigned u=__float_as_uint(f); u += 0x7FFFu + ((u>>16)&1u); return (u16)(u>>16); }
__device__ __forceinline__ float rb(float f){ return b2f(f2b(f)); }

__global__ void k_ln(const u16* x, u16* y){
  int r=blockIdx.x, l=threadIdx.x; float v[10]; float s=0;
  for(int i=0;i<10;i++){ v[i]=b2f(x[r*D+i*64+l]); s+=v[i]; }
  for(int o=32;o;o>>=1) s+=__shfl_xor(s,o);
  float m=s/D; float q=0;
  for(int i=0;i<10;i++){ float d=v[i]-m; q+=d*d; }
  for(int o=32;o;o>>=1) q+=__shfl_xor(q,o);
  float mb=rb(m), vb=rb(q/D); float sd=rb(sqrtf(rb(vb+1e-5f)));
  for(int i=0;i<10;i++) y[r*D+i*64+l]=f2b(rb(v[i]-mb)/sd);
}
__global__ void k_gemm(const u16* A,const u16* B,u16* C,int M,int N,int K,int mode,const u16* bias,const u16* res){
  __shared__ float sa[16][17], sb[16][17];
  int tx=threadIdx.x&15, ty=threadIdx.x>>4; int n=blockIdx.x*16+tx, m=blockIdx.y*16+ty; float acc=0;
  for(int k0=0;k0<K;k0+=16){
    sa[ty][tx]=b2f(A[(size_t)m*K+k0+tx]); sb[ty][tx]=b2f(B[(size_t)(k0+ty)*N+n]);
    __syncthreads();
    for(int k=0;k<16;k++) acc+=sa[ty][k]*sb[k][tx];
    __syncthreads();
  }
  float c=rb(acc);
  if(mode==1){ c=rb(c+b2f(bias[n])); float t=tanhf(0.7978845608028654f*(c+0.044715f*c*c*c)); c=0.5f*c*(1.f+t); }
  else if(mode==2){ c=b2f(res[(size_t)m*N+n])+c; }
  else if(mode==3){ c=rb(c+b2f(bias[n])); c=b2f(res[(size_t)m*N+n])+c; }
  C[(size_t)m*N+n]=f2b(c);
}
__device__ __forceinline__ float phi(float t){ float r = t>0.f ? rb(t+1.f) : rb(expf(t)); return rb(r+1e-6f); }
__global__ void k_kv(const u16* qkv, float* kv){
  int h=blockIdx.x, t=threadIdx.x; if(t>=420) return;
  int d = t<400? t/20 : t-400, e = t<400? t%20 : 0; float acc=0;
  for(int s=0;s<S;s++){ float pk=phi(b2f(qkv[s*1920+640+h*20+d])); float v = t<400? b2f(qkv[s*1920+1280+h*20+e]) : 1.f; acc+=pk*v; }
  kv[h*420+t]=rb(acc);
}
__global__ void k_o(const u16* qkv,const float* kv,u16* o){
  int s=blockIdx.x, c=threadIdx.x; int h=c/20,e=c%20; float num=0,den=0;
  for(int d=0;d<20;d++){ float pq=phi(b2f(qkv[s*1920+h*20+d])); num+=pq*kv[h*420+d*20+e]; den+=pq*kv[h*420+400+d]; }
  num=rb(num); den=rb(rb(den)+1e-6f); o[s*D+c]=f2b(num/den);
}
extern "C" void kernel_launch(void* const* d_in, const int* in_sizes, int n_in, void* d_out, int out_size, void* d_ws, size_t ws_size, hipStream_t stream){
  const u16* x=(const u16*)d_in[0]; const u16* wqkv=(const u16*)d_in[1]; const u16* wo=(const u16*)d_in[2];
  const u16* w1=(const u16*)d_in[4]; const u16* b1=(const u16*)d_in[5]; const u16* w2=(const u16*)d_in[6]; const u16* b2=(const u16*)d_in[7];
  char* ws=(char*)d_ws;
  u16* xn=(u16*)ws; u16* qkv=(u16*)(ws+(4<<20)); float* kv=(float*)(ws+(16<<20)); u16* o=(u16*)(ws+(20<<20)); u16* h=(u16*)(ws+(24<<20));
  u16* y1=(u16*)(ws+(28<<20)); u16* y2=(u16*)(ws+(32<<20)); u16* hid=(u16*)(ws+(36<<20));
  k_ln<<<S,64,0,stream>>>(x,xn);
  k_gemm<<<dim3(1920/16,S/16),256,0,stream>>>(xn,wqkv,qkv,S,1920,D,0,nullptr,nullptr);
  k_kv<<<H,448,0,stream>>>(qkv,kv);
  k_o<<<S,640,0,stream>>>(qkv,kv,o);
  k_gemm<<<dim3(D/16,S/16),256,0,stream>>>(o,wo,h,S,D,D,2,nullptr,x);
  k_ln<<<S,64,0,stream>>>(h,y1);
  k_ln<<<S,64,0,stream>>>(y1,y2);
  k_gemm<<<dim3(F/16,S/16),256,0,stream>>>(y2,w1,hid,S,F,D,1,b1,nullptr);
  k_gemm<<<dim3(D/16,S/16),256,0,stream>>>(hid,w2,(u16*)d_out,S,D,F,3,b2,h);
}
```

```cpp
#include <hip/hip_runtime.h>
#include <stdint.h>
#pragma clang diagnostic ignored "-Wint-to-pointer-cast"

typedef unsigned short u16;
typedef unsigned u32;
typedef unsigned long long u64;
typedef float f2 __attribute__((ext_vector_type(2)));
typedef float f4 __attribute__((ext_vector_type(4)));
typedef unsigned u2 __attribute__((ext_vector_type(2)));
typedef unsigned u4 __attribute__((ext_vector_type(4)));
typedef short s4 __attribute__((ext_vector_type(4)));
typedef __bf16 bf2 __attribute__((ext_vector_type(2)));
typedef __bf16 bf8 __attribute__((ext_vector_type(8)));
typedef __amdgpu_buffer_rsrc_t rsrc_t;
#define LAS __attribute__((address_space(3)))
#define GAS __attribute__((address_space(1)))
#define DI __device__ __forceinline__

template <int I> struct IC { static constexpr int v = I; constexpr operator int() const { return I; } };
template <int B, int E, class F> DI void sfor(F&& f) {
  if constexpr (B < E) { f(IC<B>{}); sfor<B + 1, E>(f); }
}

constexpr u32 WS_EPOCH = 0, WS_FRT = 4096, WS_FKV = 8192, WS_FO = 16384, WS_FH = 24576, WS_FHID = 32768;
constexpr u32 WS_KVP = 65536;
constexpr u32 WS_OB = 2u << 20;
constexpr u32 WS_HB = WS_OB + 32 * 81920;
constexpr u32 WS_ST = WS_HB + 32 * 81920;
constexpr u32 WS_CS1 = WS_ST + 2048 * 64;
constexpr u32 WS_W1T = WS_CS1 + 32768;
constexpr u32 WS_W2T = WS_W1T + 108 * 20480;
constexpr u32 WS_HID = WS_W2T + 8 * 54 * 5120;
constexpr u32 WS_END = WS_HID + 32 * 4 * 54 * 1024;
constexpr u32 WS_PROF = WS_END;
constexpr u32 OOB = 0x7FFFFF00u;
constexpr int SC1 = 16;

constexpr u32 L_X = 0, L_RING = 81920;
constexpr u32 L_WO = 0, L_W1A = 102400, L_CB = 152576, L_IMG = 155648, L_W1B = 0, L_RED = 0;

DI f4 mfma(u4 a, u4 b, f4 c) {
  return __builtin_amdgcn_mfma_f32_16x16x32_bf16(__builtin_bit_cast(bf8, a), __builtin_bit_cast(bf8, b), c, 0, 0, 0);
}
DI void mm(f4& c, u4 a, u4 b) { asm volatile("v_mfma_f32_16x16x32_bf16 %0, %1, %2, %0" : "+v"(c) : "v"(a), "v"(b) : "memory"); }
DI void mm0(f4& c, u4 a, u4 b) { asm volatile("v_mfma_f32_16x16x32_bf16 %0, %1, %2, 0" : "=&v"(c) : "v"(a), "v"(b) : "memory"); }
DI void mm_av(f4& c, u4 a, u4 b) { asm volatile("v_mfma_f32_16x16x32_bf16 %0, %1, %2, %0" : "+v"(c) : "a"(a), "v"(b) : "memory"); }
DI void mm0_av(f4& c, u4 a, u4 b) { asm volatile("v_mfma_f32_16x16x32_bf16 %0, %1, %2, 0" : "=&v"(c) : "a"(a), "v"(b) : "memory"); }
DI void mm_va(f4& c, u4 a, u4 b) { asm volatile("v_mfma_f32_16x16x32_bf16 %0, %1, %2, %0" : "+v"(c) : "v"(a), "a"(b) : "memory"); }
#define MM_DONE() asm volatile("s_nop 7\n\ts_nop 1" ::: "memory")
DI void pin(f4& c) { asm volatile("" : "+v"(c)); }
DI u32 opaque(u32 v) { asm volatile("" : "+v"(v)); return v; }

DI u32 pk2(float a, float b) { f2 v = {a, b}; return __builtin_bit_cast(u32, __builtin_convertvector(v, bf2)); }
DI float rbf(float a) { return __builtin_bit_cast(float, pk2(a, a) << 16); }
DI float blo(u32 v) { return __builtin_bit_cast(float, v << 16); }
DI float bhi(u32 v) { return __builtin_bit_cast(float, v & 0xFFFF0000u); }
DI float asf(u32 v) { return __builtin_bit_cast(float, v); }
DI u32 asu(float v) { return __builtin_bit_cast(u32, v); }

template <int OFF> DI void dma(rsrc_t r, u32 voff, u32 soff, u32 lds) {
  asm volatile("s_mov_b32 m0, %0\n\ts_nop 0\n\tbuffer_load_dwordx4 %1, %2, %3 offen offset:%4 lds" ::"s"(lds), "v"(voff), "s"(r), "s"(soff), "n"(OFF) : "memory");
}
template <int OFF> DI void dma_c(rsrc_t r, u32 voff, u32 soff) {
  asm volatile("buffer_load_dwordx4 %0, %1, %2 offen offset:%3 lds" ::"v"(voff), "s"(r), "s"(soff), "n"(OFF) : "memory");
}
DI void dma_sc1(rsrc_t r, u32 voff, u32 soff, u32 lds) {
  asm volatile("s_mov_b32 m0, %0\n\ts_nop 0\n\tbuffer_load_dwordx4 %1, %2, %3 offen sc1 lds" ::"s"(lds), "v"(voff), "s"(r), "s"(soff) : "memory");
}
template <int OFF> DI void ldv(u4& d, rsrc_t r, u32 voff, u32 soff) {
  asm volatile("buffer_load_dwordx4 %0, %1, %2, %3 offen offset:%4 sc1" : "=v"(d) : "v"(voff), "s"(r), "s"(soff), "n"(OFF) : "memory");
}
template <int OFF> DI void lda(u4& d, rsrc_t r, u32 voff, u32 soff) {
  asm volatile("buffer_load_dwordx4 %0, %1, %2, %3 offen offset:%4 sc1" : "=a"(d) : "v"(voff), "s"(r), "s"(soff), "n"(OFF) : "memory");
}
DI void ldv_plain(u4& d, rsrc_t r, u32 voff, u32 soff) {
  asm volatile("buffer_load_dwordx4 %0, %1, %2, %3 offen" : "=v"(d) : "v"(voff), "s"(r), "s"(soff) : "memory");
}
template <int N> DI void waitv() { asm volatile("s_waitcnt vmcnt(%0)" ::"n"(N) : "memory"); }
DI void landed(u4& d) { asm volatile("" : "+v"(d)); }
#define BAR() asm volatile("s_waitcnt lgkmcnt(0)\n\ts_barrier" ::: "memory")
DI u4 lds128(u32 a) { return *(LAS u4*)(a); }
DI u2 lds64(u32 a) { return *(LAS u2*)(a); }
DI float lds32(u32 a) { return *(LAS float*)(a); }
DI void sts128(u32 a, u4 v) { *(LAS u4*)(a) = v; }
DI void sts64(u32 a, u2 v) { *(LAS u2*)(a) = v; }
DI void sts32(u32 a, float v) { *(LAS float*)(a) = v; }
DI u4 ldstr(u32 a0, u32 a1) {
  u2 x = __builtin_bit_cast(u2, __builtin_amdgcn_ds_read_tr16_b64_v4i16((LAS s4*)(a0)));
  u2 y = __builtin_bit_cast(u2, __builtin_amdgcn_ds_read_tr16_b64_v4i16((LAS s4*)(a1)));
  return u4{x.x, x.y, y.x, y.y};
}
DI float xsum16_32(float v) {
  auto r = __builtin_amdgcn_permlane16_swap(asu(v), asu(v), false, false);
  v = asf((u32)r[0]) + asf((u32)r[1]);
  auto q = __builtin_amdgcn_permlane32_swap(asu(v), asu(v), false, false);
  return asf((u32)q[0]) + asf((u32)q[1]);
}
DI void poll(const char* ws, u32 off, u64 tag) {
  const GAS u64* f = (const GAS u64*)(ws + off);
#pragma nounroll
  for (u32 it = 0; it < (1u << 18); ++it) {
    u64 v = __hip_atomic_load(f, __ATOMIC_RELAXED, __HIP_MEMORY_SCOPE_AGENT);
    if (__all(v == tag)) break;
    __builtin_amdgcn_s_sleep(1);
  }
  __builtin_amdgcn_fence(__ATOMIC_ACQUIRE, "wavefront");
  asm volatile("" ::: "memory");
}
DI void setflag(char* ws, u32 off, u64 tag, u32 lane) {
  if (lane == 0) __hip_atomic_store((GAS u64*)(ws + off), tag, __ATOMIC_RELAXED, __HIP_MEMORY_SCOPE_AGENT);
  asm volatile("" ::: "memory");
}
DI float phi(float t) {
  return fmaxf(t, 0.f) + (__builtin_amdgcn_exp2f(fminf(t, 0.f) * 1.4426950408889634f) + 1e-6f);
}
DI float sel4(f4 v, u32 i) { return i == 0 ? v[0] : i == 1 ? v[1] : i == 2 ? v[2] : v[3]; }
DI void st128(u4 v, rsrc_t r, u32 voff, u32 soff) { __builtin_amdgcn_raw_buffer_store_b128(v, r, voff, soff, SC1); asm volatile("s_nop 1" ::"v"(v) : "memory"); }
DI void st64(u2 v, rsrc_t r, u32 voff, u32 soff) { __builtin_amdgcn_raw_buffer_store_b64(v, r, voff, soff, SC1); }
DI void publish20(const u32* v, rsrc_t r, u32 lane, u32 so, u32 j) {
  if ((j & 1) == 0) {
    st128(u4{v[0], v[1], v[2], v[3]}, r, lane * 16, so);
    st128(u4{v[4], v[5], v[6], v[7]}, r, lane * 16, so + 1024);
    st64(u2{v[8], v[9]}, r, lane * 16, so + 2048);
  } else {
    st64(u2{v[0], v[1]}, r, lane * 16 + 8, so + 2048);
    st128(u4{v[2], v[3], v[4], v[5]}, r, lane * 16, so + 3072);
    st128(u4{v[6], v[7], v[8], v[9]}, r, lane * 16, so + 4096);
  }
}

#define STAMP(i)

__global__ __launch_bounds__(256, 1) void fused_block(const u16* __restrict__ x, const u16* __restrict__ wqkv, const u16* __restrict__ wo,
                                                      const u16* __restrict__ w1, const u16* __restrict__ b1, const u16* __restrict__ w2,
                                                      const u16* __restrict__ b2, u16* __restrict__ out, char* __restrict__ ws) {
  __shared__ __attribute__((aligned(1024))) char smem[163840];
  asm volatile("" ::"v"((u32)(uintptr_t)(LAS char*)smem));
  const u32 tid = threadIdx.x, lane = tid & 63, lr = lane & 15, g = lane >> 4;
  const u32 w = __builtin_amdgcn_readfirstlane(tid >> 6);
  const u32 b = blockIdx.x, xg = b & 7, rr = b >> 3, p = 4 * xg + (rr >> 3), j = rr & 7;
  STAMP(0);
  const rsrc_t rX = __builtin_amdgcn_make_buffer_rsrc((void*)x, 0, 2048 * 1280, 0x00020000);
  const rsrc_t rQ = __builtin_amdgcn_make_buffer_rsrc((void*)wqkv, 0, 640 * 3840, 0x00020000);
  const rsrc_t rW = __builtin_amdgcn_make_buffer_rsrc((void*)ws, 0, WS_END, 0x00020000);
  const u32 l16 = lane * 16;
  u4 ONES = {0x3F803F80u, 0x3F803F80u, 0x3F803F80u, 0x3F803F80u};
  asm volatile("" : "+v"(ONES));

  u4 ep;
  ldv<0>(ep, rW, 0, WS_EPOCH);
  u32 voffW[5];
#pragma unroll
  for (int i = 0; i < 5; i++) {
    u32 f = 64 * i + lane, sg = f / 10, ch = f - sg * 10;
    voffW[i] = (sg ^ (((sg >> 3) & 1) << 2)) * 3840 + ch * 16 - (i & 3) * 1024;
  }
  const u32 voffX = (lane >> 3) * 1280 + ((lane & 7) ^ (lane >> 3)) * 16;
  const u32 voffX1 = voffX + 8 * 1280 - 1024;
  const u32 rot = (xg + 3 * (p & 3)) % 10;
  const u32 soX = (p * 64 + 16 * w) * 1280, soW = (w >> 1) * 32 * 3840 + ((w & 1) + 1) * 1280 + j * 160;
  auto issueW = [&](auto Q, u32 so, u32 ld) {
    constexpr int q = Q;
    if constexpr (q == 0) dma<0>(rQ, voffW[0], so, ld);
    else if constexpr (q < 4) dma_c<q * 1024>(rQ, voffW[q], so);
    else dma<0>(rQ, voffW[4], so, ld + 4096);
  };
  auto issueKV = [&](auto Q, u32 i) {
    constexpr int q = Q;
    u32 st = i + rot; st = st >= 10 ? st - 10 : st;
    if constexpr (q == 0) dma<0>(rX, voffX, soX + st * 128, L_X + st * 8192 + w * 2048);
    else if constexpr (q == 1) dma_c<1024>(rX, voffX1, soX + st * 128);
    else issueW(IC<q - 2>{}, soW + st * 64 * 3840, L_RING + (i & 3) * 20480 + w * 5120);
  };
  const u32 rotq = (xg + (p & 3)) % 5;
  auto issueQ = [&](auto Q, u32 qi, u32 slot) {
    u32 qs = qi + rotq; qs = qs >= 5 ? qs - 5 : qs;
    issueW(Q, (qs * 128 + w * 32) * 3840 + j * 160, L_RING + slot * 20480 + w * 5120);
  };
#pragma unroll
  for (u32 i = 0; i < 3; i++) sfor<0, 7>([&](auto Q) { issueKV(Q, i); });
  u32 voffO[25];
#pragma unroll
  for (int i = 0; i < 25; i++) {
    u32 f = 64 * i + lane, sg = f / 10, ch = f - sg * 10;
    u32 om = sg & 31, ka = om ^ (((om >> 3) & 1) << 2), i40 = 8 * (sg >> 5) + (ka & 7);
    u32 hf = i40 >= 20 ? 1u : 0u, id = i40 - 20 * hf, hh = id / 5;
    voffO[i] = opaque((80 * hf + 20 * hh + 5 * (ka >> 3) + (id - 5 * hh)) * 1280 + ch * 16 + 4096 - (i & 3) * 1024);
  }
  const u32 mh = w & 1, nh = w >> 1;
  const u32 xa0 = L_X + (lr >> 3) * 1024 + (lr & 7) * 128 + ((g ^ (lr & 7)) << 4);
  const u32 tr0 = (8 * g + (lr >> 2) + 4 * (g & 1)) * 160 + (lr & 3) * 8;
  const u32 tr1 = (8 * g + (lr >> 2) + 4 * ((g & 1) ^ 1)) * 160 + (lr & 3) * 8;
  f4 acc[2][5], cs[3], rsum = {0, 0, 0, 0}, gram = {0, 0, 0, 0};
#pragma unroll
  for (int i = 0; i < 5; i++) { acc[0][i] = f4{0, 0, 0, 0}; acc[1][i] = f4{0, 0, 0, 0}; pin(acc[0][i]); pin(acc[1][i]); }
#pragma unroll
  for (int i = 0; i < 3; i++) { cs[i] = f4{0, 0, 0, 0}; pin(cs[i]); }
  pin(rsum); pin(gram);
  auto kv_stage = [&](auto MODE, u32 i) {
    constexpr int mode = MODE;
    u32 st = i + rot; st = st >= 10 ? st - 10 : st;
    const u32 xb0 = xa0 + st * 8192 + mh * 4096 + nh * 2048, xb1 = xb0 ^ 2048;
    const u32 wb = L_RING + (i & 3) * 20480;
    const u32 oP = wb + nh * 96 + mh * 5120, oR = wb + nh * 96 + (mh ^ 1) * 5120, oC = wb + 64 + nh * 5120;
    const u32 p0 = tr0 + oP, p1 = tr1 + oP, r0 = tr0 + oR, r1 = tr1 + oR, c0 = tr0 + oC, c1 = tr1 + oC;
    auto iss = [&](auto Q) {
      constexpr int q = Q;
      if constexpr (mode == 0) issueKV(Q, i + 3);
      else if constexpr (mode < 3 && q < 5) issueQ(Q, mode - 1, mode + 1);
    };
    u4 xf0 = lds128(xb0), wf0 = ldstr(p0, p1), xf1 = lds128(xb1), wf1 = ldstr(p0 + 32, p1 + 32), wf2 = ldstr(c0, c1);
    u4 wf3 = ldstr(r0, r1), wf4 = ldstr(r0 + 32, r1 + 32);
    iss(IC<0>{});
    mm(rsum, xf0, ONES); mm(gram, xf0, xf0);
    u4 yf0 = lds128(xb0 ^ 64), yf1 = lds128(xb1 ^ 64);
    mm(acc[0][0], xf0, wf0); mm(acc[1][0], xf1, wf0); mm(cs[0], ONES, wf0);
    iss(IC<1>{});
    u4 vf0 = ldstr(p0 + 10240, p1 + 10240);
    mm(acc[0][1], xf0, wf1); mm(acc[1][1], xf1, wf1); mm(cs[1], ONES, wf1);
    u4 vf1 = ldstr(p0 + 10272, p1 + 10272);
    mm(acc[0][2], xf0, wf2); mm(acc[1][2], xf1, wf2); mm(cs[2], ONES, wf2);
    iss(IC<2>{});
    u4 vf2 = ldstr(c0 + 10240, c1 + 10240);
    mm(acc[0][3], xf0, wf3); mm(acc[1][3], xf1, wf3);
    u4 vf3 = ldstr(r0 + 10240, r1 + 10240);
    mm(acc[0][4], xf0, wf4); mm(acc[1][4], xf1, wf4);
    u4 vf4 = ldstr(r0 + 10272, r1 + 10272);
    iss(IC<3>{});
    mm(rsum, yf0, ONES); mm(gram, yf0, yf0);
    mm(acc[0][0], yf0, vf0); mm(acc[1][0], yf1, vf0);
    iss(IC<4>{});
    mm(cs[0], ONES, vf0);
    mm(acc[0][1], yf0, vf1); mm(acc[1][1], yf1, vf1); mm(cs[1], ONES, vf1);
    iss(IC<5>{});
    mm(acc[0][2], yf0, vf2); mm(acc[1][2], yf1, vf2); mm(cs[2], ONES, vf2);
    iss(IC<6>{});
    mm(acc[0][3], yf0, vf3); mm(acc[1][3], yf1, vf3);
    mm(acc[0][4], yf0, vf4); mm(acc[1][4], yf1, vf4);
  };
  STAMP(1);
#pragma nounroll
  for (u32 i = 0; i < 7; i++) {
    waitv<14>(); BAR();
    kv_stage(IC<0>{}, i);
  }
  waitv<14>(); BAR(); kv_stage(IC<1>{}, 7);
  waitv<12>(); BAR(); kv_stage(IC<2>{}, 8);
  waitv<10>(); BAR(); kv_stage(IC<3>{}, 9);
  MM_DONE();
  STAMP(2);
  landed(ep);
  const u64 tag = (((u64)(u32)__builtin_amdgcn_readfirstlane(ep.y) << 32) | (u32)__builtin_amdgcn_readfirstlane(ep.x)) + 1;
  float qmb, qrs;
  {
    const u32 LS = L_RING, LC = L_RING + 512, EX = L_RING + 20480;
    pin(rsum); pin(gram);
    {
      float m = sel4(rsum, lr & 3) * (1.f / 640.f);
      float v = fmaxf(sel4(gram, lr & 3) * (1.f / 640.f) - m * m, 0.f);
      float sd = rbf(__builtin_sqrtf(rbf(rbf(v) + 1e-5f)));
      if ((lr >> 2) == g) sts64(LS + ((2 * mh + nh) * 16 + lr) * 8, u2{asu(rbf(m)), asu(__builtin_amdgcn_rcpf(sd))});
    }
    const u32 iP = 3 * nh + 5 * mh, iR = 3 * nh + 5 * (mh ^ 1), iC = 2 + 5 * nh;
#pragma unroll
    for (int i = 0; i < 3; i++) pin(cs[i]);
    if (g == 0) { sts32(LC + iP * 64 + lr * 4, cs[0][0]); sts32(LC + iP * 64 + 64 + lr * 4, cs[1][0]); sts32(LC + iC * 64 + lr * 4, cs[2][0]); }
    BAR();
    f4 st[2][2];
#pragma unroll
    for (int m = 0; m < 2; m++) {
      u32 a = LS + ((2 * mh + (nh ^ m)) * 16 + 4 * g) * 8;
      st[m][0] = __builtin_bit_cast(f4, lds128(a)); st[m][1] = __builtin_bit_cast(f4, lds128(a + 16));
    }
    float cR0 = lds32(LC + iR * 64 + lr * 4), cR1 = lds32(LC + iR * 64 + 64 + lr * 4);
    { u2 q = lds64(LS + (16 * w + lr) * 8); qmb = asf(q.x); qrs = asf(q.y); }
    sfor<0, 5>([&](auto N) {
      constexpr int n = N;
      pin(acc[0][n]); pin(acc[1][n]);
      float c = n < 3 ? cs[n < 3 ? n : 0][0] : (n == 3 ? cR0 : cR1);
      float t[8];
#pragma unroll
      for (int m = 0; m < 2; m++)
#pragma unroll
        for (int i = 0; i < 4; i++) t[4 * m + i] = (acc[m][n][i] - st[m][i >> 1][2 * (i & 1)] * c) * st[m][i >> 1][2 * (i & 1) + 1];
      bool isk = n < 2 ? mh == 0 : (n == 2 ? nh == 0 : mh == 1);
      if (isk) {
#pragma unroll
        for (int i = 0; i < 8; i++) t[i] = phi(t[i]);
      }
      u32 idx = n < 2 ? iP + n : (n == 2 ? iC : iR + n - 3);
      u32 a = EX + mh * 10240 + idx * 1024 + l16;
      sts64(a + nh * 8, u2{pk2(t[0], t[1]), pk2(t[2], t[3])});
      sts64(a + (nh ^ 1) * 8, u2{pk2(t[4], t[5]), pk2(t[6], t[7])});
    });
    BAR();
    const u32 ex = EX + w * 1024 + l16;
    f4 kv[2][2], zz[2];
#pragma unroll
    for (int a = 0; a < 2; a++) { kv[a][0] = kv[a][1] = zz[a] = f4{0, 0, 0, 0}; }
#pragma unroll
    for (int m = 0; m < 2; m++) {
      u4 k0 = lds128(ex + m * 10240), k1 = lds128(ex + m * 10240 + 1024), v0 = lds128(ex + m * 10240 + 5120), v1 = lds128(ex + m * 10240 + 6144);
      kv[0][0] = mfma(k0, v0, kv[0][0]); kv[0][1] = mfma(k0, v1, kv[0][1]);
      kv[1][0] = mfma(k1, v0, kv[1][0]); kv[1][1] = mfma(k1, v1, kv[1][1]);
      zz[0] = mfma(k0, ONES, zz[0]); zz[1] = mfma(k1, ONES, zz[1]);
    }
    const u32 so = WS_KVP + ((j * 32 + p) * 4 + w) * 1680;
#pragma unroll
    for (int a = 0; a < 2; a++) {
      u32 dq = 4 * a + g - w;
#pragma unroll
      for (int c = 0; c < 2; c++) {
        u32 e = 16 * c + lr - 4 * w;
        st128(__builtin_bit_cast(u4, kv[a][c]), rW, (dq < 5 && e < 20) ? e * 80 + dq * 16 : OOB, so);
      }
      st128(__builtin_bit_cast(u4, zz[a]), rW, (dq < 5 && lr == 0) ? 1600 + dq * 16 : OOB, so);
    }
  }
  waitv<0>();
  setflag(ws, WS_FKV + ((j * 4 + w) * 32 + p) * 8, tag, lane);
  STAMP(3);
  BAR();
  sfor<0, 5>([&](auto Q) { issueQ(Q, 2, 0); });
  sfor<0, 5>([&](auto Q) { issueQ(Q, 3, 1); });
  u4 rt[5];
  const bool rt_w1 = b < 108;
  const u32 ru = rt_w1 ? b : b - 108, rua = ru / 27, rub = ru - rua * 27;
  {
    const rsrc_t rS = __builtin_amdgcn_make_buffer_rsrc((void*)(rt_w1 ? w1 : w2), 0, b < 216 ? 640 * 3456 : 0, 0x00020000);
    const u32 pitch = rt_w1 ? 3456 : 1280;
    const u32 vo = (8 * w + (lane >> 3)) * pitch + (lane & 7) * 16;
#pragma unroll
    for (int q = 0; q < 5; q++) {
      u32 B = 5 * ru + q, cg = B / 54;
      ldv_plain(rt[q], rS, vo, rt_w1 ? (rua * 160 + q * 32) * 3456 + rub * 128 : (B - cg * 54) * 32 * 1280 + cg * 128);
    }
  }
  f4 qa[5], qc[5];
  auto q_stage = [&](auto FIRST, auto ISS, u32 qi, u32 slot) {
    u32 qs = qi + rotq; qs = qs >= 5 ? qs - 5 : qs;
    const u32 xb = xa0 + qs * 16384 + w * 2048, xc = xb ^ 64;
    const u32 a0 = L_RING + slot * 20480 + tr0, a1 = L_RING + slot * 20480 + tr1;
    u4 xf[4], wf[5];
    xf[0] = lds128(xb);
#pragma unroll
    for (int n = 0; n < 5; n++) wf[n] = ldstr(a0 + n * 32, a1 + n * 32);
    xf[1] = lds128(xc); xf[2] = lds128(xb + 8192); xf[3] = lds128(xc + 8192);
    sfor<0, 4>([&](auto KS) {
      constexpr int ks = KS;
      sfor<0, 5>([&](auto N) {
        constexpr int n = N;
        u4 cur = wf[n];
        if constexpr (FIRST && ks == 0) { mm0(qa[n], cur, xf[ks]); mm0(qc[n], cur, ONES); }
        else { mm(qa[n], cur, xf[ks]); mm(qc[n], cur, ONES); }
        if constexpr (ks < 3) wf[n] = ldstr(a0 + (ks + 1) * 5120 + n * 32, a1 + (ks + 1) * 5120 + n * 32);
        if constexpr (ISS && ks == 0) issueQ(N, 4, 2);
      });
    });
  };
  q_stage(IC<1>{}, IC<0>{}, 0, 2);
  BAR(); q_stage(IC<0>{}, IC<1>{}, 1, 3);
  waitv<15>(); BAR(); q_stage(IC<0>{}, IC<0>{}, 2, 0);
  waitv<10>(); BAR(); q_stage(IC<0>{}, IC<0>{}, 3, 1);
  waitv<0>(); BAR(); q_stage(IC<0>{}, IC<0>{}, 4, 2);
  MM_DONE();
  STAMP(4);
  u2 xres[5];
#pragma unroll
  for (int n = 0; n < 5; n++) {
    u32 ch = 10 * j + 2 * n + (g >> 1);
    xres[n] = lds64(L_X + (ch >> 3) * 8192 + (2 * w + (lr >> 3)) * 1024 + (lr & 7) * 128 + (((ch & 7) ^ (lr & 7)) << 4) + (g & 1) * 8);
  }
  {
    u32 row = 8 * w + (lane >> 3);
#pragma unroll
    for (int q = 0; q < 5; q++) { landed(rt[q]); sts128(L_RING + q * 4096 + row * 128 + (((lane & 7) ^ (((row >> 1) & 3) << 1)) << 4), rt[q]); }
    sts128(L_IMG + tid * 16, u4{0, 0, 0, 0}); sts128(L_IMG + 4096 + tid * 16, u4{0, 0, 0, 0});
  }
  BAR();
  poll(ws, WS_FKV + ((j * 4 + w) * 32 + (lane & 31)) * 8, tag);
  STAMP(5);
  u32 pq[5][2];
  {
    const u32 so = WS_KVP + (j * 32 * 4 + w) * 1680;
    const u32 v1 = lane < 41 ? 1024 + l16 : OOB;
    u4 a[16], c[16];
    sfor<0, 16>([&](auto Q) { ldv<0>(a[Q], rW, l16, so + Q * 6720); ldv<0>(c[Q], rW, v1, so + Q * 6720); });
    {
      u32 r0 = 4 * g + (lr >> 2);
      u32 ta = L_RING + r0 * 128 + (((2 * w + ((lr >> 1) & 1)) ^ (((r0 >> 1) & 3) << 1)) << 4) + (lr & 1) * 8;
      f4 csum = {0, 0, 0, 0};
      const u32 vo = b < 216 ? l16 : OOB;
#pragma unroll
      for (int q = 0; q < 5; q++) {
        u4 f = ldstr(ta + q * 4096, ta + q * 4096 + 2048);
        csum = mfma(f, ONES, csum);
        u32 B = 5 * ru + q, cg = B / 54, tau = 4 * cg + w, jj = tau / 5;
        st128(f, rW, vo, rt_w1 ? WS_W1T + ((4 * rub + w) * 20 + rua * 5 + q) * 1024 : WS_W2T + ((jj * 54 + (B - cg * 54)) * 5 + (tau - jj * 5)) * 1024);
      }
      st128(__builtin_bit_cast(u4, csum), rW, (rt_w1 && lr == 0) ? g * 16 : OOB, WS_CS1 + rua * 6912 + (4 * rub + w) * 64);
    }
    asm volatile("" ::: "memory");
#pragma unroll
    for (int n = 0; n < 5; n++) {
      pin(qa[n]); pin(qc[n]);
      float t[4];
#pragma unroll
      for (int i = 0; i < 4; i++) t[i] = phi((qa[n][i] - qmb * qc[n][i]) * qrs);
      pq[n][0] = opaque(pk2(t[0], t[1])); pq[n][1] = opaque(pk2(t[2], t[3]));
    }
    f4 s0 = {0, 0, 0, 0}, s1 = {0, 0, 0, 0};
    sfor<0, 16>([&](auto Q) {
      waitv<30 + 6>(); landed(a[Q]); landed(c[Q]);
      s0 += __builtin_bit_cast(f4, a[Q]); s1 += __builtin_bit_cast(f4, c[Q]);
      pin(s0); pin(s1);
      ldv<0>(a[Q], rW, l16, so + (16 + Q) * 6720); ldv<0>(c[Q], rW, v1, so + (16 + Q) * 6720);
    });
    sfor<0, 16>([&](auto Q) {
      waitv<30 - 2 * Q>(); landed(a[Q]); landed(c[Q]);
      s0 += __builtin_bit_cast(f4, a[Q]); s1 += __builtin_bit_cast(f4, c[Q]);
    });
#pragma unroll
    for (int h = 0; h < 2; h++) {
      f4 s = h ? s1 : s0;
      u32 ck = lane + 64 * h, e = ck / 5, dq = ck - e * 5;
      u32 gs = dq + w, grp = e / 5, ii = e - grp * 5;
      u2 v = {pk2(s[0], s[1]), pk2(s[2], s[3])};
      u32 base = L_IMG + w * 2048 + (gs & 3) * 256 + (gs >> 2) * 8;
      if (e < 20) sts64(base + (ii < 4 ? (4 * grp + ii) * 16 : 1024 + 4 * grp * 16), v);
      if (h == 1 && e == 20) {
#pragma unroll
        for (int q = 0; q < 4; q++) sts64(base + 1024 + (4 * q + 1) * 16, v);
      }
    }
  }
  BAR();
  if (w == 0) setflag(ws, WS_FRT + b * 8, tag, lane);
  STAMP(6);
  {
    float o[20];
#pragma unroll
    for (int h = 0; h < 4; h++) {
      u4 bq = {pq[h][0], pq[h][1], pq[h + 1][0], pq[h + 1][1]};
      f4 z4 = {0, 0, 0, 0};
      f4 d0 = mfma(lds128(L_IMG + h * 2048 + l16), bq, z4);
      f4 d1 = mfma(lds128(L_IMG + h * 2048 + 1024 + l16), bq, z4);
      float rd = __builtin_amdgcn_rcpf(rbf(rbf(d1[1]) + 1e-6f));
#pragma unroll
      for (int i = 0; i < 4; i++) o[5 * h + i] = rbf(d0[i]) * rd;
      o[5 * h + 4] = rbf(d1[0]) * rd;
    }
    u32 ov[10];
#pragma unroll
    for (int i = 0; i < 10; i++) ov[i] = pk2(o[2 * i], o[2 * i + 1]);
    publish20(ov, rW, lane, WS_OB + ((p * 4 + w) * 20 + 5 * (j >> 1)) * 1024, j);
  }
  waitv<0>();
  setflag(ws, WS_FO + ((p * 4 + w) * 8 + j) * 8, tag, lane);
  STAMP(7);
  {
    const rsrc_t rO = __builtin_amdgcn_make_buffer_rsrc((void*)((const char*)wo - 4096), 0, 640 * 1280 + 4096, 0x00020000);
    const u32 so = w * 160 * 1280 + j * 160;
    sfor<0, 25>([&](auto I) {
      constexpr int i = I;
      if constexpr ((i & 3) == 0) dma<0>(rO, voffO[i], so, L_WO + w * 25600 + i * 1024);
      else dma_c<(i & 3) * 1024>(rO, voffO[i], so);
    });
  }
  const u32 P = p >> 1, c = ((p & 1) << 3) | j, rho = c >> 1, odd = c & 1;
  const u32 t0 = rho < 6 ? 14 * rho + 8 * odd : 84 + 12 * (rho - 6) + 6 * odd;
  const u32 tl = rho < 6 ? 14 * rho + 6 + odd : t0;
  poll(ws, WS_FO + ((p * 4 + w) * 8 + (lane & 7)) * 8, tag);
  STAMP(8);
  u32 hres[10];
  {
    u4 of[20];
    sfor<0, 20>([&](auto S) { ldv<(S & 3) * 1024>(of[S], rW, l16, WS_OB + ((p * 4 + w) * 20 + (S & ~3)) * 1024); });
    u4 fr[2];
    ldv<0>(fr[0], rW, lane * 32, WS_FRT); ldv<16>(fr[1], rW, lane * 32, WS_FRT);
    BAR();
    const u32 a0 = opaque(L_WO + tr0), a1 = opaque(L_WO + tr1), a2 = opaque(L_WO + tr0 + 51200), a3 = opaque(L_WO + tr1 + 51200);
    f4 ha[5];
    u4 wf[5];
#pragma unroll
    for (int n = 0; n < 5; n++) wf[n] = ldstr(a0 + n * 32, a1 + n * 32);
    u32 cbv[5];
    sfor<0, 20>([&](auto S) {
      constexpr int s = S;
      if constexpr (s < 19) waitv<19 - s + 2>();
      if constexpr (s == 19) {
        waitv<0>(); landed(fr[0]); landed(fr[1]);
        const u32 tl32 = (u32)tag, th32 = (u32)(tag >> 32);
        bool ok = fr[0].x == tl32 && fr[0].y == th32 && fr[0].z == tl32 && fr[0].w == th32 && fr[1].x == tl32 && fr[1].y == th32 && fr[1].z == tl32 && fr[1].w == th32;
        if (!__all(ok)) {
#pragma nounroll
          for (int q = 0; q < 4; q++) poll(ws, WS_FRT + (64 * q + lane) * 8, tag);
        }
#pragma unroll
        for (int i = 0; i < 13; i++) {
          u32 id = 4 * i + w; id = id > 48 ? 48 : id;
          u32 ss = id / 7, n = id - ss * 7;
          dma_sc1(rW, l16, WS_W1T + ((n < 6 ? t0 + n : tl) * 20 + ss) * 1024, L_W1A + id * 1024);
        }
      }
      sfor<0, 5>([&](auto N) {
        constexpr int n = N;
        u4 cur = wf[n];
        if constexpr (s == 0) mm0(ha[n], cur, of[s]); else mm(ha[n], cur, of[s]);
        if constexpr (s < 19) {
          constexpr int o = ((s + 1) % 10) * 5120 + n * 32;
          wf[n] = (s + 1 < 10) ? ldstr(a0 + o, a1 + o) : ldstr(a2 + o, a3 + o);
        }
      });
    });
    if (b == 0 && tid == 0) __hip_atomic_store((GAS u64*)(ws + WS_EPOCH), tag, __ATOMIC_RELAXED, __HIP_MEMORY_SCOPE_AGENT);
    {
      u32 ci = lane + 64 * (w & 1); ci = ci > 111 ? 111 : ci;
      u32 n = ci >> 4, col = (n < 6 ? t0 + n : tl) * 16 + (ci & 15);
#pragma unroll
      for (int q = 0; q < 4; q++) cbv[q] = __builtin_amdgcn_raw_buffer_load_b32(rW, col * 4, WS_CS1 + q * 6912, SC1);
      cbv[4] = b1[col];
    }
    MM_DONE();
    STAMP(9);
    float hv[20], sum = 0.f;
#pragma unroll
    for (int n = 0; n < 5; n++) {
      pin(ha[n]);
      hres[2 * n] = pk2(blo(xres[n].x) + rbf(ha[n][0]), bhi(xres[n].x) + rbf(ha[n][1]));
      hres[2 * n + 1] = pk2(blo(xres[n].y) + rbf(ha[n][2]), bhi(xres[n].y) + rbf(ha[n][3]));
      hv[4 * n] = blo(hres[2 * n]); hv[4 * n + 1] = bhi(hres[2 * n]); hv[4 * n + 2] = blo(hres[2 * n + 1]); hv[4 * n + 3] = bhi(hres[2 * n + 1]);
      sum += (hv[4 * n] + hv[4 * n + 1]) + (hv[4 * n + 2] + hv[4 * n + 3]);
    }
    publish20(hres, rW, lane, WS_HB + ((p * 4 + w) * 20 + 5 * (j >> 1)) * 1024, j);
    sum = xsum16_32(sum);
    float mu = sum * (1.f / 80.f), m2 = 0.f;
#pragma unroll
    for (int i = 0; i < 20; i++) { float d = hv[i] - mu; m2 += d * d; }
    m2 = xsum16_32(m2);
    st64(u2{asu(sum), asu(m2)}, rW, g == 0 ? lr * 64 : OOB, WS_ST + (p * 64 + w * 16) * 64 + j * 8);
    {
      u32 ci = lane + 64 * (w & 1); ci = ci > 111 ? 111 : ci;
      sts32(L_CB + ci * 4, (asf(cbv[0]) + asf(cbv[1])) + (asf(cbv[2]) + asf(cbv[3])));
      sts32(L_CB + 512 + ci * 4, blo(cbv[4]));
    }
  }
  waitv<0>();
  setflag(ws, WS_FH + ((p * 4 + w) * 8 + j) * 8, tag, lane);
  STAMP(10);
  BAR();
#pragma unroll
  for (int i = 0; i < 23; i++) {
    u32 id = 4 * i + w; id = id > 90 ? 90 : id;
    u32 s = id / 7, n = id - s * 7;
    dma_sc1(rW, l16, WS_W1T + ((n < 6 ? t0 + n : tl) * 20 + 7 + s) * 1024, L_W1B + id * 1024);
  }
  const u32 s0 = w < 2 ? 14 * w : 13 * w + 2;
  const u32 soW2 = WS_W2T + (j * 54 + s0) * 5120;
  u4 w2f[70];
  auto ldw2 = [&](auto I) {
    constexpr int i = I;
    if constexpr (i < 60) lda<(i & 3) * 1024>(w2f[i], rW, l16, soW2 + (i & ~3) * 1024);
    else ldv<(i & 3) * 1024>(w2f[i], rW, (i >= 65 && w >= 2) ? OOB : l16, soW2 + (i & ~3) * 1024);
  };
  constexpr int W2PRE = 8, W2PER = 3;
  sfor<0, W2PRE>(ldw2);
  const u32 pc = 2 * P + (w >> 1), mt0 = 2 * (w & 1);
  poll(ws, WS_FH + ((pc * 4 + mt0) * 8 + (lane & 15)) * 8, tag);
  STAMP(11);
  {
    u4 hf[2][20], stv[2];
    const u32 soH = WS_HB + (pc * 4 + mt0) * 20480;
    ldv<0>(stv[0], rW, lr * 64 + g * 16, WS_ST + (pc * 64 + mt0 * 16) * 64);
    ldv<1024>(stv[1], rW, lr * 64 + g * 16, WS_ST + (pc * 64 + mt0 * 16) * 64);
    sfor<0, 20>([&](auto S) {
      constexpr int s = S;
      if constexpr (s < 17) { ldv<(s & 3) * 1024>(hf[0][s], rW, l16, soH + (s & ~3) * 1024); ldv<(s & 3) * 1024>(hf[1][s], rW, l16, soH + 20480 + (s & ~3) * 1024); }
      else { lda<(s & 3) * 1024>(hf[0][s], rW, l16, soH + (s & ~3) * 1024); lda<(s & 3) * 1024>(hf[1][s], rW, l16, soH + 20480 + (s & ~3) * 1024); }
    });
    BAR();
    const u32 a0 = opaque(L_W1A + l16), a1 = opaque(L_W1B + l16), a2 = opaque(L_W1B + 49 * 1024 + l16);
    f4 ca[2][7];
    u4 wf[7];
#pragma unroll
    for (int n = 0; n < 7; n++) wf[n] = lds128(a0 + n * 1024);
    sfor<0, 20>([&](auto S) {
      constexpr int s = S;
      constexpr int issued = W2PER * s < 60 - W2PRE - 6 ? W2PER * s : 60 - W2PRE - 6;
      waitv<2 * (19 - s) + issued>();
      sfor<0, 7>([&](auto N) {
        constexpr int n = N;
        u4 cur = wf[n];
        if constexpr (s == 0) { mm0(ca[0][n], cur, hf[0][s]); mm0(ca[1][n], cur, hf[1][s]); }
        else if constexpr (s < 17) { mm(ca[0][n], cur, hf[0][s]); mm(ca[1][n], cur, hf[1][s]); }
        else { mm_va(ca[0][n], cur, hf[0][s]); mm_va(ca[1][n], cur, hf[1][s]); }
        if constexpr (s < 19) {
          constexpr int o = (((s + 1) % 7) * 7 + n) * 1024;
          wf[n] = lds128((s + 1 < 7 ? a0 : s + 1 < 14 ? a1 : a2) + o);
        }
        if constexpr ((n & 1) && n / 2 < W2PER && W2PER * s + n / 2 < 60 - W2PRE - 6) ldw2(IC<W2PRE + W2PER * s + n / 2>{});
      });
    });
    MM_DONE();
    STAMP(12);
    landed(stv[0]); landed(stv[1]);
    float sc[2], sm[2];
#pragma unroll
    for (int m = 0; m < 2; m++) {
      f4 v = __builtin_bit_cast(f4, stv[m]);
      float mean = xsum16_32(v[0] + v[2]) * (1.f / 640.f);
      float d0 = v[0] * (1.f / 80.f) - mean, d1 = v[2] * (1.f / 80.f) - mean;
      float var = xsum16_32((v[1] + v[3]) + 80.f * (d0 * d0 + d1 * d1)) * (1.f / 640.f);
      float m1 = rbf(mean), sd1 = rbf(__builtin_sqrtf(rbf(rbf(var) + 1e-5f))), r1 = __builtin_amdgcn_rcpf(sd1);
      float m2 = rbf((mean - m1) * r1), sd2 = rbf(__builtin_sqrtf(rbf(rbf(var * r1 * r1) + 1e-5f)));
      sc[m] = r1 * __builtin_amdgcn_rcpf(sd2);
      sm[m] = sc[m] * (m1 + m2 * sd1);
    }
    const u32 so = WS_HID + (pc * 4 + mt0) * 54 * 1024;
    u32 hp[2][2][2];
    sfor<0, 7>([&](auto N) {
      constexpr int n = N;
      f4 c4 = __builtin_bit_cast(f4, lds128(L_CB + n * 64 + g * 16)), b4 = __builtin_bit_cast(f4, lds128(L_CB + 512 + n * 64 + g * 16));
#pragma unroll
      for (int m = 0; m < 2; m++) {
        pin(ca[m][n]);
        float t[4];
#pragma unroll
        for (int i = 0; i < 4; i++) {
          float v = ca[m][n][i] * sc[m] + (b4[i] - sm[m] * c4[i]);
          float u = v * (-2.302208198f - 0.1029432397f * v * v);
          t[i] = v * __builtin_amdgcn_rcpf(1.f + __builtin_amdgcn_exp2f(u));
        }
        hp[m][n & 1][0] = pk2(t[0], t[1]); hp[m][n & 1][1] = pk2(t[2], t[3]);
        if constexpr (n & 1) st128(u4{hp[m][0][0], hp[m][0][1], hp[m][1][0], hp[m][1][1]}, rW, l16, so + m * 54 * 1024 + (t0 / 2 + n / 2) * 1024);
        if constexpr (n == 6) st64(u2{hp[m][0][0], hp[m][0][1]}, rW, rho < 6 ? l16 + odd * 8 : OOB, so + m * 54 * 1024 + (tl >> 1) * 1024);
      }
    });
  }
  waitv<0>();
  setflag(ws, WS_FHID + ((P * 4 + w) * 16 + c) * 8, tag, lane);
  STAMP(13);
  {
    sfor<54, 70>(ldw2);
    poll(ws, WS_FHID + ((P * 4 + 2 * (p & 1)) * 16 + (lane & 31)) * 8, tag);
    STAMP(14);
    f4 da[4][5];
    u4 xf[7][4];
    u32 soX2[4];
#pragma unroll
    for (int a = 0; a < 4; a++) soX2[a] = WS_HID + ((p * 4 + ((w + a) & 3)) * 54 + s0) * 1024;
    const u32 vl = w >= 2 ? OOB : l16;
    auto ldx = [&](auto S, auto A) {
      constexpr int s = S, a = A;
      ldv<(s & 3) * 1024>(xf[s % 7][a], rW, s == 13 ? vl : l16, soX2[a] + (s & ~3) * 1024);
    };
    sfor<0, 7>([&](auto S) { sfor<0, 4>([&](auto A) { ldx(S, A); }); });
    sfor<0, 14>([&](auto S) {
      constexpr int s = S;
      sfor<0, 4>([&](auto A) {
        constexpr int a = A;
        waitv<(s < 7 ? 27 : 4 * (13 - s) + 3 - a)>();
        u4 cur = xf[s % 7][a];
        sfor<0, 5>([&](auto N) {
          constexpr int n = N, i = 5 * s + n;
          if constexpr (s == 0) mm0_av(da[a][n], w2f[i], cur);
          else if constexpr (i < 60) mm_av(da[a][n], w2f[i], cur);
          else mm(da[a][n], w2f[i], cur);
        });
        if constexpr (s < 7) ldx(IC<s + 7>{}, A);
      });
    });
    MM_DONE();
    STAMP(15);
    BAR();
#pragma unroll
    for (int a = 1; a < 4; a++)
#pragma unroll
      for (int n = 0; n < 5; n++) { pin(da[a][n]); sts128(L_RED + ((((w + a) & 3) * 3 + (a - 1)) * 5 + n) * 1024 + l16, __builtin_bit_cast(u4, da[a][n])); }
    BAR();
    const rsrc_t rOut = __builtin_amdgcn_make_buffer_rsrc((void*)out, 0, 2048 * 1280, 0x00020000);
#pragma unroll
    for (int n = 0; n < 5; n++) {
      pin(da[0][n]);
      f4 v = da[0][n];
#pragma unroll
      for (int a = 0; a < 3; a++) v += __builtin_bit_cast(f4, lds128(L_RED + ((w * 3 + a) * 5 + n) * 1024 + l16));
      u2 bb = *(const u2*)(b2 + 80 * j + 16 * n + 4 * g);
      u32 o0 = pk2(blo(hres[2 * n]) + rbf(rbf(v[0]) + blo(bb.x)), bhi(hres[2 * n]) + rbf(rbf(v[1]) + bhi(bb.x)));
      u32 o1 = pk2(blo(hres[2 * n + 1]) + rbf(rbf(v[2]) + blo(bb.y)), bhi(hres[2 * n + 1]) + rbf(rbf(v[3]) + bhi(bb.y)));
      __builtin_amdgcn_raw_buffer_store_b64(u2{o0, o1}, rOut, lr * 1280 + g * 8 + n * 32, (p * 64 + w * 16) * 1280 + j * 160, 0);
    }
  }
}

extern "C" void kernel_launch(void* const* d_in, const int* in_sizes, int n_in, void* d_out, int out_size, void* d_ws, size_t ws_size,
                              hipStream_t stream) {
  fused_block<<<256, 256, 0, stream>>>((const u16*)d_in[0], (const u16*)d_in[1], (const u16*)d_in[2], (const u16*)d_in[4], (const u16*)d_in[5],
                                       (const u16*)d_in[6], (const u16*)d_in[7], (u16*)d_out, (char*)d_ws);
}
```

```cpp
#include <hip/hip_runtime.h>
#include <stdint.h>
#pragma clang diagnostic ignored "-Wint-to-pointer-cast"

typedef unsigned short u16;
typedef unsigned u32;
typedef unsigned long long u64;
typedef float f2 __attribute__((ext_vector_type(2)));
typedef float f4 __attribute__((ext_vector_type(4)));
typedef unsigned u2 __attribute__((ext_vector_type(2)));
typedef unsigned u4 __attribute__((ext_vector_type(4)));
typedef short s4 __attribute__((ext_vector_type(4)));
typedef __bf16 bf2 __attribute__((ext_vector_type(2)));
typedef __bf16 bf8 __attribute__((ext_vector_type(8)));
typedef __amdgpu_buffer_rsrc_t rsrc_t;
#define LAS __attribute__((address_space(3)))
#define GAS __attribute__((address_space(1)))
#define DI __device__ __forceinline__

template <int I> struct IC { static constexpr int v = I; constexpr operator int() const { return I; } };
template <int B, int E, class F> DI void sfor(F&& f) {
  if constexpr (B < E) { f(IC<B>{}); sfor<B + 1, E>(f); }
}

constexpr u32 WS_EPOCH = 0, WS_FRT = 4096, WS_FKV = 8192, WS_FO = 16384, WS_FH = 24576, WS_FHID = 32768;
constexpr u32 WS_KVP = 65536;
constexpr u32 WS_OB = 2u << 20;
constexpr u32 WS_HB = WS_OB + 32 * 81920;
constexpr u32 WS_ST = WS_HB + 32 * 81920;
constexpr u32 WS_CS1 = WS_ST + 2048 * 64;
constexpr u32 WS_W1T = WS_CS1 + 32768;
constexpr u32 WS_W2T = WS_W1T + 108 * 20480;
constexpr u32 WS_HID = WS_W2T + 8 * 54 * 5120;
constexpr u32 WS_END = WS_HID + 32 * 4 * 54 * 1024;
constexpr u32 WS_PROF = WS_END;
constexpr u32 OOB = 0x7FFFFF00u;
constexpr int SC1 = 16;

constexpr u32 L_X = 0, L_RING = 81920;
constexpr u32 L_WO = 0, L_IMG = 102400, L_W1A = 110592, L_CB = 160768, L_W1B = 0, L_RED = 0;

DI f4 mfma(u4 a, u4 b, f4 c) {
  return __builtin_amdgcn_mfma_f32_16x16x32_bf16(__builtin_bit_cast(bf8, a), __builtin_bit_cast(bf8, b), c, 0, 0, 0);
}
DI void mm(f4& c, u4 a, u4 b) { asm volatile("v_mfma_f32_16x16x32_bf16 %0, %1, %2, %0" : "+v"(c) : "v"(a), "v"(b) : "memory"); }
DI void mm0(f4& c, u4 a, u4 b) { asm volatile("v_mfma_f32_16x16x32_bf16 %0, %1, %2, 0" : "=&v"(c) : "v"(a), "v"(b) : "memory"); }
#define MM_DONE() asm volatile("s_nop 7\n\ts_nop 1" ::: "memory")
DI void pin(f4& c) { asm volatile("" : "+v"(c)); }
DI u32 opaque(u32 v) { asm volatile("" : "+v"(v)); return v; }

DI u32 pk2(float a, float b) { f2 v = {a, b}; return __builtin_bit_cast(u32, __builtin_convertvector(v, bf2)); }
DI float rbf(float a) { return __builtin_bit_cast(float, pk2(a, a) << 16); }
DI float blo(u32 v) { return __builtin_bit_cast(float, v << 16); }
DI float bhi(u32 v) { return __builtin_bit_cast(float, v & 0xFFFF0000u); }
DI float asf(u32 v) { return __builtin_bit_cast(float, v); }
DI u32 asu(float v) { return __builtin_bit_cast(u32, v); }

template <int OFF> DI void dma(rsrc_t r, u32 voff, u32 soff, u32 lds) {
  asm volatile("s_mov_b32 m0, %0\n\ts_nop 0\n\tbuffer_load_dwordx4 %1, %2, %3 offen offset:%4 lds" ::"s"(lds), "v"(voff), "s"(r), "s"(soff), "n"(OFF) : "memory");
}
template <int OFF> DI void dma_c(rsrc_t r, u32 voff, u32 soff) {
  asm volatile("buffer_load_dwordx4 %0, %1, %2 offen offset:%3 lds" ::"v"(voff), "s"(r), "s"(soff), "n"(OFF) : "memory");
}
DI void dma_sc1(rsrc_t r, u32 voff, u32 soff, u32 lds) {
  asm volatile("s_mov_b32 m0, %0\n\ts_nop 0\n\tbuffer_load_dwordx4 %1, %2, %3 offen sc1 lds" ::"s"(lds), "v"(voff), "s"(r), "s"(soff) : "memory");
}
template <int OFF> DI void ldv(u4& d, rsrc_t r, u32 voff, u32 soff) {
  asm volatile("buffer_load_dwordx4 %0, %1, %2, %3 offen offset:%4 sc1" : "=v"(d) : "v"(voff), "s"(r), "s"(soff), "n"(OFF) : "memory");
}
DI void ldv_plain(u4& d, rsrc_t r, u32 voff, u32 soff) {
  asm volatile("buffer_load_dwordx4 %0, %1, %2, %3 offen" : "=v"(d) : "v"(voff), "s"(r), "s"(soff) : "memory");
}
template <int N> DI void waitv() { asm volatile("s_waitcnt vmcnt(%0)" ::"n"(N) : "memory"); }
DI void landed(u4& d) { asm volatile("" : "+v"(d)); }
#define BAR() do { asm volatile("" ::: "memory"); __builtin_amdgcn_s_waitcnt(0xC07F); asm volatile("s_barrier" ::: "memory"); } while (0)
DI u4 lds128(u32 a) { return *(LAS u4*)(a); }
DI u2 lds64(u32 a) { return *(LAS u2*)(a); }
DI float lds32(u32 a) { return *(LAS float*)(a); }
DI void sts128(u32 a, u4 v) { *(LAS u4*)(a) = v; }
DI void sts64(u32 a, u2 v) { *(LAS u2*)(a) = v; }
DI void sts32(u32 a, float v) { *(LAS float*)(a) = v; }
DI u4 ldstr(u32 a0, u32 a1) {
  u2 x = __builtin_bit_cast(u2, __builtin_amdgcn_ds_read_tr16_b64_v4i16((LAS s4*)(a0)));
  u2 y = __builtin_bit_cast(u2, __builtin_amdgcn_ds_read_tr16_b64_v4i16((LAS s4*)(a1)));
  return u4{x.x, x.y, y.x, y.y};
}
DI float xsum16_32(float v) {
  auto r = __builtin_amdgcn_permlane16_swap(asu(v), asu(v), false, false);
  v = asf((u32)r[0]) + asf((u32)r[1]);
  auto q = __builtin_amdgcn_permlane32_swap(asu(v), asu(v), false, false);
  return asf((u32)q[0]) + asf((u32)q[1]);
}
DI void poll(const char* ws, u32 off, u64 tag) {
  const GAS u64* f = (const GAS u64*)(ws + off);
#pragma nounroll
  for (u32 it = 0; it < (1u << 18); ++it) {
    u64 v = __hip_atomic_load(f, __ATOMIC_RELAXED, __HIP_MEMORY_SCOPE_AGENT);
    if (__all(v == tag)) break;
    __builtin_amdgcn_s_sleep(1);
  }
  __builtin_amdgcn_fence(__ATOMIC_ACQUIRE, "wavefront");
  asm volatile("" ::: "memory");
}
DI void setflag(char* ws, u32 off, u64 tag, u32 lane) {
  if (lane == 0) __hip_atomic_store((GAS u64*)(ws + off), tag, __ATOMIC_RELAXED, __HIP_MEMORY_SCOPE_AGENT);
  asm volatile("" ::: "memory");
}
DI float phi(float t) {
  return fmaxf(t, 0.f) + (__builtin_amdgcn_exp2f(fminf(t, 0.f) * 1.4426950408889634f) + 1e-6f);
}
DI float sel4(f4 v, u32 i) { return i == 0 ? v[0] : i == 1 ? v[1] : i == 2 ? v[2] : v[3]; }
DI void st128(u4 v, rsrc_t r, u32 voff, u32 soff) {
  __builtin_amdgcn_raw_buffer_store_b128(v, r, voff, soff, SC1);
  asm volatile("s_nop 1" ::"v"(v) : "memory");
}
DI void st64(u2 v, rsrc_t r, u32 voff, u32 soff) { __builtin_amdgcn_raw_buffer_store_b64(v, r, voff, soff, SC1); }
DI void publish20(const u32* v, rsrc_t r, u32 lane, u32 so, u32 j) {
  if ((j & 1) == 0) {
    st128(u4{v[0], v[1], v[2], v[3]}, r, lane * 16, so);
    st128(u4{v[4], v[5], v[6], v[7]}, r, lane * 16, so + 1024);
    st64(u2{v[8], v[9]}, r, lane * 16, so + 2048);
  } else {
    st64(u2{v[0], v[1]}, r, lane * 16 + 8, so + 2048);
    st128(u4{v[2], v[3], v[4], v[5]}, r, lane * 16, so + 3072);
    st128(u4{v[6], v[7], v[8], v[9]}, r, lane * 16, so + 4096);
  }
}

#define STAMP(i)

__global__ __launch_bounds__(512, 2) void fused_block(const u16* __restrict__ x, const u16* __restrict__ wqkv, const u16* __restrict__ wo,
                                                      const u16* __restrict__ w1, const u16* __restrict__ b1, const u16* __restrict__ w2,
                                                      const u16* __restrict__ b2, u16* __restrict__ out, char* __restrict__ ws) {
  __shared__ __attribute__((aligned(1024))) char smem[163840];
  asm volatile("" ::"v"((u32)(uintptr_t)(LAS char*)smem));
  const u32 tid = threadIdx.x, lane = tid & 63, lr = lane & 15, g = lane >> 4;
  const u32 w = __builtin_amdgcn_readfirstlane(tid >> 6), mt = w & 3, nh = w >> 2;
  const bool helper = nh != 0;
  const u32 b = blockIdx.x, xg = b & 7, rr = b >> 3, p = 4 * xg + (rr >> 3), j = rr & 7;
  STAMP(0);
  const rsrc_t rX = __builtin_amdgcn_make_buffer_rsrc((void*)x, 0, 2048 * 1280, 0x00020000);
  const rsrc_t rQ = __builtin_amdgcn_make_buffer_rsrc((void*)wqkv, 0, 640 * 3840, 0x00020000);
  const rsrc_t rW = __builtin_amdgcn_make_buffer_rsrc((void*)ws, 0, WS_END, 0x00020000);
  const u32 l16 = lane * 16;
  u4 ONES = {0x3F803F80u, 0x3F803F80u, 0x3F803F80u, 0x3F803F80u};
  asm volatile("" : "+v"(ONES));

  u2 ep;
  asm volatile("buffer_load_dwordx2 %0, off, %1, 0 sc1" : "=v"(ep) : "s"(rW) : "memory");
  const u32 rot = (xg + 3 * (p & 3)) % 10, rotq = (xg + (p & 3)) % 5;
  const u32 xa0 = L_X + (lr >> 3) * 1024 + (lr & 7) * 128 + ((g ^ (lr & 7)) << 4);
  const u32 tr0 = (8 * g + (lr >> 2) + 4 * (g & 1)) * 160 + (lr & 3) * 8;
  const u32 tr1 = (8 * g + (lr >> 2) + 4 * ((g & 1) ^ 1)) * 160 + (lr & 3) * 8;
  const u32 P = p >> 1, c = ((p & 1) << 3) | j, rho = c >> 1, odd = c & 1;
  const u32 t0 = rho < 6 ? 14 * rho + 8 * odd : 84 + 12 * (rho - 6) + 6 * odd;
  const u32 tl = rho < 6 ? 14 * rho + 6 + odd : t0;
  const u32 s0 = w < 6 ? 7 * w : 6 * w + 6;
  const u32 soW2 = WS_W2T + (j * 54 + s0) * 5120;
  const u32 vlast = w >= 6 ? OOB : l16;
  u4 w2f[35];
  auto ldw2 = [&](auto I) {
    constexpr int i = I;
    ldv<(i & 3) * 1024>(w2f[i], rW, i >= 30 ? vlast : l16, soW2 + (i & ~3) * 1024);
  };
  u32 hres[10];
  u64 tag;
#define GET_TAG() do { u32 lo_, hi_; asm volatile("v_readfirstlane_b32 %0, %2\n\tv_readfirstlane_b32 %1, %3\n\ts_nop 3" : "=s"(lo_), "=s"(hi_) : "v"(ep.x), "v"(ep.y) : "memory"); \
    tag = (((u64)hi_ << 32) | lo_) + 1; } while (0)
  if (!helper) {
    const u32 mh = mt & 1, nq = mt >> 1;
    f4 acc[2][5], cs[3], rsum = {0, 0, 0, 0}, gram = {0, 0, 0, 0};
#pragma unroll
    for (int i = 0; i < 5; i++) { acc[0][i] = f4{0, 0, 0, 0}; acc[1][i] = f4{0, 0, 0, 0}; pin(acc[0][i]); pin(acc[1][i]); }
#pragma unroll
    for (int i = 0; i < 3; i++) { cs[i] = f4{0, 0, 0, 0}; pin(cs[i]); }
    pin(rsum); pin(gram);
    {
      const u32 oP = L_RING + nq * 96 + mh * 5120, oR = L_RING + nq * 96 + (mh ^ 1) * 5120, oC = L_RING + 64 + nq * 5120;
      const u32 bP0 = tr0 + oP, bP1 = tr1 + oP, bR0 = tr0 + oR, bR1 = tr1 + oR, bC0 = tr0 + oC, bC1 = tr1 + oC;
      const u32 xs0 = xa0 + mh * 4096 + nq * 2048;
      auto half = [&](u4* cur, u4* nxt, u32 xa, u32 so, auto KS) {
        constexpr int o = KS * 10240;
        const u32 p0 = bP0 + so, p1 = bP1 + so, r0 = bR0 + so, r1 = bR1 + so, c0 = bC0 + so, c1 = bC1 + so;
        nxt[5] = lds128(xa); nxt[0] = ldstr(p0 + o, p1 + o);
        mm(rsum, cur[5], ONES); mm(gram, cur[5], cur[5]);
        nxt[6] = lds128(xa ^ 2048);
        mm(acc[0][0], cur[5], cur[0]); mm(acc[1][0], cur[6], cur[0]);
        nxt[1] = ldstr(p0 + o + 32, p1 + o + 32);
        mm(cs[0], ONES, cur[0]); mm(acc[0][1], cur[5], cur[1]);
        nxt[2] = ldstr(c0 + o, c1 + o);
        mm(acc[1][1], cur[6], cur[1]); mm(cs[1], ONES, cur[1]);
        nxt[3] = ldstr(r0 + o, r1 + o);
        mm(acc[0][2], cur[5], cur[2]); mm(acc[1][2], cur[6], cur[2]);
        nxt[4] = ldstr(r0 + o + 32, r1 + o + 32);
        mm(cs[2], ONES, cur[2]);
        mm(acc[0][3], cur[5], cur[3]); mm(acc[1][3], cur[6], cur[3]);
        mm(acc[0][4], cur[5], cur[4]); mm(acc[1][4], cur[6], cur[4]);
      };
      u4 fa[7], fb[7];
      u32 st = rot;
      waitv<0>();
      GET_TAG();
      BAR();
      STAMP(1);
      {
        const u32 xa = xs0 + st * 8192;
        fa[5] = lds128(xa); fa[6] = lds128(xa ^ 2048); fa[0] = ldstr(bP0, bP1); fa[1] = ldstr(bP0 + 32, bP1 + 32); fa[2] = ldstr(bC0, bC1);
        fa[3] = ldstr(bR0, bR1); fa[4] = ldstr(bR0 + 32, bR1 + 32);
      }
#pragma nounroll
      for (u32 i = 0; i < 10; i++) {
        half(fa, fb, (xs0 ^ 64) + st * 8192, (i & 3) * 20480, IC<1>{});
        BAR();
        st = st + 1 >= 10 ? 0 : st + 1;
        half(fb, fa, xs0 + st * 8192, ((i + 1) & 3) * 20480, IC<0>{});
      }
      MM_DONE();
    }
    STAMP(2);
    float qmb, qrs;
    {
      const u32 LS = L_RING, LC = L_RING + 512, EX = L_RING + 20480;
      pin(rsum); pin(gram);
      {
        const float rs_ = (lr & 2) ? ((lr & 1) ? rsum[3] : rsum[2]) : ((lr & 1) ? rsum[1] : rsum[0]);
        const float gr_ = (lr & 2) ? ((lr & 1) ? gram[3] : gram[2]) : ((lr & 1) ? gram[1] : gram[0]);
        float m = rs_ * (1.f / 640.f);
        float v = fmaxf(gr_ * (1.f / 640.f) - m * m, 0.f);
        float sd = rbf(__builtin_amdgcn_sqrtf(rbf(rbf(v) + 1e-5f)));
        float rs = __builtin_amdgcn_rcpf(sd);
        if ((lr >> 2) == g) sts64(LS + ((2 * mh + nq) * 16 + lr) * 8, u2{asu(-rbf(m) * rs), asu(rs)});
      }
      const u32 iP = 3 * nq + 5 * mh, iR = 3 * nq + 5 * (mh ^ 1), iC = 2 + 5 * nq;
#pragma unroll
      for (int i = 0; i < 3; i++) pin(cs[i]);
      if (g == 0) { sts32(LC + iP * 64 + lr * 4, cs[0][0]); sts32(LC + iP * 64 + 64 + lr * 4, cs[1][0]); }
      BAR();
      f4 st[2][2];
#pragma unroll
      for (int m = 0; m < 2; m++) {
        u32 a = LS + ((2 * mh + (nq ^ m)) * 16 + 4 * g) * 8;
        st[m][0] = __builtin_bit_cast(f4, lds128(a)); st[m][1] = __builtin_bit_cast(f4, lds128(a + 16));
      }
      float cR0 = lds32(LC + iR * 64 + lr * 4), cR1 = lds32(LC + iR * 64 + 64 + lr * 4);
      { u2 q = lds64(LS + (16 * mt + lr) * 8); qmb = asf(q.x); qrs = asf(q.y); }
      sfor<0, 5>([&](auto N) {
        constexpr int n = N;
        pin(acc[0][n]); pin(acc[1][n]);
        float c = n < 3 ? cs[n < 3 ? n : 0][0] : (n == 3 ? cR0 : cR1);
        float t[8];
#pragma unroll
        for (int m = 0; m < 2; m++)
#pragma unroll
          for (int i = 0; i < 4; i++) t[4 * m + i] = acc[m][n][i] * st[m][i >> 1][2 * (i & 1) + 1] + st[m][i >> 1][2 * (i & 1)] * c;
        bool isk = n < 2 ? mh == 0 : (n == 2 ? nq == 0 : mh == 1);
        if (isk) {
#pragma unroll
          for (int i = 0; i < 8; i++) t[i] = phi(t[i]);
        }
        u32 idx = n < 2 ? iP + n : (n == 2 ? iC : iR + n - 3);
        u32 a = EX + mh * 10240 + idx * 1024 + l16;
        sts64(a + nq * 8, u2{pk2(t[0], t[1]), pk2(t[2], t[3])});
        sts64(a + (nq ^ 1) * 8, u2{pk2(t[4], t[5]), pk2(t[6], t[7])});
      });
      BAR();
      const u32 ex = EX + mt * 1024 + l16;
      u4 kf[2][2], vf[2][2];
#pragma unroll
      for (int m = 0; m < 2; m++) {
        kf[m][0] = lds128(ex + m * 10240); kf[m][1] = lds128(ex + m * 10240 + 1024);
        vf[m][0] = lds128(ex + m * 10240 + 5120); vf[m][1] = lds128(ex + m * 10240 + 6144);
      }
      BAR();
      const u32 so = WS_KVP + ((j * 32 + p) * 4 + mt) * 896;
      const u32 e0 = lr - 4 * mt, e1 = e0 + 16;
#pragma unroll
      for (int a = 0; a < 2; a++) {
        f4 z4 = {0, 0, 0, 0};
        f4 kv0 = mfma(kf[1][a], vf[1][0], mfma(kf[0][a], vf[0][0], z4)), kv1 = mfma(kf[1][a], vf[1][1], mfma(kf[0][a], vf[0][1], z4));
        f4 zz = mfma(kf[1][a], ONES, mfma(kf[0][a], ONES, z4));
        const u32 dq = 4 * a + g - mt;
        st64(u2{pk2(kv0[0], kv0[1]), pk2(kv0[2], kv0[3])}, rW, (dq < 5 && e0 < 20) ? e0 * 40 + dq * 8 : OOB, so);
        st64(u2{pk2(kv1[0], kv1[1]), pk2(kv1[2], kv1[3])}, rW, (dq < 5 && e1 < 20) ? e1 * 40 + dq * 8 : OOB, so);
        st64(u2{pk2(zz[0], zz[1]), pk2(zz[2], zz[3])}, rW, (dq < 5 && lr == 0) ? 800 + dq * 8 : OOB, so);
      }
    }
    waitv<0>();
    setflag(ws, WS_FKV + ((j * 4 + mt) * 32 + p) * 8, tag, lane);
    STAMP(3);
    f4 qa[5], qc[5];
    auto q_stage = [&](auto FIRST, u32 qi, u32 slot) {
      u32 qs = qi + rotq; qs = qs >= 5 ? qs - 5 : qs;
      const u32 xb = xa0 + mt * 2048 + qs * 16384, xc = xb ^ 64;
      const u32 a0 = L_RING + slot * 20480 + tr0, a1 = L_RING + slot * 20480 + tr1;
      u4 xf[4], wf[5];
      xf[0] = lds128(xb);
#pragma unroll
      for (int n = 0; n < 5; n++) wf[n] = ldstr(a0 + n * 32, a1 + n * 32);
      xf[1] = lds128(xc); xf[2] = lds128(xb + 8192); xf[3] = lds128(xc + 8192);
      sfor<0, 4>([&](auto KS) {
        constexpr int ks = KS;
        sfor<0, 5>([&](auto N) {
          constexpr int n = N;
          u4 cur = wf[n];
          if constexpr (FIRST && ks == 0) { mm0(qa[n], cur, xf[ks]); mm0(qc[n], cur, ONES); }
          else { mm(qa[n], cur, xf[ks]); mm(qc[n], cur, ONES); }
          if constexpr (ks < 3) wf[n] = ldstr(a0 + (ks + 1) * 5120 + n * 32, a1 + (ks + 1) * 5120 + n * 32);
        });
      });
    };
    q_stage(IC<1>{}, 0, 2);
    BAR();
#pragma nounroll
    for (u32 qi = 1; qi < 5; qi++) {
      q_stage(IC<0>{}, qi, (qi + 2) & 3);
      if (qi == 1 || qi == 3) BAR();
    }
    MM_DONE();
    STAMP(4);
    u2 xres[5];
#pragma unroll
    for (int n = 0; n < 5; n++) {
      u32 ch = 10 * j + 2 * n + (g >> 1);
      xres[n] = lds64(L_X + (ch >> 3) * 8192 + (2 * mt + (lr >> 3)) * 1024 + (lr & 7) * 128 + (((ch & 7) ^ (lr & 7)) << 4) + (g & 1) * 8);
    }
    u32 pq[5][2];
#pragma unroll
    for (int n = 0; n < 5; n++) {
      pin(qa[n]); pin(qc[n]);
      float t[4];
#pragma unroll
      for (int i = 0; i < 4; i++) t[i] = phi(qa[n][i] * qrs + qmb * qc[n][i]);
      pq[n][0] = opaque(pk2(t[0], t[1])); pq[n][1] = opaque(pk2(t[2], t[3]));
    }
    STAMP(5);
    BAR();
    STAMP(6);
    {
      float o[20];
#pragma unroll
      for (int h = 0; h < 4; h++) {
        u4 bq = {pq[h][0], pq[h][1], pq[h + 1][0], pq[h + 1][1]};
        f4 z4 = {0, 0, 0, 0};
        f4 d0 = mfma(lds128(L_IMG + h * 2048 + l16), bq, z4);
        f4 d1 = mfma(lds128(L_IMG + h * 2048 + 1024 + l16), bq, z4);
        float rd = __builtin_amdgcn_rcpf(rbf(rbf(d1[1]) + 1e-6f));
#pragma unroll
        for (int i = 0; i < 4; i++) o[5 * h + i] = rbf(d0[i]) * rd;
        o[5 * h + 4] = rbf(d1[0]) * rd;
      }
      u32 ov[10];
#pragma unroll
      for (int i = 0; i < 10; i++) ov[i] = pk2(o[2 * i], o[2 * i + 1]);
      publish20(ov, rW, lane, WS_OB + ((p * 4 + mt) * 20 + 5 * (j >> 1)) * 1024, j);
    }
    waitv<0>();
    setflag(ws, WS_FO + ((p * 4 + mt) * 8 + j) * 8, tag, lane);
    STAMP(7);
    poll(ws, WS_FO + ((p * 4 + mt) * 8 + (lane & 7)) * 8, tag);
    STAMP(8);
    u4 of[20];
    auto ldo = [&](auto S) { ldv<(S & 3) * 1024>(of[S], rW, l16, WS_OB + ((p * 4 + mt) * 20 + (S & ~3)) * 1024); };
    sfor<0, 8>(ldo);
    BAR();
    const u32 a0 = opaque(L_WO + tr0), a1 = opaque(L_WO + tr1), a2 = opaque(L_WO + tr0 + 51200), a3 = opaque(L_WO + tr1 + 51200);
    f4 ha[5];
    u4 wf[5];
#pragma unroll
    for (int n = 0; n < 5; n++) wf[n] = ldstr(a0 + n * 32, a1 + n * 32);
    sfor<0, 20>([&](auto S) {
      constexpr int s = S;
      waitv<(s < 12 ? 7 : 19 - s)>();
      if constexpr (s < 12) ldo(IC<s + 8>{});
      sfor<0, 5>([&](auto N) {
        constexpr int n = N;
        u4 cur = wf[n];
        if constexpr (s == 0) mm0(ha[n], cur, of[s]); else mm(ha[n], cur, of[s]);
        if constexpr (s < 19) {
          constexpr int o = ((s + 1) % 10) * 5120 + n * 32;
          wf[n] = (s + 1 < 10) ? ldstr(a0 + o, a1 + o) : ldstr(a2 + o, a3 + o);
        }
      });
    });
    MM_DONE();
    STAMP(9);
    float hv[20], sum = 0.f;
#pragma unroll
    for (int n = 0; n < 5; n++) {
      pin(ha[n]);
      hres[2 * n] = pk2(blo(xres[n].x) + rbf(ha[n][0]), bhi(xres[n].x) + rbf(ha[n][1]));
      hres[2 * n + 1] = pk2(blo(xres[n].y) + rbf(ha[n][2]), bhi(xres[n].y) + rbf(ha[n][3]));
      hv[4 * n] = blo(hres[2 * n]); hv[4 * n + 1] = bhi(hres[2 * n]); hv[4 * n + 2] = blo(hres[2 * n + 1]); hv[4 * n + 3] = bhi(hres[2 * n + 1]);
      sum += (hv[4 * n] + hv[4 * n + 1]) + (hv[4 * n + 2] + hv[4 * n + 3]);
    }
    publish20(hres, rW, lane, WS_HB + ((p * 4 + mt) * 20 + 5 * (j >> 1)) * 1024, j);
    sum = xsum16_32(sum);
    float mu = sum * (1.f / 80.f), m2 = 0.f;
#pragma unroll
    for (int i = 0; i < 20; i++) { float d = hv[i] - mu; m2 += d * d; }
    m2 = xsum16_32(m2);
    st64(u2{asu(sum), asu(m2)}, rW, g == 0 ? lr * 64 : OOB, WS_ST + (p * 64 + mt * 16) * 64 + j * 8);
    waitv<0>();
    setflag(ws, WS_FH + ((p * 4 + mt) * 8 + j) * 8, tag, lane);
    STAMP(10);
    BAR();
  } else {
    u32 voffW[5];
#pragma unroll
    for (int i = 0; i < 5; i++) {
      u32 f = 64 * i + lane, sg = (f * 6554) >> 16, ch = f - sg * 10;
      voffW[i] = (sg ^ (((sg >> 3) & 1) << 2)) * 3840 + ch * 16 - (i & 3) * 1024;
    }
    const u32 voffX = (lane >> 3) * 1280 + ((lane & 7) ^ (lane >> 3)) * 16;
    const u32 voffX1 = voffX + 8 * 1280 - 1024;
    const u32 soX = (p * 64 + 16 * mt) * 1280, soW = (mt >> 1) * 32 * 3840 + ((mt & 1) + 1) * 1280 + j * 160;
    auto issueW = [&](u32 so, u32 ld) {
      dma<0>(rQ, voffW[0], so, ld); dma_c<1024>(rQ, voffW[1], so); dma_c<2048>(rQ, voffW[2], so); dma_c<3072>(rQ, voffW[3], so);
      dma<0>(rQ, voffW[4], so, ld + 4096);
    };
    auto issueKV = [&](u32 i) {
      u32 st = i + rot; st = st >= 10 ? st - 10 : st;
      dma<0>(rX, voffX, soX + st * 128, L_X + st * 8192 + mt * 2048); dma_c<1024>(rX, voffX1, soX + st * 128);
      issueW(soW + st * 64 * 3840, L_RING + (i & 3) * 20480 + mt * 5120);
    };
    auto issueQ = [&](u32 qi, u32 slot) {
      u32 qs = qi + rotq; qs = qs >= 5 ? qs - 5 : qs;
      issueW((qs * 128 + mt * 32) * 3840 + j * 160, L_RING + slot * 20480 + mt * 5120);
    };
    issueKV(0); issueKV(1);
    waitv<7>(); BAR();
    issueKV(2); issueKV(3);
#pragma nounroll
    for (u32 i = 0; i < 6; i++) { waitv<14>(); BAR(); issueKV(i + 4); }
    waitv<14>(); BAR(); issueQ(0, 2);
    waitv<12>(); BAR(); issueQ(1, 3);
    waitv<10>(); BAR();
    u4 rt[5];
    const bool rt_w1 = b < 108;
    const u32 ru = rt_w1 ? b : b - 108, rua = ru / 27, rub = ru - rua * 27;
    {
      const rsrc_t rS = __builtin_amdgcn_make_buffer_rsrc((void*)(rt_w1 ? w1 : w2), 0, b < 216 ? 640 * 3456 : 0, 0x00020000);
      const u32 pitch = rt_w1 ? 3456 : 1280;
      const u32 vo = (8 * mt + (lane >> 3)) * pitch + (lane & 7) * 16;
#pragma unroll
      for (int q = 0; q < 5; q++) {
        u32 B = 5 * ru + q, cg = B / 54;
        ldv_plain(rt[q], rS, vo, rt_w1 ? (rua * 160 + q * 32) * 3456 + rub * 128 : (B - cg * 54) * 32 * 1280 + cg * 128);
      }
    }
    BAR();
    GET_TAG();
    u32 voffO[25];
#pragma unroll
    for (int i = 0; i < 25; i++) {
      u32 f = 64 * i + lane, sg = (f * 6554) >> 16, ch = f - sg * 10;
      u32 om = sg & 31, ka = om ^ (((om >> 3) & 1) << 2), i40 = 8 * (sg >> 5) + (ka & 7);
      u32 hf = i40 >= 20 ? 1u : 0u, id = i40 - 20 * hf, hh = (id * 13) >> 6;
      voffO[i] = opaque((80 * hf + 15 * hh + 5 * (ka >> 3) + id) * 1280 + ch * 16 + 4096 - (i & 3) * 1024);
    }
    BAR();
    BAR();
    waitv<5>();
    BAR();
    issueQ(2, 0); issueQ(3, 1);
    BAR();
    issueQ(4, 2);
    waitv<5>();
    BAR();
    {
      u32 row = 8 * mt + (lane >> 3);
#pragma unroll
      for (int q = 0; q < 5; q++) { landed(rt[q]); sts128(L_RING + 61440 + q * 4096 + row * 128 + (((lane & 7) ^ (((row >> 1) & 3) << 1)) << 4), rt[q]); }
    }
    waitv<0>();
    BAR();
    {
      sts128(L_IMG + mt * 2048 + l16, u4{0, 0, 0, 0}); sts128(L_IMG + mt * 2048 + 1024 + l16, u4{0, 0, 0, 0});
      u32 r0 = 4 * g + (lr >> 2);
      u32 ta = L_RING + 61440 + r0 * 128 + (((2 * mt + ((lr >> 1) & 1)) ^ (((r0 >> 1) & 3) << 1)) << 4) + (lr & 1) * 8;
      f4 csum = {0, 0, 0, 0};
      const u32 vo = b < 216 ? l16 : OOB;
#pragma unroll
      for (int q = 0; q < 5; q++) {
        u4 f = ldstr(ta + q * 4096, ta + q * 4096 + 2048);
        csum = mfma(f, ONES, csum);
        u32 B = 5 * ru + q, cg = B / 54, tau = 4 * cg + mt, jj = tau / 5;
        st128(f, rW, vo, rt_w1 ? WS_W1T + ((4 * rub + mt) * 20 + rua * 5 + q) * 1024 : WS_W2T + ((jj * 54 + (B - cg * 54)) * 5 + (tau - jj * 5)) * 1024);
      }
      st128(__builtin_bit_cast(u4, csum), rW, (rt_w1 && lr == 0) ? g * 16 : OOB, WS_CS1 + rua * 6912 + (4 * rub + mt) * 64);
    }
    STAMP(4);
    poll(ws, WS_FKV + ((j * 4 + mt) * 32 + (lane & 31)) * 8, tag);
    STAMP(5);
    {
      const u32 so = WS_KVP + (j * 32 * 4 + mt) * 896;
      const u32 v0 = lane < 53 ? l16 : OOB;
      u4 a[32];
      sfor<0, 32>([&](auto Q) { ldv<0>(a[Q], rW, v0, so + Q * 3584); });
      float s[8] = {0, 0, 0, 0, 0, 0, 0, 0};
      sfor<0, 32>([&](auto Q) {
        waitv<31 - Q>(); landed(a[Q]);
#pragma unroll
        for (int i = 0; i < 4; i++) { s[2 * i] += blo(a[Q][i]); s[2 * i + 1] += bhi(a[Q][i]); }
      });
#pragma unroll
      for (int h = 0; h < 2; h++) {
        u32 qd = 2 * lane + h, e = (qd * 205) >> 10, dq = qd - e * 5;
        u32 gs = dq + mt, grp = (e * 13) >> 6, ii = e - grp * 5;
        u2 v = {pk2(s[4 * h], s[4 * h + 1]), pk2(s[4 * h + 2], s[4 * h + 3])};
        u32 base = L_IMG + mt * 2048 + (gs & 3) * 256 + (gs >> 2) * 8;
        if (e < 20) sts64(base + (ii < 4 ? (4 * grp + ii) * 16 : 1024 + 4 * grp * 16), v);
        if (e == 20) {
#pragma unroll
          for (int q = 0; q < 4; q++) sts64(base + 1024 + (4 * q + 1) * 16, v);
        }
      }
    }
    STAMP(6);
    BAR();
    if (mt == 0) setflag(ws, WS_FRT + b * 8, tag, lane);
    {
      const rsrc_t rO = __builtin_amdgcn_make_buffer_rsrc((void*)((const char*)wo - 4096), 0, 640 * 1280 + 4096, 0x00020000);
      const u32 so = mt * 160 * 1280 + j * 160;
      sfor<0, 25>([&](auto I) {
        constexpr int i = I;
        if constexpr ((i & 3) == 0) dma<0>(rO, voffO[i], so, L_WO + mt * 25600 + i * 1024);
        else dma_c<(i & 3) * 1024>(rO, voffO[i], so);
      });
    }
    waitv<0>();
    STAMP(7);
    BAR();
#pragma nounroll
    for (int q = 0; q < 4; q++) poll(ws, WS_FRT + (64 * q + lane) * 8, tag);
    if (b == 0 && tid == 256) __hip_atomic_store((GAS u64*)(ws + WS_EPOCH), tag, __ATOMIC_RELAXED, __HIP_MEMORY_SCOPE_AGENT);
#pragma unroll
    for (int i = 0; i < 13; i++) {
      u32 id = 4 * i + mt; id = id > 48 ? 48 : id;
      u32 ss = id / 7, n = id - ss * 7;
      dma_sc1(rW, l16, WS_W1T + ((n < 6 ? t0 + n : tl) * 20 + ss) * 1024, L_W1A + id * 1024);
    }
    {
      u32 ci = lane + 64 * (mt & 1); ci = ci > 111 ? 111 : ci;
      u32 n = ci >> 4, col = (n < 6 ? t0 + n : tl) * 16 + (ci & 15);
      u32 cbv[5];
#pragma unroll
      for (int q = 0; q < 4; q++) cbv[q] = __builtin_amdgcn_raw_buffer_load_b32(rW, col * 4, WS_CS1 + q * 6912, SC1);
      cbv[4] = b1[col];
      sts32(L_CB + ci * 4, (asf(cbv[0]) + asf(cbv[1])) + (asf(cbv[2]) + asf(cbv[3])));
      sts32(L_CB + 512 + ci * 4, blo(cbv[4]));
      if (lane < 40) *(LAS u32*)(L_CB + 1024 + lane * 4) = ((const u32*)b2)[40 * j + lane];
    }
    STAMP(8);
    BAR();
#pragma unroll
    for (int i = 0; i < 23; i++) {
      u32 id = 4 * i + mt; id = id > 90 ? 90 : id;
      u32 s = id / 7, n = id - s * 7;
      dma_sc1(rW, l16, WS_W1T + ((n < 6 ? t0 + n : tl) * 20 + 7 + s) * 1024, L_W1B + id * 1024);
    }
#pragma unroll
    for (int i = 0; i < 10; i++) hres[i] = 0;
  }
  const u32 pc = 2 * P + nh;
  poll(ws, WS_FH + ((pc * 4 + mt) * 8 + (lane & 7)) * 8, tag);
  STAMP(11);
  {
    u4 hf[20], stv;
    const u32 soH = WS_HB + (pc * 4 + mt) * 20480;
    ldv<0>(stv, rW, lr * 64 + g * 16, WS_ST + (pc * 64 + mt * 16) * 64);
    auto ldh = [&](auto S) { ldv<(S & 3) * 1024>(hf[S], rW, l16, soH + (S & ~3) * 1024); };
    sfor<0, 6>(ldh);
    BAR();
    const u32 a0 = opaque(L_W1A + l16), a1 = opaque(L_W1B + l16), a2 = opaque(L_W1B + 49 * 1024 + l16);
    f4 ca[7];
    u4 wf[7];
#pragma unroll
    for (int n = 0; n < 7; n++) wf[n] = lds128(a0 + n * 1024);
    sfor<0, 20>([&](auto S) {
      constexpr int s = S;
      constexpr int w2lo = s - 5 > 8 ? s - 5 : 8;
      constexpr int yh = (s < 14 ? 5 : 19 - s), yw = s > w2lo ? s - w2lo : 0;
      waitv<yh + yw + (s - 6 >= 8 ? 1 : 0)>();
      if constexpr (s < 14) ldh(IC<s + 6>{});
      if constexpr (s >= 8) ldw2(IC<s - 8>{});
      sfor<0, 7>([&](auto N) {
        constexpr int n = N;
        u4 cur = wf[n];
        if constexpr (s == 0) mm0(ca[n], cur, hf[s]); else mm(ca[n], cur, hf[s]);
        if constexpr (s < 19) {
          constexpr int o = (((s + 1) % 7) * 7 + n) * 1024;
          wf[n] = lds128((s + 1 < 7 ? a0 : s + 1 < 14 ? a1 : a2) + o);
        }
      });
    });
    MM_DONE();
    STAMP(12);
    landed(stv);
    float sc, sm;
    {
      f4 v = __builtin_bit_cast(f4, stv);
      float mean = xsum16_32(v[0] + v[2]) * (1.f / 640.f);
      float d0 = v[0] * (1.f / 80.f) - mean, d1 = v[2] * (1.f / 80.f) - mean;
      float var = xsum16_32((v[1] + v[3]) + 80.f * (d0 * d0 + d1 * d1)) * (1.f / 640.f);
      float m1 = rbf(mean), sd1 = rbf(__builtin_amdgcn_sqrtf(rbf(rbf(var) + 1e-5f))), r1 = __builtin_amdgcn_rcpf(sd1);
      float m2 = rbf((mean - m1) * r1), sd2 = rbf(__builtin_amdgcn_sqrtf(rbf(rbf(var * r1 * r1) + 1e-5f)));
      sc = r1 * __builtin_amdgcn_rcpf(sd2);
      sm = sc * (m1 + m2 * sd1);
    }
    const u32 so = WS_HID + (pc * 4 + mt) * 54 * 1024;
    u32 hp[2][2];
    sfor<0, 7>([&](auto N) {
      constexpr int n = N;
      f4 c4 = __builtin_bit_cast(f4, lds128(L_CB + n * 64 + g * 16)), b4 = __builtin_bit_cast(f4, lds128(L_CB + 512 + n * 64 + g * 16));
      pin(ca[n]);
      ldw2(IC<12 + n>{});
      float t[4];
#pragma unroll
      for (int i = 0; i < 4; i++) {
        float v = ca[n][i] * sc + (b4[i] - sm * c4[i]);
        float u = v * (-2.302208198f - 0.1029432397f * v * v);
        t[i] = v * __builtin_amdgcn_rcpf(1.f + __builtin_amdgcn_exp2f(u));
      }
      hp[n & 1][0] = pk2(t[0], t[1]); hp[n & 1][1] = pk2(t[2], t[3]);
      if constexpr (n & 1) st128(u4{hp[0][0], hp[0][1], hp[1][0], hp[1][1]}, rW, l16, so + (t0 / 2 + n / 2) * 1024);
      if constexpr (n == 6) st64(u2{hp[0][0], hp[0][1]}, rW, rho < 6 ? l16 + odd * 8 : OOB, so + (tl >> 1) * 1024);
    });
  }
  waitv<0>();
  setflag(ws, WS_FHID + ((P * 8 + w) * 16 + c) * 8, tag, lane);
  STAMP(13);
  {
    sfor<19, 30>(ldw2);
    poll(ws, WS_FHID + ((P * 8 + 4 * (p & 1)) * 16 + lane) * 8, tag);
    STAMP(14);
    f4 da[4][5];
    u4 xf[7][4];
    u32 soX2[4];
#pragma unroll
    for (int a = 0; a < 4; a++) soX2[a] = WS_HID + ((p * 4 + ((mt + a) & 3)) * 54 + s0) * 1024;
    auto ldx = [&](auto S) {
      constexpr int s = S;
      sfor<0, 4>([&](auto A) { ldv<(s & 3) * 1024>(xf[s][A], rW, s == 6 ? vlast : l16, soX2[A] + (s & ~3) * 1024); });
    };
    ldx(IC<0>{}); ldx(IC<1>{});
    sfor<0, 7>([&](auto S) {
      constexpr int s = S;
      sfor<0, 4>([&](auto A) {
        constexpr int a = A;
        constexpr int yo[7] = {4, 9, 13, 12, 8, 4, 0};
        waitv<yo[s] + 3 - a>();
        u4 cur = xf[s][a];
        sfor<0, 5>([&](auto N) {
          constexpr int n = N;
          if constexpr (s == 0) mm0(da[a][n], w2f[n], cur); else mm(da[a][n], w2f[5 * s + n], cur);
        });
      });
      if constexpr (s == 0) { ldx(IC<2>{}); sfor<30, 35>(ldw2); }
      if constexpr (s == 1) { ldx(IC<3>{}); ldx(IC<4>{}); }
      if constexpr (s == 2) { ldx(IC<5>{}); ldx(IC<6>{}); }
    });
    MM_DONE();
    STAMP(15);
    BAR();
#pragma unroll
    for (int a = 0; a < 4; a++) {
      if (a > 0 || helper) {
#pragma unroll
        for (int n = 0; n < 5; n++) {
          pin(da[a][n]);
          sts128(L_RED + ((((mt + a) & 3) * 7 + (a + 4 * nh - 1)) * 5 + n) * 1024 + l16, __builtin_bit_cast(u4, da[a][n]));
        }
      }
    }
    BAR();
    if (!helper) {
      const rsrc_t rOut = __builtin_amdgcn_make_buffer_rsrc((void*)out, 0, 2048 * 1280, 0x00020000);
      const u32 ra = opaque(L_RED + mt * 35 * 1024 + l16);
#pragma unroll
      for (int n = 0; n < 5; n++) {
        f4 v = da[0][n];
        u2 bb[5]; bb[n] = lds64(L_CB + 1024 + n * 32 + g * 8);
#pragma unroll
        for (int a = 0; a < 7; a++) v += __builtin_bit_cast(f4, lds128(ra + (a * 5 + n) * 1024));
        u32 o0 = pk2(blo(hres[2 * n]) + rbf(rbf(v[0]) + blo(bb[n].x)), bhi(hres[2 * n]) + rbf(rbf(v[1]) + bhi(bb[n].x)));
        u32 o1 = pk2(blo(hres[2 * n + 1]) + rbf(rbf(v[2]) + blo(bb[n].y)), bhi(hres[2 * n + 1]) + rbf(rbf(v[3]) + bhi(bb[n].y)));
        __builtin_amdgcn_raw_buffer_store_b64(u2{o0, o1}, rOut, lr * 1280 + g * 8 + n * 32, (p * 64 + mt * 16) * 1280 + j * 160, 0);
      }
    }
  }
}

extern "C" void kernel_launch(void* const* d_in, const int* in_sizes, int n_in, void* d_out, int out_size, void* d_ws, size_t ws_size,
                              hipStream_t stream) {
  fused_block<<<256, 512, 0, stream>>>((const u16*)d_in[0], (const u16*)d_in[1], (const u16*)d_in[2], (const u16*)d_in[4], (const u16*)d_in[5],
                                       (const u16*)d_in[6], (const u16*)d_in[7], (u16*)d_out, (char*)d_ws);
}
```

```cpp
#include <hip/hip_runtime.h>
#include <stdint.h>
#define PROF
#define PROBE 1
#pragma clang diagnostic ignored "-Wint-to-pointer-cast"

typedef unsigned short u16;
typedef unsigned u32;
typedef unsigned long long u64;
typedef float f2 __attribute__((ext_vector_type(2)));
typedef float f4 __attribute__((ext_vector_type(4)));
typedef unsigned u2 __attribute__((ext_vector_type(2)));
typedef unsigned u4 __attribute__((ext_vector_type(4)));
typedef short s4 __attribute__((ext_vector_type(4)));
typedef __bf16 bf2 __attribute__((ext_vector_type(2)));
typedef __bf16 bf8 __attribute__((ext_vector_type(8)));
typedef __amdgpu_buffer_rsrc_t rsrc_t;
#define LAS __attribute__((address_space(3)))
#define GAS __attribute__((address_space(1)))
#define DI __device__ __forceinline__

template <int I> struct IC { static constexpr int v = I; constexpr operator int() const { return I; } };
template <int B, int E, class F> DI void sfor(F&& f) {
  if constexpr (B < E) { f(IC<B>{}); sfor<B + 1, E>(f); }
}

constexpr u32 WS_EPOCH = 0, WS_FRT = 4096, WS_FKV = 8192, WS_FO = 16384, WS_FH = 24576, WS_FHID = 32768;
constexpr u32 WS_KVP = 65536;
constexpr u32 WS_OB = 2u << 20;
constexpr u32 WS_HB = WS_OB + 32 * 81920;
constexpr u32 WS_ST = WS_HB + 32 * 81920;
constexpr u32 WS_CS1 = WS_ST + 2048 * 64;
constexpr u32 WS_W1T = WS_CS1 + 32768;
constexpr u32 WS_W2T = WS_W1T + 108 * 20480;
constexpr u32 WS_HID = WS_W2T + 8 * 54 * 5120;
constexpr u32 WS_END = WS_HID + 32 * 4 * 54 * 1024;
constexpr u32 WS_PROF = WS_END;
constexpr u32 OOB = 0x7FFFFF00u;
constexpr int SC1 = 16;

constexpr u32 L_X = 0, L_RING = 81920;
constexpr u32 L_WO = 0, L_IMG = 102400, L_W1A = 110592, L_CB = 160768, L_W1B = 0, L_RED = 0;

DI f4 mfma(u4 a, u4 b, f4 c) {
  return __builtin_amdgcn_mfma_f32_16x16x32_bf16(__builtin_bit_cast(bf8, a), __builtin_bit_cast(bf8, b), c, 0, 0, 0);
}
DI void mm(f4& c, u4 a, u4 b) { asm volatile("v_mfma_f32_16x16x32_bf16 %0, %1, %2, %0" : "+v"(c) : "v"(a), "v"(b) : "memory"); }
DI void mm0(f4& c, u4 a, u4 b) { asm volatile("v_mfma_f32_16x16x32_bf16 %0, %1, %2, 0" : "=&v"(c) : "v"(a), "v"(b) : "memory"); }
#define MM_DONE() asm volatile("s_nop 7\n\ts_nop 1" ::: "memory")
DI void pin(f4& c) { asm volatile("" : "+v"(c)); }
DI u32 opaque(u32 v) { asm volatile("" : "+v"(v)); return v; }

DI u32 pk2(float a, float b) { f2 v = {a, b}; return __builtin_bit_cast(u32, __builtin_convertvector(v, bf2)); }
DI float rbf(float a) { return __builtin_bit_cast(float, pk2(a, a) << 16); }
DI float blo(u32 v) { return __builtin_bit_cast(float, v << 16); }
DI float bhi(u32 v) { return __builtin_bit_cast(float, v & 0xFFFF0000u); }
DI float asf(u32 v) { return __builtin_bit_cast(float, v); }
DI u32 asu(float v) { return __builtin_bit_cast(u32, v); }

template <int OFF> DI void dma(rsrc_t r, u32 voff, u32 soff, u32 lds) {
  asm volatile("s_mov_b32 m0, %0\n\ts_nop 0\n\tbuffer_load_dwordx4 %1, %2, %3 offen offset:%4 lds" ::"s"(lds), "v"(voff), "s"(r), "s"(soff), "n"(OFF) : "memory");
}
template <int OFF> DI void dma_c(rsrc_t r, u32 voff, u32 soff) {
  asm volatile("buffer_load_dwordx4 %0, %1, %2 offen offset:%3 lds" ::"v"(voff), "s"(r), "s"(soff), "n"(OFF) : "memory");
}
DI void dma_sc1(rsrc_t r, u32 voff, u32 soff, u32 lds) {
  asm volatile("s_mov_b32 m0, %0\n\ts_nop 0\n\tbuffer_load_dwordx4 %1, %2, %3 offen sc1 lds" ::"s"(lds), "v"(voff), "s"(r), "s"(soff) : "memory");
}
template <int OFF> DI void ldv(u4& d, rsrc_t r, u32 voff, u32 soff) {
  asm volatile("buffer_load_dwordx4 %0, %1, %2, %3 offen offset:%4 sc1" : "=v"(d) : "v"(voff), "s"(r), "s"(soff), "n"(OFF) : "memory");
}
DI void ldv_plain(u4& d, rsrc_t r, u32 voff, u32 soff) {
  asm volatile("buffer_load_dwordx4 %0, %1, %2, %3 offen" : "=v"(d) : "v"(voff), "s"(r), "s"(soff) : "memory");
}
template <int N> DI void waitv() { asm volatile("s_waitcnt vmcnt(%0)" ::"n"(N) : "memory"); }
DI void landed(u4& d) { asm volatile("" : "+v"(d)); }
#define BAR() do { asm volatile("" ::: "memory"); __builtin_amdgcn_s_waitcnt(0xC07F); asm volatile("s_barrier" ::: "memory"); } while (0)
DI u4 lds128(u32 a) { return *(LAS u4*)(a); }
DI u2 lds64(u32 a) { return *(LAS u2*)(a); }
DI float lds32(u32 a) { return *(LAS float*)(a); }
DI void sts128(u32 a, u4 v) { *(LAS u4*)(a) = v; }
DI void sts64(u32 a, u2 v) { *(LAS u2*)(a) = v; }
DI void sts32(u32 a, float v) { *(LAS float*)(a) = v; }
DI u4 ldstr(u32 a0, u32 a1) {
  u2 x = __builtin_bit_cast(u2, __builtin_amdgcn_ds_read_tr16_b64_v4i16((LAS s4*)(a0)));
  u2 y = __builtin_bit_cast(u2, __builtin_amdgcn_ds_read_tr16_b64_v4i16((LAS s4*)(a1)));
  return u4{x.x, x.y, y.x, y.y};
}
DI float xsum16_32(float v) {
  auto r = __builtin_amdgcn_permlane16_swap(asu(v), asu(v), false, false);
  v = asf((u32)r[0]) + asf((u32)r[1]);
  auto q = __builtin_amdgcn_permlane32_swap(asu(v), asu(v), false, false);
  return asf((u32)q[0]) + asf((u32)q[1]);
}
DI void poll(const char* ws, u32 off, u64 tag) {
  const GAS u64* f = (const GAS u64*)(ws + off);
#pragma nounroll
  for (u32 it = 0; it < (1u << 18); ++it) {
    u64 v = __hip_atomic_load(f, __ATOMIC_RELAXED, __HIP_MEMORY_SCOPE_AGENT);
    if (__all(v == tag)) break;
    __builtin_amdgcn_s_sleep(1);
  }
  __builtin_amdgcn_fence(__ATOMIC_ACQUIRE, "wavefront");
  asm volatile("" ::: "memory");
}
DI void setflag(char* ws, u32 off, u64 tag, u32 lane) {
  if (lane == 0) __hip_atomic_store((GAS u64*)(ws + off), tag, __ATOMIC_RELAXED, __HIP_MEMORY_SCOPE_AGENT);
  asm volatile("" ::: "memory");
}
DI float phi(float t) {
  return fmaxf(t, 0.f) + (__builtin_amdgcn_exp2f(fminf(t, 0.f) * 1.4426950408889634f) + 1e-6f);
}
DI float sel4(f4 v, u32 i) { return i == 0 ? v[0] : i == 1 ? v[1] : i == 2 ? v[2] : v[3]; }
DI void st128(u4 v, rsrc_t r, u32 voff, u32 soff) {
  __builtin_amdgcn_raw_buffer_store_b128(v, r, voff, soff, SC1);
  asm volatile("s_nop 1" ::"v"(v) : "memory");
}
DI void st64(u2 v, rsrc_t r, u32 voff, u32 soff) { __builtin_amdgcn_raw_buffer_store_b64(v, r, voff, soff, SC1); }
DI void publish20(const u32* v, rsrc_t r, u32 lane, u32 so, u32 j) {
  if ((j & 1) == 0) {
    st128(u4{v[0], v[1], v[2], v[3]}, r, lane * 16, so);
    st128(u4{v[4], v[5], v[6], v[7]}, r, lane * 16, so + 1024);
    st64(u2{v[8], v[9]}, r, lane * 16, so + 2048);
  } else {
    st64(u2{v[0], v[1]}, r, lane * 16 + 8, so + 2048);
    st128(u4{v[2], v[3], v[4], v[5]}, r, lane * 16, so + 3072);
    st128(u4{v[6], v[7], v[8], v[9]}, r, lane * 16, so + 4096);
  }
}

#define STAMP(i) do { u64 t_; asm volatile("s_memrealtime %0\n\ts_waitcnt lgkmcnt(0)" : "=s"(t_) :: "memory"); asm volatile("s_nop 0\n\tv_writelane_b32 %0, %1, %2" : "+v"(tsv) : "s"((u32)t_), "n"(i)); } while (0)

__global__ __launch_bounds__(512, 2) void fused_block(const u16* __restrict__ x, const u16* __restrict__ wqkv, const u16* __restrict__ wo,
                                                      const u16* __restrict__ w1, const u16* __restrict__ b1, const u16* __restrict__ w2,
                                                      const u16* __restrict__ b2, u16* __restrict__ out, char* __restrict__ ws) {
  __shared__ __attribute__((aligned(1024))) char smem[163840];
  asm volatile("" ::"v"((u32)(uintptr_t)(LAS char*)smem));
  u32 tsv = 0;
  const u32 tid = threadIdx.x, lane = tid & 63, lr = lane & 15, g = lane >> 4;
  const u32 w = __builtin_amdgcn_readfirstlane(tid >> 6), mt = w & 3, nh = w >> 2;
  const bool helper = nh != 0;
  const u32 b = blockIdx.x, xg = b & 7, rr = b >> 3, p = 4 * xg + (rr >> 3), j = rr & 7;
  STAMP(0);
  const rsrc_t rX = __builtin_amdgcn_make_buffer_rsrc((void*)x, 0, 2048 * 1280, 0x00020000);
  const rsrc_t rQ = __builtin_amdgcn_make_buffer_rsrc((void*)wqkv, 0, 640 * 3840, 0x00020000);
  const rsrc_t rW = __builtin_amdgcn_make_buffer_rsrc((void*)ws, 0, WS_END, 0x00020000);
  const u32 l16 = lane * 16;
  u4 ONES = {0x3F803F80u, 0x3F803F80u, 0x3F803F80u, 0x3F803F80u};
  asm volatile("" : "+v"(ONES));

  u2 ep;
  asm volatile("buffer_load_dwordx2 %0, off, %1, 0 sc1" : "=v"(ep) : "s"(rW) : "memory");
  const u32 rot = (xg + 3 * (p & 3)) % 10, rotq = (xg + (p & 3)) % 5;
  const u32 xa0 = L_X + (lr >> 3) * 1024 + (lr & 7) * 128 + ((g ^ (lr & 7)) << 4);
  const u32 tr0 = (8 * g + (lr >> 2) + 4 * (g & 1)) * 160 + (lr & 3) * 8;
  const u32 tr1 = (8 * g + (lr >> 2) + 4 * ((g & 1) ^ 1)) * 160 + (lr & 3) * 8;
  const u32 P = p >> 1, c = ((p & 1) << 3) | j, rho = c >> 1, odd = c & 1;
  const u32 t0 = rho < 6 ? 14 * rho + 8 * odd : 84 + 12 * (rho - 6) + 6 * odd;
  const u32 tl = rho < 6 ? 14 * rho + 6 + odd : t0;
  const u32 s0 = w < 6 ? 7 * w : 6 * w + 6;
  const u32 soW2 = WS_W2T + (j * 54 + s0) * 5120;
  const u32 vlast = w >= 6 ? OOB : l16;
  u4 w2f[35];
  auto ldw2 = [&](auto I) {
    constexpr int i = I;
    ldv<(i & 3) * 1024>(w2f[i], rW, i >= 30 ? vlast : l16, soW2 + (i & ~3) * 1024);
  };
  u32 hres[10];
  u64 tag;
#define GET_TAG() do { u32 lo_, hi_; asm volatile("v_readfirstlane_b32 %0, %2\n\tv_readfirstlane_b32 %1, %3\n\ts_nop 3" : "=s"(lo_), "=s"(hi_) : "v"(ep.x), "v"(ep.y) : "memory"); \
    tag = (((u64)hi_ << 32) | lo_) + 1; } while (0)
  if (!helper) {
    const u32 mh = mt & 1, nq = mt >> 1;
    f4 acc[2][5], cs[3], rsum = {0, 0, 0, 0}, gram = {0, 0, 0, 0};
#pragma unroll
    for (int i = 0; i < 5; i++) { acc[0][i] = f4{0, 0, 0, 0}; acc[1][i] = f4{0, 0, 0, 0}; pin(acc[0][i]); pin(acc[1][i]); }
#pragma unroll
    for (int i = 0; i < 3; i++) { cs[i] = f4{0, 0, 0, 0}; pin(cs[i]); }
    pin(rsum); pin(gram);
    {
      const u32 oP = L_RING + nq * 96 + mh * 5120, oR = L_RING + nq * 96 + (mh ^ 1) * 5120, oC = L_RING + 64 + nq * 5120;
      const u32 bP0 = tr0 + oP, bP1 = tr1 + oP, bR0 = tr0 + oR, bR1 = tr1 + oR, bC0 = tr0 + oC, bC1 = tr1 + oC;
      const u32 xs0 = xa0 + mh * 4096 + nq * 2048;
      auto half = [&](u4* cur, u4* nxt, u32 xa, u32 so, auto KS) {
        constexpr int o = KS * 10240;
        const u32 p0 = bP0 + so, p1 = bP1 + so, r0 = bR0 + so, r1 = bR1 + so, c0 = bC0 + so, c1 = bC1 + so;
        nxt[5] = lds128(xa); nxt[0] = ldstr(p0 + o, p1 + o);
        mm(rsum, cur[5], ONES); mm(gram, cur[5], cur[5]);
        nxt[6] = lds128(xa ^ 2048);
        mm(acc[0][0], cur[5], cur[0]); mm(acc[1][0], cur[6], cur[0]);
        nxt[1] = ldstr(p0 + o + 32, p1 + o + 32);
        mm(cs[0], ONES, cur[0]); mm(acc[0][1], cur[5], cur[1]);
        nxt[2] = ldstr(c0 + o, c1 + o);
        mm(acc[1][1], cur[6], cur[1]); mm(cs[1], ONES, cur[1]);
        nxt[3] = ldstr(r0 + o, r1 + o);
        mm(acc[0][2], cur[5], cur[2]); mm(acc[1][2], cur[6], cur[2]);
        nxt[4] = ldstr(r0 + o + 32, r1 + o + 32);
        mm(cs[2], ONES, cur[2]);
        mm(acc[0][3], cur[5], cur[3]); mm(acc[1][3], cur[6], cur[3]);
        mm(acc[0][4], cur[5], cur[4]); mm(acc[1][4], cur[6], cur[4]);
      };
      u4 fa[7], fb[7];
      u32 st = rot;
      waitv<0>();
      GET_TAG();
      BAR();
      STAMP(1);
      {
        const u32 xa = xs0 + st * 8192;
        fa[5] = lds128(xa); fa[6] = lds128(xa ^ 2048); fa[0] = ldstr(bP0, bP1); fa[1] = ldstr(bP0 + 32, bP1 + 32); fa[2] = ldstr(bC0, bC1);
        fa[3] = ldstr(bR0, bR1); fa[4] = ldstr(bR0 + 32, bR1 + 32);
      }
#pragma nounroll
      for (u32 i = 0; i < 10; i++) {
        half(fa, fb, (xs0 ^ 64) + st * 8192, (i & 3) * 20480, IC<1>{});
        BAR();
        st = st + 1 >= 10 ? 0 : st + 1;
        half(fb, fa, xs0 + st * 8192, ((i + 1) & 3) * 20480, IC<0>{});
      }
      MM_DONE();
    }
    STAMP(2);
    float qmb, qrs;
    {
      const u32 LS = L_RING, LC = L_RING + 512, EX = L_RING + 20480;
      pin(rsum); pin(gram);
      {
        const float rs_ = (lr & 2) ? ((lr & 1) ? rsum[3] : rsum[2]) : ((lr & 1) ? rsum[1] : rsum[0]);
        const float gr_ = (lr & 2) ? ((lr & 1) ? gram[3] : gram[2]) : ((lr & 1) ? gram[1] : gram[0]);
        float m = rs_ * (1.f / 640.f);
        float v = fmaxf(gr_ * (1.f / 640.f) - m * m, 0.f);
        float sd = rbf(__builtin_amdgcn_sqrtf(rbf(rbf(v) + 1e-5f)));
        float rs = __builtin_amdgcn_rcpf(sd);
        if ((lr >> 2) == g) sts64(LS + ((2 * mh + nq) * 16 + lr) * 8, u2{asu(-rbf(m) * rs), asu(rs)});
      }
      const u32 iP = 3 * nq + 5 * mh, iR = 3 * nq + 5 * (mh ^ 1), iC = 2 + 5 * nq;
#pragma unroll
      for (int i = 0; i < 3; i++) pin(cs[i]);
      if (g == 0) { sts32(LC + iP * 64 + lr * 4, cs[0][0]); sts32(LC + iP * 64 + 64 + lr * 4, cs[1][0]); }
      BAR();
      f4 st[2][2];
#pragma unroll
      for (int m = 0; m < 2; m++) {
        u32 a = LS + ((2 * mh + (nq ^ m)) * 16 + 4 * g) * 8;
        st[m][0] = __builtin_bit_cast(f4, lds128(a)); st[m][1] = __builtin_bit_cast(f4, lds128(a + 16));
      }
      float cR0 = lds32(LC + iR * 64 + lr * 4), cR1 = lds32(LC + iR * 64 + 64 + lr * 4);
      { u2 q = lds64(LS + (16 * mt + lr) * 8); qmb = asf(q.x); qrs = asf(q.y); }
      sfor<0, 5>([&](auto N) {
        constexpr int n = N;
        pin(acc[0][n]); pin(acc[1][n]);
        float c = n < 3 ? cs[n < 3 ? n : 0][0] : (n == 3 ? cR0 : cR1);
        float t[8];
#pragma unroll
        for (int m = 0; m < 2; m++)
#pragma unroll
          for (int i = 0; i < 4; i++) t[4 * m + i] = acc[m][n][i] * st[m][i >> 1][2 * (i & 1) + 1] + st[m][i >> 1][2 * (i & 1)] * c;
        bool isk = n < 2 ? mh == 0 : (n == 2 ? nq == 0 : mh == 1);
        if (isk) {
#pragma unroll
          for (int i = 0; i < 8; i++) t[i] = phi(t[i]);
        }
        u32 idx = n < 2 ? iP + n : (n == 2 ? iC : iR + n - 3);
        u32 a = EX + mh * 10240 + idx * 1024 + l16;
        sts64(a + nq * 8, u2{pk2(t[0], t[1]), pk2(t[2], t[3])});
        sts64(a + (nq ^ 1) * 8, u2{pk2(t[4], t[5]), pk2(t[6], t[7])});
      });
      BAR();
      const u32 ex = EX + mt * 1024 + l16;
      u4 kf[2][2], vf[2][2];
#pragma unroll
      for (int m = 0; m < 2; m++) {
        kf[m][0] = lds128(ex + m * 10240); kf[m][1] = lds128(ex + m * 10240 + 1024);
        vf[m][0] = lds128(ex + m * 10240 + 5120); vf[m][1] = lds128(ex + m * 10240 + 6144);
      }
      BAR();
      const u32 so = WS_KVP + ((j * 32 + p) * 4 + mt) * 896;
      const u32 e0 = lr - 4 * mt, e1 = e0 + 16;
#pragma unroll
      for (int a = 0; a < 2; a++) {
        f4 z4 = {0, 0, 0, 0};
        f4 kv0 = mfma(kf[1][a], vf[1][0], mfma(kf[0][a], vf[0][0], z4)), kv1 = mfma(kf[1][a], vf[1][1], mfma(kf[0][a], vf[0][1], z4));
        f4 zz = mfma(kf[1][a], ONES, mfma(kf[0][a], ONES, z4));
        const u32 dq = 4 * a + g - mt;
        st64(u2{pk2(kv0[0], kv0[1]), pk2(kv0[2], kv0[3])}, rW, (dq < 5 && e0 < 20) ? e0 * 40 + dq * 8 : OOB, so);
        st64(u2{pk2(kv1[0], kv1[1]), pk2(kv1[2], kv1[3])}, rW, (dq < 5 && e1 < 20) ? e1 * 40 + dq * 8 : OOB, so);
        st64(u2{pk2(zz[0], zz[1]), pk2(zz[2], zz[3])}, rW, (dq < 5 && lr == 0) ? 800 + dq * 8 : OOB, so);
      }
    }
    waitv<0>();
    setflag(ws, WS_FKV + ((j * 4 + mt) * 32 + p) * 8, tag, lane);
    STAMP(3);
    f4 qa[5], qc[5];
    auto q_stage = [&](auto FIRST, u32 qi, u32 slot) {
      u32 qs = qi + rotq; qs = qs >= 5 ? qs - 5 : qs;
      const u32 xb = xa0 + mt * 2048 + qs * 16384, xc = xb ^ 64;
      const u32 a0 = L_RING + slot * 20480 + tr0, a1 = L_RING + slot * 20480 + tr1;
      u4 xf[4], wf[5];
      xf[0] = lds128(xb);
#pragma unroll
      for (int n = 0; n < 5; n++) wf[n] = ldstr(a0 + n * 32, a1 + n * 32);
      xf[1] = lds128(xc); xf[2] = lds128(xb + 8192); xf[3] = lds128(xc + 8192);
      sfor<0, 4>([&](auto KS) {
        constexpr int ks = KS;
        sfor<0, 5>([&](auto N) {
          constexpr int n = N;
          u4 cur = wf[n];
          if constexpr (FIRST && ks == 0) { mm0(qa[n], cur, xf[ks]); mm0(qc[n], cur, ONES); }
          else { mm(qa[n], cur, xf[ks]); mm(qc[n], cur, ONES); }
          if constexpr (ks < 3) wf[n] = ldstr(a0 + (ks + 1) * 5120 + n * 32, a1 + (ks + 1) * 5120 + n * 32);
        });
      });
    };
    q_stage(IC<1>{}, 0, 2);
    BAR();
#pragma nounroll
    for (u32 qi = 1; qi < 5; qi++) {
      q_stage(IC<0>{}, qi, (qi + 2) & 3);
      if (qi == 1 || qi == 3) BAR();
    }
    MM_DONE();
    STAMP(4);
    u2 xres[5];
#pragma unroll
    for (int n = 0; n < 5; n++) {
      u32 ch = 10 * j + 2 * n + (g >> 1);
      xres[n] = lds64(L_X + (ch >> 3) * 8192 + (2 * mt + (lr >> 3)) * 1024 + (lr & 7) * 128 + (((ch & 7) ^ (lr & 7)) << 4) + (g & 1) * 8);
    }
    u32 pq[5][2];
#pragma unroll
    for (int n = 0; n < 5; n++) {
      pin(qa[n]); pin(qc[n]);
      float t[4];
#pragma unroll
      for (int i = 0; i < 4; i++) t[i] = phi(qa[n][i] * qrs + qmb * qc[n][i]);
      pq[n][0] = opaque(pk2(t[0], t[1])); pq[n][1] = opaque(pk2(t[2], t[3]));
    }
    STAMP(5);
    BAR();
    STAMP(6);
    {
      float o[20];
#pragma unroll
      for (int h = 0; h < 4; h++) {
        u4 bq = {pq[h][0], pq[h][1], pq[h + 1][0], pq[h + 1][1]};
        f4 z4 = {0, 0, 0, 0};
        f4 d0 = mfma(lds128(L_IMG + h * 2048 + l16), bq, z4);
        f4 d1 = mfma(lds128(L_IMG + h * 2048 + 1024 + l16), bq, z4);
        float rd = __builtin_amdgcn_rcpf(rbf(rbf(d1[1]) + 1e-6f));
#pragma unroll
        for (int i = 0; i < 4; i++) o[5 * h + i] = rbf(d0[i]) * rd;
        o[5 * h + 4] = rbf(d1[0]) * rd;
      }
      u32 ov[10];
#pragma unroll
      for (int i = 0; i < 10; i++) ov[i] = pk2(o[2 * i], o[2 * i + 1]);
      publish20(ov, rW, lane, WS_OB + ((p * 4 + mt) * 20 + 5 * (j >> 1)) * 1024, j);
    }
    waitv<0>();
    setflag(ws, WS_FO + ((p * 4 + mt) * 8 + j) * 8, tag, lane);
    STAMP(7);
    poll(ws, WS_FO + ((p * 4 + mt) * 8 + (lane & 7)) * 8, tag);
    STAMP(8);
    u4 of[20];
    auto ldo = [&](auto S) { ldv<(S & 3) * 1024>(of[S], rW, l16, WS_OB + ((p * 4 + mt) * 20 + (S & ~3)) * 1024); };
    sfor<0, 8>(ldo);
    BAR();
    const u32 a0 = opaque(L_WO + tr0), a1 = opaque(L_WO + tr1), a2 = opaque(L_WO + tr0 + 51200), a3 = opaque(L_WO + tr1 + 51200);
    f4 ha[5];
    u4 wf[5];
#pragma unroll
    for (int n = 0; n < 5; n++) wf[n] = ldstr(a0 + n * 32, a1 + n * 32);
    sfor<0, 20>([&](auto S) {
      constexpr int s = S;
      waitv<(s < 12 ? 7 : 19 - s)>();
      if constexpr (s < 12) ldo(IC<s + 8>{});
      sfor<0, 5>([&](auto N) {
        constexpr int n = N;
        u4 cur = wf[n];
        if constexpr (s == 0) mm0(ha[n], cur, of[s]); else mm(ha[n], cur, of[s]);
        if constexpr (s < 19) {
          constexpr int o = ((s + 1) % 10) * 5120 + n * 32;
          wf[n] = (s + 1 < 10) ? ldstr(a0 + o, a1 + o) : ldstr(a2 + o, a3 + o);
        }
      });
    });
    MM_DONE();
    STAMP(9);
    float hv[20], sum = 0.f;
#pragma unroll
    for (int n = 0; n < 5; n++) {
      pin(ha[n]);
      hres[2 * n] = pk2(blo(xres[n].x) + rbf(ha[n][0]), bhi(xres[n].x) + rbf(ha[n][1]));
      hres[2 * n + 1] = pk2(blo(xres[n].y) + rbf(ha[n][2]), bhi(xres[n].y) + rbf(ha[n][3]));
      hv[4 * n] = blo(hres[2 * n]); hv[4 * n + 1] = bhi(hres[2 * n]); hv[4 * n + 2] = blo(hres[2 * n + 1]); hv[4 * n + 3] = bhi(hres[2 * n + 1]);
      sum += (hv[4 * n] + hv[4 * n + 1]) + (hv[4 * n + 2] + hv[4 * n + 3]);
    }
    publish20(hres, rW, lane, WS_HB + ((p * 4 + mt) * 20 + 5 * (j >> 1)) * 1024, j);
    sum = xsum16_32(sum);
    float mu = sum * (1.f / 80.f), m2 = 0.f;
#pragma unroll
    for (int i = 0; i < 20; i++) { float d = hv[i] - mu; m2 += d * d; }
    m2 = xsum16_32(m2);
    st64(u2{asu(sum), asu(m2)}, rW, g == 0 ? lr * 64 : OOB, WS_ST + (p * 64 + mt * 16) * 64 + j * 8);
    waitv<0>();
    setflag(ws, WS_FH + ((p * 4 + mt) * 8 + j) * 8, tag, lane);
    STAMP(10);
    BAR();
  } else {
    u32 voffW[5];
#pragma unroll
    for (int i = 0; i < 5; i++) {
      u32 f = 64 * i + lane, sg = (f * 6554) >> 16, ch = f - sg * 10;
      voffW[i] = (sg ^ (((sg >> 3) & 1) << 2)) * 3840 + ch * 16 - (i & 3) * 1024;
    }
    const u32 voffX = (lane >> 3) * 1280 + ((lane & 7) ^ (lane >> 3)) * 16;
    const u32 voffX1 = voffX + 8 * 1280 - 1024;
    const u32 soX = (p * 64 + 16 * mt) * 1280, soW = (mt >> 1) * 32 * 3840 + ((mt & 1) + 1) * 1280 + j * 160;
    auto issueW = [&](u32 so, u32 ld) {
      dma<0>(rQ, voffW[0], so, ld); dma_c<1024>(rQ, voffW[1], so); dma_c<2048>(rQ, voffW[2], so); dma_c<3072>(rQ, voffW[3], so);
      dma<0>(rQ, voffW[4], so, ld + 4096);
    };
    auto issueKV = [&](u32 i) {
      u32 st = i + rot; st = st >= 10 ? st - 10 : st;
      dma<0>(rX, voffX, soX + st * 128, L_X + st * 8192 + mt * 2048); dma_c<1024>(rX, voffX1, soX + st * 128);
      issueW(soW + st * 64 * 3840, L_RING + (i & 3) * 20480 + mt * 5120);
    };
    auto issueQ = [&](u32 qi, u32 slot) {
      u32 qs = qi + rotq; qs = qs >= 5 ? qs - 5 : qs;
      issueW((qs * 128 + mt * 32) * 3840 + j * 160, L_RING + slot * 20480 + mt * 5120);
    };
    issueKV(0); issueKV(1);
    waitv<7>(); BAR();
    issueKV(2); issueKV(3);
#pragma nounroll
    for (u32 i = 0; i < 6; i++) { waitv<14>(); BAR(); issueKV(i + 4); }
    waitv<14>(); BAR(); issueQ(0, 2);
    waitv<12>(); BAR(); issueQ(1, 3);
    waitv<10>(); BAR();
    u4 rt[5];
    const bool rt_w1 = b < 108;
    const u32 ru = rt_w1 ? b : b - 108, rua = ru / 27, rub = ru - rua * 27;
    {
      const rsrc_t rS = __builtin_amdgcn_make_buffer_rsrc((void*)(rt_w1 ? w1 : w2), 0, b < 216 ? 640 * 3456 : 0, 0x00020000);
      const u32 pitch = rt_w1 ? 3456 : 1280;
      const u32 vo = (8 * mt + (lane >> 3)) * pitch + (lane & 7) * 16;
#pragma unroll
      for (int q = 0; q < 5; q++) {
        u32 B = 5 * ru + q, cg = B / 54;
        ldv_plain(rt[q], rS, vo, rt_w1 ? (rua * 160 + q * 32) * 3456 + rub * 128 : (B - cg * 54) * 32 * 1280 + cg * 128);
      }
    }
    BAR();
    GET_TAG();
    u32 voffO[25];
#pragma unroll
    for (int i = 0; i < 25; i++) {
      u32 f = 64 * i + lane, sg = (f * 6554) >> 16, ch = f - sg * 10;
      u32 om = sg & 31, ka = om ^ (((om >> 3) & 1) << 2), i40 = 8 * (sg >> 5) + (ka & 7);
      u32 hf = i40 >= 20 ? 1u : 0u, id = i40 - 20 * hf, hh = (id * 13) >> 6;
      voffO[i] = opaque((80 * hf + 15 * hh + 5 * (ka >> 3) + id) * 1280 + ch * 16 + 4096 - (i & 3) * 1024);
    }
    BAR();
    BAR();
    waitv<5>();
    BAR();
    issueQ(2, 0); issueQ(3, 1);
    BAR();
    issueQ(4, 2);
    waitv<5>();
    BAR();
    {
      u32 row = 8 * mt + (lane >> 3);
#pragma unroll
      for (int q = 0; q < 5; q++) { landed(rt[q]); sts128(L_RING + 61440 + q * 4096 + row * 128 + (((lane & 7) ^ (((row >> 1) & 3) << 1)) << 4), rt[q]); }
    }
    waitv<0>();
    BAR();
    {
      sts128(L_IMG + mt * 2048 + l16, u4{0, 0, 0, 0}); sts128(L_IMG + mt * 2048 + 1024 + l16, u4{0, 0, 0, 0});
      u32 r0 = 4 * g + (lr >> 2);
      u32 ta = L_RING + 61440 + r0 * 128 + (((2 * mt + ((lr >> 1) & 1)) ^ (((r0 >> 1) & 3) << 1)) << 4) + (lr & 1) * 8;
      f4 csum = {0, 0, 0, 0};
      const u32 vo = b < 216 ? l16 : OOB;
#pragma unroll
      for (int q = 0; q < 5; q++) {
        u4 f = ldstr(ta + q * 4096, ta + q * 4096 + 2048);
        csum = mfma(f, ONES, csum);
        u32 B = 5 * ru + q, cg = B / 54, tau = 4 * cg + mt, jj = tau / 5;
        st128(f, rW, vo, rt_w1 ? WS_W1T + ((4 * rub + mt) * 20 + rua * 5 + q) * 1024 : WS_W2T + ((jj * 54 + (B - cg * 54)) * 5 + (tau - jj * 5)) * 1024);
      }
      st128(__builtin_bit_cast(u4, csum), rW, (rt_w1 && lr == 0) ? g * 16 : OOB, WS_CS1 + rua * 6912 + (4 * rub + mt) * 64);
    }
    STAMP(4);
    poll(ws, WS_FKV + ((j * 4 + mt) * 32 + (lane & 31)) * 8, tag);
    STAMP(5);
    {
      const u32 so = WS_KVP + (j * 32 * 4 + mt) * 896;
      const u32 v0 = lane < 53 ? l16 : OOB;
      u4 a[32];
      sfor<0, 32>([&](auto Q) { ldv<0>(a[Q], rW, v0, so + Q * 3584); });
      float s[8] = {0, 0, 0, 0, 0, 0, 0, 0};
      sfor<0, 32>([&](auto Q) {
        waitv<31 - Q>(); landed(a[Q]);
#pragma unroll
        for (int i = 0; i < 4; i++) { s[2 * i] += blo(a[Q][i]); s[2 * i + 1] += bhi(a[Q][i]); }
      });
#pragma unroll
      for (int h = 0; h < 2; h++) {
        u32 qd = 2 * lane + h, e = (qd * 205) >> 10, dq = qd - e * 5;
        u32 gs = dq + mt, grp = (e * 13) >> 6, ii = e - grp * 5;
        u2 v = {pk2(s[4 * h], s[4 * h + 1]), pk2(s[4 * h + 2], s[4 * h + 3])};
        u32 base = L_IMG + mt * 2048 + (gs & 3) * 256 + (gs >> 2) * 8;
        if (e < 20) sts64(base + (ii < 4 ? (4 * grp + ii) * 16 : 1024 + 4 * grp * 16), v);
        if (e == 20) {
#pragma unroll
          for (int q = 0; q < 4; q++) sts64(base + 1024 + (4 * q + 1) * 16, v);
        }
      }
    }
    STAMP(6);
    BAR();
    if (mt == 0) setflag(ws, WS_FRT + b * 8, tag, lane);
    {
      const rsrc_t rO = __builtin_amdgcn_make_buffer_rsrc((void*)((const char*)wo - 4096), 0, 640 * 1280 + 4096, 0x00020000);
      const u32 so = mt * 160 * 1280 + j * 160;
      sfor<0, 25>([&](auto I) {
        constexpr int i = I;
        if constexpr ((i & 3) == 0) dma<0>(rO, voffO[i], so, L_WO + mt * 25600 + i * 1024);
        else dma_c<(i & 3) * 1024>(rO, voffO[i], so);
      });
    }
    waitv<0>();
    STAMP(7);
    BAR();
#pragma nounroll
    for (int q = 0; q < 4; q++) poll(ws, WS_FRT + (64 * q + lane) * 8, tag);
    if (b == 0 && tid == 256) __hip_atomic_store((GAS u64*)(ws + WS_EPOCH), tag, __ATOMIC_RELAXED, __HIP_MEMORY_SCOPE_AGENT);
#pragma unroll
    for (int i = 0; i < 13; i++) {
      u32 id = 4 * i + mt; id = id > 48 ? 48 : id;
      u32 ss = id / 7, n = id - ss * 7;
      dma_sc1(rW, l16, WS_W1T + ((n < 6 ? t0 + n : tl) * 20 + ss) * 1024, L_W1A + id * 1024);
    }
    {
      u32 ci = lane + 64 * (mt & 1); ci = ci > 111 ? 111 : ci;
      u32 n = ci >> 4, col = (n < 6 ? t0 + n : tl) * 16 + (ci & 15);
      u32 cbv[5];
#pragma unroll
      for (int q = 0; q < 4; q++) cbv[q] = __builtin_amdgcn_raw_buffer_load_b32(rW, col * 4, WS_CS1 + q * 6912, SC1);
      cbv[4] = b1[col];
      sts32(L_CB + ci * 4, (asf(cbv[0]) + asf(cbv[1])) + (asf(cbv[2]) + asf(cbv[3])));
      sts32(L_CB + 512 + ci * 4, blo(cbv[4]));
      if (lane < 40) *(LAS u32*)(L_CB + 1024 + lane * 4) = ((const u32*)b2)[40 * j + lane];
    }
    STAMP(8);
    BAR();
#pragma unroll
    for (int i = 0; i < 23; i++) {
      u32 id = 4 * i + mt; id = id > 90 ? 90 : id;
      u32 s = id / 7, n = id - s * 7;
      dma_sc1(rW, l16, WS_W1T + ((n < 6 ? t0 + n : tl) * 20 + 7 + s) * 1024, L_W1B + id * 1024);
    }
#pragma unroll
    for (int i = 0; i < 10; i++) hres[i] = 0;
  }
  const u32 pc = 2 * P + nh;
  poll(ws, WS_FH + ((pc * 4 + mt) * 8 + (lane & 7)) * 8, tag);
  STAMP(11);
  {
    u4 hf[20], stv;
    const u32 soH = WS_HB + (pc * 4 + mt) * 20480;
    ldv<0>(stv, rW, lr * 64 + g * 16, WS_ST + (pc * 64 + mt * 16) * 64);
    auto ldh = [&](auto S) { ldv<(S & 3) * 1024>(hf[S], rW, l16, soH + (S & ~3) * 1024); };
    sfor<0, 6>(ldh);
    BAR();
    const u32 a0 = opaque(L_W1A + l16), a1 = opaque(L_W1B + l16), a2 = opaque(L_W1B + 49 * 1024 + l16);
    f4 ca[7];
    u4 wf[7];
#pragma unroll
    for (int n = 0; n < 7; n++) wf[n] = lds128(a0 + n * 1024);
    sfor<0, 20>([&](auto S) {
      constexpr int s = S;
      constexpr int w2lo = s - 5 > 8 ? s - 5 : 8;
      constexpr int yh = (s < 14 ? 5 : 19 - s), yw = s > w2lo ? s - w2lo : 0;
      waitv<yh + yw + (s - 6 >= 8 ? 1 : 0)>();
      if constexpr (s < 14) ldh(IC<s + 6>{});
      if constexpr (s >= 8) ldw2(IC<s - 8>{});
      sfor<0, 7>([&](auto N) {
        constexpr int n = N;
        u4 cur = wf[n];
        if constexpr (s == 0) mm0(ca[n], cur, hf[s]); else mm(ca[n], cur, hf[s]);
        if constexpr (s < 19) {
          constexpr int o = (((s + 1) % 7) * 7 + n) * 1024;
          wf[n] = lds128((s + 1 < 7 ? a0 : s + 1 < 14 ? a1 : a2) + o);
        }
      });
    });
    MM_DONE();
    STAMP(12);
    landed(stv);
    float sc, sm;
    {
      f4 v = __builtin_bit_cast(f4, stv);
      float mean = xsum16_32(v[0] + v[2]) * (1.f / 640.f);
      float d0 = v[0] * (1.f / 80.f) - mean, d1 = v[2] * (1.f / 80.f) - mean;
      float var = xsum16_32((v[1] + v[3]) + 80.f * (d0 * d0 + d1 * d1)) * (1.f / 640.f);
      float m1 = rbf(mean), sd1 = rbf(__builtin_amdgcn_sqrtf(rbf(rbf(var) + 1e-5f))), r1 = __builtin_amdgcn_rcpf(sd1);
      float m2 = rbf((mean - m1) * r1), sd2 = rbf(__builtin_amdgcn_sqrtf(rbf(rbf(var * r1 * r1) + 1e-5f)));
      sc = r1 * __builtin_amdgcn_rcpf(sd2);
      sm = sc * (m1 + m2 * sd1);
    }
    const u32 so = WS_HID + (pc * 4 + mt) * 54 * 1024;
    u32 hp[2][2];
    sfor<0, 7>([&](auto N) {
      constexpr int n = N;
      f4 c4 = __builtin_bit_cast(f4, lds128(L_CB + n * 64 + g * 16)), b4 = __builtin_bit_cast(f4, lds128(L_CB + 512 + n * 64 + g * 16));
      pin(ca[n]);
      ldw2(IC<12 + n>{});
      float t[4];
#pragma unroll
      for (int i = 0; i < 4; i++) {
        float v = ca[n][i] * sc + (b4[i] - sm * c4[i]);
        float u = v * (-2.302208198f - 0.1029432397f * v * v);
        t[i] = v * __builtin_amdgcn_rcpf(1.f + __builtin_amdgcn_exp2f(u));
      }
      hp[n & 1][0] = pk2(t[0], t[1]); hp[n & 1][1] = pk2(t[2], t[3]);
      if constexpr (n & 1) st128(u4{hp[0][0], hp[0][1], hp[1][0], hp[1][1]}, rW, l16, so + (t0 / 2 + n / 2) * 1024);
      if constexpr (n == 6) st64(u2{hp[0][0], hp[0][1]}, rW, rho < 6 ? l16 + odd * 8 : OOB, so + (tl >> 1) * 1024);
    });
  }
  waitv<0>();
  setflag(ws, WS_FHID + ((P * 8 + w) * 16 + c) * 8, tag, lane);
  STAMP(13);
  {
    sfor<19, 30>(ldw2);
    poll(ws, WS_FHID + ((P * 8 + 4 * (p & 1)) * 16 + lane) * 8, tag);
    STAMP(14);
    f4 da[4][5];
    u4 xf[7][4];
    u32 soX2[4];
#pragma unroll
    for (int a = 0; a < 4; a++) soX2[a] = WS_HID + ((p * 4 + ((mt + a) & 3)) * 54 + s0) * 1024;
    auto ldx = [&](auto S) {
      constexpr int s = S;
      sfor<0, 4>([&](auto A) { ldv<(s & 3) * 1024>(xf[s][A], rW, s == 6 ? vlast : l16, soX2[A] + (s & ~3) * 1024); });
    };
    ldx(IC<0>{}); ldx(IC<1>{});
    sfor<0, 7>([&](auto S) {
      constexpr int s = S;
      sfor<0, 4>([&](auto A) {
        constexpr int a = A;
        constexpr int yo[7] = {4, 9, 13, 12, 8, 4, 0};
        waitv<yo[s] + 3 - a>();
        u4 cur = xf[s][a];
        sfor<0, 5>([&](auto N) {
          constexpr int n = N;
          if constexpr (s == 0) mm0(da[a][n], w2f[n], cur); else mm(da[a][n], w2f[5 * s + n], cur);
        });
      });
      if constexpr (s == 0) { ldx(IC<2>{}); sfor<30, 35>(ldw2); }
      if constexpr (s == 1) { ldx(IC<3>{}); ldx(IC<4>{}); }
      if constexpr (s == 2) { ldx(IC<5>{}); ldx(IC<6>{}); }
    });
    MM_DONE();
    STAMP(15);
    BAR();
#pragma unroll
    for (int a = 0; a < 4; a++) {
      if (a > 0 || helper) {
#pragma unroll
        for (int n = 0; n < 5; n++) {
          pin(da[a][n]);
          sts128(L_RED + ((((mt + a) & 3) * 7 + (a + 4 * nh - 1)) * 5 + n) * 1024 + l16, __builtin_bit_cast(u4, da[a][n]));
        }
      }
    }
    BAR();
    if (!helper) {
      const rsrc_t rOut = __builtin_amdgcn_make_buffer_rsrc((void*)out, 0, 2048 * 1280, 0x00020000);
      const u32 ra = opaque(L_RED + mt * 35 * 1024 + l16);
#pragma unroll
      for (int n = 0; n < 5; n++) {
        f4 v = da[0][n];
        u2 bb[5]; bb[n] = lds64(L_CB + 1024 + n * 32 + g * 8);
#pragma unroll
        for (int a = 0; a < 7; a++) v += __builtin_bit_cast(f4, lds128(ra + (a * 5 + n) * 1024));
        u32 o0 = pk2(blo(hres[2 * n]) + rbf(rbf(v[0]) + blo(bb[n].x)), bhi(hres[2 * n]) + rbf(rbf(v[1]) + bhi(bb[n].x)));
        u32 o1 = pk2(blo(hres[2 * n + 1]) + rbf(rbf(v[2]) + blo(bb[n].y)), bhi(hres[2 * n + 1]) + rbf(rbf(v[3]) + bhi(bb[n].y)));
        __builtin_amdgcn_raw_buffer_store_b64(u2{o0, o1}, rOut, lr * 1280 + g * 8 + n * 32, (p * 64 + mt * 16) * 1280 + j * 160, 0);
      }
    }
  }
  waitv<0>();
  STAMP(16);
  if (b == 77 && w == 0) {
    u32 d = __builtin_amdgcn_readlane(tsv, PROBE) - __builtin_amdgcn_readlane(tsv, 0);
    u64 t0 = __builtin_amdgcn_s_memrealtime();
    while (__builtin_amdgcn_s_memrealtime() - t0 < 20ull * d) __builtin_amdgcn_s_sleep(2);
  }
}

extern "C" void kernel_launch(void* const* d_in, const int* in_sizes, int n_in, void* d_out, int out_size, void* d_ws, size_t ws_size,
                              hipStream_t stream) {
  fused_block<<<256, 512, 0, stream>>>((const u16*)d_in[0], (const u16*)d_in[1], (const u16*)d_in[2], (const u16*)d_in[4], (const u16*)d_in[5],
                                       (const u16*)d_in[6], (const u16*)d_in[7], (u16*)d_out, (char*)d_ws);
}
```

```cpp
#include <hip/hip_runtime.h>
#include <stdint.h>
#pragma clang diagnostic ignored "-Wint-to-pointer-cast"

typedef unsigned short u16;
typedef unsigned u32;
typedef unsigned long long u64;
typedef float f2 __attribute__((ext_vector_type(2)));
typedef float f4 __attribute__((ext_vector_type(4)));
typedef unsigned u2 __attribute__((ext_vector_type(2)));
typedef unsigned u4 __attribute__((ext_vector_type(4)));
typedef short s4 __attribute__((ext_vector_type(4)));
typedef __bf16 bf2 __attribute__((ext_vector_type(2)));
typedef __bf16 bf8 __attribute__((ext_vector_type(8)));
typedef __amdgpu_buffer_rsrc_t rsrc_t;
#define LAS __attribute__((address_space(3)))
#define GAS __attribute__((address_space(1)))
#define DI __device__ __forceinline__

template <int I> struct IC { static constexpr int v = I; constexpr operator int() const { return I; } };
template <int B, int E, class F> DI void sfor(F&& f) {
  if constexpr (B < E) { f(IC<B>{}); sfor<B + 1, E>(f); }
}

constexpr u32 WS_FRT = 4096, WS_FKV = 8192, WS_FO = 16384, WS_FH = 24576, WS_FHID = 32768;
constexpr u32 WS_KVP = 65536;
constexpr u32 WS_OB = 1u << 20;
constexpr u32 WS_HB = WS_OB + 32 * 81920;
constexpr u32 WS_ST = WS_HB + 32 * 81920;
constexpr u32 WS_CS1 = WS_ST + 2048 * 64;
constexpr u32 WS_WOT = WS_CS1 + 32768;
constexpr u32 WS_W1T = WS_WOT + 40 * 20480;
constexpr u32 WS_W2T = WS_W1T + 108 * 20480;
constexpr u32 WS_HID = WS_W2T + 8 * 54 * 5120;
constexpr u32 WS_END = WS_HID + 32 * 4 * 54 * 1024;
constexpr u32 WS_PROF = WS_END;
constexpr u32 OOB = 0x7FFFFF00u;
constexpr int SC1 = 16;
#define MAP 1
#define ROT 2
#define HA 10
#define HB2 20
#define CW 3
#define CG 32

constexpr u32 L_X = 0, L_RING = 81920;
constexpr u32 L_WO = 0, L_IMG = 147456, L_W1A = 110592, L_HR = 147456, L_CB = 160768, L_W1B = 0, L_RED = 0;

DI f4 mfma(u4 a, u4 b, f4 c) {
  return __builtin_amdgcn_mfma_f32_16x16x32_bf16(__builtin_bit_cast(bf8, a), __builtin_bit_cast(bf8, b), c, 0, 0, 0);
}
DI void mm(f4& c, u4 a, u4 b) { asm volatile("v_mfma_f32_16x16x32_bf16 %0, %1, %2, %0" : "+v"(c) : "v"(a), "v"(b) : "memory"); }
DI void mm0(f4& c, u4 a, u4 b) { asm volatile("v_mfma_f32_16x16x32_bf16 %0, %1, %2, 0" : "=&v"(c) : "v"(a), "v"(b) : "memory"); }
#define MM_DONE() asm volatile("s_nop 7\n\ts_nop 1" ::: "memory")
DI void pin(f4& c) { asm volatile("" : "+v"(c)); }
DI void zero(f4& c) { c = f4{0, 0, 0, 0}; asm volatile("" : "+v"(c)); }
DI void undef(u4& d) { asm volatile("" : "=v"(d)); }
DI u32 uni(u32 v) { v = __builtin_amdgcn_readfirstlane(v); asm volatile("" : "+s"(v)); return v; }
DI u32 opaque(u32 v) { asm volatile("" : "+v"(v)); return v; }

DI u32 pk2(float a, float b) { f2 v = {a, b}; return __builtin_bit_cast(u32, __builtin_convertvector(v, bf2)); }
DI u32 pk2(f2 v) { return __builtin_bit_cast(u32, __builtin_convertvector(v, bf2)); }
DI float rbf(float a) { return __builtin_bit_cast(float, pk2(a, a) << 16); }
DI float blo(u32 v) { return __builtin_bit_cast(float, v << 16); }
DI float bhi(u32 v) { return __builtin_bit_cast(float, v & 0xFFFF0000u); }
DI f2 unp(u32 v) { return f2{blo(v), bhi(v)}; }
DI float asf(u32 v) { return __builtin_bit_cast(float, v); }
DI u32 asu(float v) { return __builtin_bit_cast(u32, v); }

template <int OFF> DI void dma(rsrc_t r, u32 voff, u32 soff, u32 lds) {
  asm volatile("s_mov_b32 m0, %0\n\ts_nop 0\n\tbuffer_load_dwordx4 %1, %2, %3 offen offset:%4 lds" ::"s"(lds), "v"(voff), "s"(r), "s"(soff), "n"(OFF) : "memory");
}
template <int OFF> DI void dma_c(rsrc_t r, u32 voff, u32 soff) {
  asm volatile("buffer_load_dwordx4 %0, %1, %2 offen offset:%3 lds" ::"v"(voff), "s"(r), "s"(soff), "n"(OFF) : "memory");
}
DI void dma4_sc1(rsrc_t r, u32 voff, u32 soff, u32 lds) {
  asm volatile("s_mov_b32 m0, %0\n\ts_nop 0\n\tbuffer_load_dwordx4 %1, %2, %3 offen sc1 lds\n\tbuffer_load_dwordx4 %1, %2, %3 offen offset:1024 sc1 lds\n\t"
               "buffer_load_dwordx4 %1, %2, %3 offen offset:2048 sc1 lds\n\tbuffer_load_dwordx4 %1, %2, %3 offen offset:3072 sc1 lds" ::"s"(lds), "v"(voff), "s"(r), "s"(soff) : "memory");
}
DI void dma2_sc1(rsrc_t r, u32 voff, u32 soff, u32 lds) {
  asm volatile("s_mov_b32 m0, %0\n\ts_nop 0\n\tbuffer_load_dwordx4 %1, %2, %3 offen sc1 lds\n\tbuffer_load_dwordx4 %1, %2, %3 offen offset:1024 sc1 lds" ::"s"(lds), "v"(voff), "s"(r), "s"(soff) : "memory");
}
DI void dma1_sc1(rsrc_t r, u32 voff, u32 soff, u32 lds) {
  asm volatile("s_mov_b32 m0, %0\n\ts_nop 0\n\tbuffer_load_dwordx4 %1, %2, %3 offen sc1 lds" ::"s"(lds), "v"(voff), "s"(r), "s"(soff) : "memory");
}
template <int OFF> DI void ldv(u4& d, rsrc_t r, u32 voff, u32 soff) {
  asm volatile("buffer_load_dwordx4 %0, %1, %2, %3 offen offset:%4 sc1" : "=v"(d) : "v"(voff), "s"(r), "s"(soff), "n"(OFF) : "memory");
}
template <int OFF> DI void ldv_rw(u4& d, rsrc_t r, u32 voff, u32 soff) {
  asm volatile("buffer_load_dwordx4 %0, %1, %2, %3 offen offset:%4 sc1" : "+v"(d) : "v"(voff), "s"(r), "s"(soff), "n"(OFF) : "memory");
}
template <int OFF> DI void ldp(u4& d, rsrc_t r, u32 voff, u32 soff) {
  asm volatile("buffer_load_dwordx4 %0, %1, %2, %3 offen offset:%4" : "=v"(d) : "v"(voff), "s"(r), "s"(soff), "n"(OFF) : "memory");
}
template <int OFF> DI void ldp2(u2& d, rsrc_t r, u32 voff, u32 soff) {
  asm volatile("buffer_load_dwordx2 %0, %1, %2, %3 offen offset:%4" : "=v"(d) : "v"(voff), "s"(r), "s"(soff), "n"(OFF) : "memory");
}
template <int N> DI void waitv() { asm volatile("s_waitcnt vmcnt(%0)" ::"n"(N) : "memory"); }
template <class T> DI void landed(T& d) { asm volatile("" : "+v"(d)); }
#define BAR() do { asm volatile("" ::: "memory"); __builtin_amdgcn_s_waitcnt(0xC07F); asm volatile("s_barrier" ::: "memory"); } while (0)
DI u4 lds128(u32 a) { return *(LAS u4*)(a); }
DI u2 lds64(u32 a) { return *(LAS u2*)(a); }
DI void sts128(u32 a, u4 v) { *(LAS u4*)(a) = v; }
DI void sts64(u32 a, u2 v) { *(LAS u2*)(a) = v; }
DI void sts32(u32 a, float v) { *(LAS float*)(a) = v; }
DI u4 ldstr(u32 a0, u32 a1) {
  u2 x = __builtin_bit_cast(u2, __builtin_amdgcn_ds_read_tr16_b64_v4i16((LAS s4*)(a0)));
  u2 y = __builtin_bit_cast(u2, __builtin_amdgcn_ds_read_tr16_b64_v4i16((LAS s4*)(a1)));
  return u4{x.x, x.y, y.x, y.y};
}
DI u4 ldstr(u32 a) { return ldstr(a, a + 2560); }
DI float xsum16_32(float v) {
  auto r = __builtin_amdgcn_permlane16_swap(asu(v), asu(v), false, false);
  v = asf((u32)r[0]) + asf((u32)r[1]);
  auto q = __builtin_amdgcn_permlane32_swap(asu(v), asu(v), false, false);
  return asf((u32)q[0]) + asf((u32)q[1]);
}
DI float xsum8(float v) {
  v += asf(__builtin_amdgcn_update_dpp(0, asu(v), 0xB1, 0xf, 0xf, true));
  v += asf(__builtin_amdgcn_update_dpp(0, asu(v), 0x4E, 0xf, 0xf, true));
  return v + asf(__builtin_amdgcn_update_dpp(0, asu(v), 0x141, 0xf, 0xf, true));
}
DI void poll(const char* ws, u32 off, u64 tag) {
  const GAS u64* f = (const GAS u64*)(ws + off);
#pragma nounroll
  for (u32 it = 0; it < (1u << 18); ++it) {
    u64 v = __hip_atomic_load(f, __ATOMIC_RELAXED, __HIP_MEMORY_SCOPE_AGENT);
    if (__all(v == tag)) break;
    __builtin_amdgcn_s_sleep(2);
  }
  __builtin_amdgcn_fence(__ATOMIC_ACQUIRE, "wavefront");
  asm volatile("" ::: "memory");
}
DI void setflag(char* ws, u32 off, u64 tag, u32 lane) {
  if (lane == 0) __hip_atomic_store((GAS u64*)(ws + off), tag, __ATOMIC_RELAXED, __HIP_MEMORY_SCOPE_AGENT);
  asm volatile("" ::: "memory");
}
template <bool PHI> DI u32 phi2(float a0, float a1, float r0, float r1) {
  const f2 a = {a0, a1}, r = {r0, r1};
  if constexpr (!PHI) return pk2(a * r);
  const f2 t = a * r + 1e-6f, u = a * (r * 1.4426950408889634f);
  return pk2(fmaxf(t[0], 1e-6f) + __builtin_amdgcn_fmed3f(__builtin_amdgcn_exp2f(u[0]), 0.f, 1.f),
             fmaxf(t[1], 1e-6f) + __builtin_amdgcn_fmed3f(__builtin_amdgcn_exp2f(u[1]), 0.f, 1.f));
}
DI float dot2c(u32 a, u32 b, float c) { return __builtin_amdgcn_fdot2_f32_bf16(__builtin_bit_cast(bf2, a), __builtin_bit_cast(bf2, b), c, false); }
DI u4 center8(u4 x, u32 lo, u32 hi, float nm) {
  u4 r; float t0, t1, t2, t3, t4, t5, t6, t7;
  asm("v_dot2_f32_bf16 %4, %12, %16, %18\n\tv_dot2_f32_bf16 %5, %12, %17, %18\n\tv_dot2_f32_bf16 %6, %13, %16, %18\n\tv_dot2_f32_bf16 %7, %13, %17, %18\n\t"
      "v_dot2_f32_bf16 %8, %14, %16, %18\n\tv_dot2_f32_bf16 %9, %14, %17, %18\n\tv_dot2_f32_bf16 %10, %15, %16, %18\n\tv_dot2_f32_bf16 %11, %15, %17, %18\n\t"
      "v_cvt_pk_bf16_f32 %0, %4, %5\n\tv_cvt_pk_bf16_f32 %1, %6, %7\n\tv_cvt_pk_bf16_f32 %2, %8, %9\n\tv_cvt_pk_bf16_f32 %3, %10, %11"
      : "=v"(r.x), "=v"(r.y), "=v"(r.z), "=v"(r.w), "=&v"(t0), "=&v"(t1), "=&v"(t2), "=&v"(t3), "=&v"(t4), "=&v"(t5), "=&v"(t6), "=&v"(t7)
      : "v"(x.x), "v"(x.y), "v"(x.z), "v"(x.w), "v"(lo), "v"(hi), "v"(nm));
  return r;
}
template <int N> DI void waitl() { asm volatile("" ::: "memory"); __builtin_amdgcn_s_waitcnt(0xC07F | (N << 8)); asm volatile("" ::: "memory"); }
DI void st128(u4 v, rsrc_t r, u32 voff, u32 soff) {
  __builtin_amdgcn_raw_buffer_store_b128(v, r, voff, soff, SC1);
  asm volatile("s_nop 1" ::"v"(v) : "memory");
}
DI void st64(u2 v, rsrc_t r, u32 voff, u32 soff) { __builtin_amdgcn_raw_buffer_store_b64(v, r, voff, soff, SC1); }
DI void publish20(const u32* v, rsrc_t r, u32 l16, u32 so, u32 j) {
  if ((j & 1) == 0) {
    st128(u4{v[0], v[1], v[2], v[3]}, r, l16, so);
    st128(u4{v[4], v[5], v[6], v[7]}, r, l16, so + 1024);
    st64(u2{v[8], v[9]}, r, l16 >> 1, so + 2048);
  } else {
    st64(u2{v[0], v[1]}, r, l16 >> 1, so + 2560);
    st128(u4{v[2], v[3], v[4], v[5]}, r, l16, so + 3072);
    st128(u4{v[6], v[7], v[8], v[9]}, r, l16, so + 4096);
  }
}
template <int OFF> DI void ldv2(u2& d, rsrc_t r, u32 voff, u32 soff) {
  asm volatile("buffer_load_dwordx2 %0, %1, %2, %3 offen offset:%4 sc1" : "=v"(d) : "v"(voff), "s"(r), "s"(soff), "n"(OFF) : "memory");
}
DI void mmh(f4& c, u4 a, u2 b0, u2 b1) { mm(c, a, u4{b0.x, b0.y, b1.x, b1.y}); }
template <int V, int L> DI void waitvl() {
  asm volatile("" ::: "memory"); __builtin_amdgcn_s_waitcnt(0x0070 | (V & 15) | ((V >> 4) << 14) | (L << 8)); asm volatile("" ::: "memory");
}

#define STAMP(i)

__global__ __launch_bounds__(512, 2) void fused_block(const u16* __restrict__ x, const u16* __restrict__ wqkv, const u16* __restrict__ wo,
                                                      const u16* __restrict__ w1, const u16* __restrict__ b1, const u16* __restrict__ w2,
                                                      const u16* __restrict__ b2, u16* __restrict__ out, char* __restrict__ ws) {
  __shared__ __attribute__((aligned(1024))) char smem[163840];
  asm volatile("" ::"v"((u32)(uintptr_t)(LAS char*)smem));
  const u32 tid = threadIdx.x, lane = tid & 63, lr = lane & 15, g = lane >> 4;
  const u32 w = __builtin_amdgcn_readfirstlane(tid >> 6), mt = w & 3, nh = w >> 2;
  const bool helper = nh != 0;
  const u32 b = blockIdx.x, xg = b & 7, rr = b >> 3, p = 8 * (xg >> 1) + (rr >> 2), j = 4 * (xg & 1) + (rr & 3);
  asm volatile("" ::"s"(x), "s"(wqkv), "s"(wo), "s"(w1), "s"(b1), "s"(w2), "s"(b2), "s"(out), "s"(ws));
  STAMP(0);
  const rsrc_t rW = __builtin_amdgcn_make_buffer_rsrc((void*)ws, 0, WS_END, 0x00020000);
  const u32 l16 = lane * 16, l8 = lane * 8;
  u2 ep;
  u64 tag;
#define LD_EPOCH() asm volatile("buffer_load_dwordx2 %0, off, %1, 0 sc1" : "=v"(ep) : "s"(rW) : "memory")
#define GET_TAG() do { u32 lo_, hi_; asm volatile("v_readfirstlane_b32 %0, %2\n\tv_readfirstlane_b32 %1, %3\n\ts_nop 3" : "=s"(lo_), "=s"(hi_) : "v"(ep.x), "v"(ep.y) : "memory"); \
    tag = (((u64)hi_ << 32) | lo_) + 1; } while (0)
  u32 rot = xg + 3 * (p & 3), rotq = xg + (p & 3);
  rot = rot >= 10 ? rot - 10 : rot; rotq = rotq >= 10 ? rotq - 10 : (rotq >= 5 ? rotq - 5 : rotq);
  const u32 P = p >> 1, c = ((p & 1) << 3) | j, rho = c >> 1, odd = c & 1;
  const u32 t0 = rho < 6 ? 14 * rho + 8 * odd : 12 * rho + 12 + 6 * odd, tl = rho < 6 ? 14 * rho + 6 + odd : t0;
  const u32 s0 = w < 6 ? 7 * w : 6 * w + 6;
  const u32 vlast = w >= 6 ? OOB : l16, soW2 = WS_W2T + (j * 5 * 54 + s0) * 1024;
  u4 w2f[35];
  auto ldw2 = [&](auto I) {
    constexpr int s = I / 5, n = I % 5;
    ldv<(s & 3) * 1024>(w2f[I], rW, s == 6 ? vlast : l16, soW2 + (n * 54 + (s & ~3)) * 1024);
  };
  constexpr u32 L_RS = L_RING + 4096;

  if (!helper) {
    const rsrc_t rX = __builtin_amdgcn_make_buffer_rsrc((void*)x, 0, 2048 * 1280, 0x00020000);
    float rsd[2];
    const u32 ONE2 = opaque(0x3F803F80u);
    {
      u4 xr[2][10];
      const u32 vx = (lane >> 3) * 1280 + (lane & 7) * 16, sx = (p * 64 + 16 * mt) * 1280;
      sfor<0, 10>([&](auto I) { ldp<I * 128>(xr[0][I], rX, vx, sx); });
      sfor<0, 10>([&](auto I) { ldp<I * 128>(xr[1][I], rX, vx, sx + 8 * 1280); });
      LD_EPOCH();
      const u32 wa = L_X + mt * 2048 + (lane >> 3) * 128 + (((lane & 7) ^ (lane >> 3)) << 4);
      const u32 lo = opaque(0x00003F80u), hi = opaque(0x3F800000u);
      float dm[2];
      sfor<0, 2>([&](auto G) {
        waitv<(G == 0 ? 11 : 1)>();
        STAMP(20 + G);
        float s0 = 0.f, s1 = 0.f;
#pragma unroll
        for (int i = 0; i < 10; i++) {
          landed(xr[G][i]);
          s0 = dot2c(xr[G][i].x, ONE2, s0); s1 = dot2c(xr[G][i].y, ONE2, s1); s0 = dot2c(xr[G][i].z, ONE2, s0); s1 = dot2c(xr[G][i].w, ONE2, s1);
        }
        const float m = xsum8(s0 + s1) * (1.f / 640.f), nm = -rbf(m);
        dm[G] = m + nm;
#pragma unroll
        for (int i = 0; i < 10; i++) { xr[G][i] = center8(xr[G][i], lo, hi, nm); sts128(wa + G * 1024 + i * 8192, xr[G][i]); }
      });
      sfor<0, 2>([&](auto G) {
        float s0 = 0.f, s1 = 0.f;
#pragma unroll
        for (int i = 0; i < 10; i++) {
          const u4 v = xr[G][i];
          s0 = dot2c(v.x, v.x, s0); s1 = dot2c(v.y, v.y, s1); s0 = dot2c(v.z, v.z, s0); s1 = dot2c(v.w, v.w, s1);
        }
        const float var = fmaxf(xsum8(s0 + s1) * (1.f / 640.f) - dm[G] * dm[G], 0.f);
        rsd[G] = __builtin_amdgcn_rcpf(rbf(__builtin_amdgcn_sqrtf(rbf(rbf(var) + 1e-5f))));
        landed(rsd[G]);
      });
    }
    STAMP(22);
    waitv<0>();
    GET_TAG();
    const u32 xa0 = L_X + (lr >> 3) * 1024 + (lr & 7) * 128 + ((g ^ (lr & 7)) << 4);
    const u32 tr0 = L_RING + (4 * g + (lr >> 2)) * 160 + (lr & 3) * 8;
    const u32 mh = mt & 1, nq = mt >> 1;
    float qrs;
    {
      f4 acc[2][5];
#pragma unroll
      for (int i = 0; i < 5; i++) { zero(acc[0][i]); zero(acc[1][i]); }
      const u32 bA = tr0 + nq * 96, bC = tr0 + 64 + nq * 5120;
      const u32 xs0 = xa0 + mh * 4096;
      auto half = [&](u4* cur, u4* nxt, u32 xa, u32 a, u32 c, auto KS, auto LD) {
        constexpr int o = KS * 10240;
        mm(acc[0][0], cur[5], cur[0]);
        if constexpr (LD) nxt[5] = lds128(xa);
        mm(acc[1][0], cur[6], cur[0]);
        if constexpr (LD) nxt[6] = lds128(xa + 2048);
        mm(acc[0][1], cur[5], cur[1]);
        if constexpr (LD) nxt[0] = ldstr(a + o);
        mm(acc[1][1], cur[6], cur[1]);
        if constexpr (LD) nxt[1] = ldstr(a + o + 32);
        mm(acc[0][2], cur[5], cur[2]);
        if constexpr (LD) nxt[2] = ldstr(c + o);
        mm(acc[1][2], cur[6], cur[2]);
        if constexpr (LD) nxt[3] = ldstr(a + o + 5120);
        mm(acc[0][3], cur[5], cur[3]);
        if constexpr (LD) nxt[4] = ldstr(a + o + 5152);
        mm(acc[1][3], cur[6], cur[3]);
        mm(acc[0][4], cur[5], cur[4]); mm(acc[1][4], cur[6], cur[4]);
      };
      u4 fa[7], fb[7];
      u32 st = rot, xa = xs0 + st * 8192, a = bA, cc = bC;
      BAR();
      STAMP(1);
      fa[5] = lds128(xa); fa[6] = lds128(xa + 2048); fa[0] = ldstr(a); fa[1] = ldstr(a + 32); fa[2] = ldstr(cc);
      fa[3] = ldstr(a + 5120); fa[4] = ldstr(a + 5152);
      u32 lkv[6];
#pragma unroll
      for (int q = 0; q < 2; q++) {
        const u32 dq = 4 * q + g - mt, e0 = lr - 4 * mt, e1 = e0 + 16, sb = L_RING + mt * 5120;
        lkv[3 * q] = opaque(sb + ((dq < 5 && e0 < 20) ? e0 * 40 + dq * 8 : 1024));
        lkv[3 * q + 1] = opaque(sb + ((dq < 5 && e1 < 20) ? e1 * 40 + dq * 8 : 1024));
        lkv[3 * q + 2] = opaque(sb + ((dq < 5 && lr == 0) ? 800 + dq * 8 : (dq == 0 && lr == 1) ? 840 : 1024));
      }
#pragma nounroll
      for (u32 i = 1; i < 10; i++) {
        waitl<0>();
        half(fa, fb, xa ^ 64, a, cc, IC<1>{}, IC<1>{});
        st = st + 1 >= 10 ? 0 : st + 1;
        xa = opaque(xs0 + st * 8192); a = opaque(bA + (i & 3) * 20480); cc = opaque(bC + (i & 3) * 20480);
        BAR();
        half(fb, fa, xa, a, cc, IC<0>{}, IC<1>{});
      }
      if ((lane & 7) == 0) { sts32(L_RS + mt * 64 + (lane >> 3) * 4, rsd[0]); sts32(L_RS + mt * 64 + 32 + (lane >> 3) * 4, rsd[1]); }
      waitl<0>();
      half(fa, fb, xa ^ 64, a, cc, IC<1>{}, IC<1>{});
      BAR();
      const f4 r0 = __builtin_bit_cast(f4, lds128(L_RS + mh * 128 + g * 16)), r1 = __builtin_bit_cast(f4, lds128(L_RS + mh * 128 + 64 + g * 16));
      qrs = *(LAS float*)(L_RS + mt * 64 + lr * 4);
      half(fb, fa, 0, 0, 0, IC<0>{}, IC<0>{});
      MM_DONE();
      STAMP(2);
      const u32 EX = L_RING + 20480;
      const u32 ew = EX + mh * 10240 + nq * 3072 + l16;
      auto put = [&](auto N, auto PHI, u32 a) {
        constexpr int n = N;
        pin(acc[0][n]); pin(acc[1][n]);
        sts128(a, u4{phi2<PHI>(acc[0][n][0], acc[0][n][1], r0[0], r0[1]), phi2<PHI>(acc[0][n][2], acc[0][n][3], r0[2], r0[3]),
                     phi2<PHI>(acc[1][n][0], acc[1][n][1], r1[0], r1[1]), phi2<PHI>(acc[1][n][2], acc[1][n][3], r1[2], r1[3])});
      };
      put(IC<0>{}, IC<1>{}, ew); put(IC<1>{}, IC<1>{}, ew + 1024); put(IC<3>{}, IC<0>{}, ew + 5120); put(IC<4>{}, IC<0>{}, ew + 6144);
      if (nq == 0) put(IC<2>{}, IC<1>{}, ew + 2048); else put(IC<2>{}, IC<0>{}, ew + 4096);
      BAR();
      const u32 ex = EX + mt * 1024 + l16;
      u4 kf[2][2], vf[2][2];
#pragma unroll
      for (int m = 0; m < 2; m++) {
        kf[m][0] = lds128(ex + m * 10240); vf[m][0] = lds128(ex + m * 10240 + 5120);
        vf[m][1] = lds128(ex + m * 10240 + 6144); kf[m][1] = lds128(ex + m * 10240 + 1024);
      }
      const u32 o0 = lr == 0 ? ONE2 : 0u;
      const u4 ONE0 = {o0, o0, o0, o0};
#pragma unroll
      for (int q = 0; q < 2; q++) {
        f4 z4 = {0, 0, 0, 0};
        f4 kv0 = mfma(kf[1][q], vf[1][0], mfma(kf[0][q], vf[0][0], z4)), kv1 = mfma(kf[1][q], vf[1][1], mfma(kf[0][q], vf[0][1], z4));
        f4 zz = mfma(kf[1][q], ONE0, mfma(kf[0][q], ONE0, z4));
        sts64(lkv[3 * q], u2{pk2(kv0[0], kv0[1]), pk2(kv0[2], kv0[3])});
        sts64(lkv[3 * q + 1], u2{pk2(kv1[0], kv1[1]), pk2(kv1[2], kv1[3])});
        sts64(lkv[3 * q + 2], u2{pk2(zz[0], zz[1]), pk2(zz[2], zz[3])});
      }
      asm volatile("" ::: "memory");
      st128(lds128(L_RING + mt * 5120 + l16), rW, lane < 56 ? l16 : OOB, WS_KVP + ((j * 32 + p) * 4 + mt) * 896);
    }
    BAR();
    waitv<0>();
    setflag(ws, WS_FKV + ((j * 4 + mt) * 32 + p) * 8, tag, lane);
    STAMP(3);
    u2 xres[5];
    sfor<0, 5>([&](auto N) { ldp2<N * 32>(xres[N], rX, lr * 1280 + g * 8, (p * 64 + mt * 16) * 1280 + j * 160); });
    u32 pq[5][2];
    {
      f4 qa[5];
#pragma unroll
      for (int n = 0; n < 5; n++) zero(qa[n]);
      u32 qs = rotq, xb = xa0 + mt * 2048 + qs * 16384, a0 = tr0 + 2 * 20480;
      u4 fa[6], fb[6];
      fa[5] = lds128(xb); fa[0] = ldstr(a0); fa[1] = ldstr(a0 + 32);
      asm volatile("" ::: "memory");
      fa[2] = ldstr(a0 + 64); fa[3] = ldstr(a0 + 96); fa[4] = ldstr(a0 + 128);
      auto step = [&](u4* cur, u4* nxt, u32 xa, u32 a) {
        waitl<6>();
        mm(qa[0], cur[0], cur[5]);
        nxt[5] = lds128(xa); nxt[0] = ldstr(a);
        mm(qa[1], cur[1], cur[5]);
        nxt[1] = ldstr(a + 32);
        waitl<5>();
        mm(qa[2], cur[2], cur[5]);
        nxt[2] = ldstr(a + 64);
        mm(qa[3], cur[3], cur[5]);
        nxt[3] = ldstr(a + 96);
        mm(qa[4], cur[4], cur[5]);
        nxt[4] = ldstr(a + 128);
      };
#pragma nounroll
      for (u32 qi = 0; qi < 5; qi++) {
        const u32 xc = opaque(xb ^ 64);
        step(fa, fb, xc, a0 + 5120);
        step(fb, fa, xb + 8192, a0 + 10240);
        step(fa, fb, xc + 8192, a0 + 15360);
        qs = qs + 1 >= 5 ? 0 : qs + 1;
        xb = opaque(xa0 + mt * 2048 + qs * 16384); a0 = opaque(tr0 + ((0x02013u >> (4 * qi)) & 3) * 20480);
        if (qi < 2) BAR();
        step(fb, fa, xb, a0);
      }
      MM_DONE();
      STAMP(4);
#pragma unroll
      for (int n = 0; n < 5; n++) {
        pin(qa[n]);
        pq[n][0] = opaque(phi2<1>(qa[n][0], qa[n][1], qrs, qrs)); pq[n][1] = opaque(phi2<1>(qa[n][2], qa[n][3], qrs, qrs));
      }
    }
    STAMP(5);
    BAR();
    {
      const u32 o = mt * 12288, o2 = 49152 + (mt & 1) * 1024, sw = WS_WOT + j * 102400;
      dma4_sc1(rW, l16, sw + o, L_WO + o); dma4_sc1(rW, l16, sw + o + 4096, L_WO + o + 4096); dma4_sc1(rW, l16, sw + o + 8192, L_WO + o + 8192);
      dma1_sc1(rW, l16, sw + o2, L_WO + o2);
    }
    __builtin_amdgcn_s_setprio(2);
    STAMP(23);
    BAR();
    STAMP(6);
    {
      float o[20];
#pragma unroll
      for (int h = 0; h < 4; h++) {
        u4 bq = {pq[h][0], pq[h][1], pq[h + 1][0], pq[h + 1][1]};
        f4 z4 = {0, 0, 0, 0};
        f4 d0 = mfma(lds128(L_IMG + h * 2048 + l16), bq, z4);
        f4 d1 = mfma(lds128(L_IMG + h * 2048 + 1024 + l16), bq, z4);
        float rd = __builtin_amdgcn_rcpf(d1[1] + 1e-6f);
#pragma unroll
        for (int i = 0; i < 4; i++) o[5 * h + i] = d0[i] * rd;
        o[5 * h + 4] = d1[0] * rd;
      }
      u32 ov[10];
#pragma unroll
      for (int i = 0; i < 10; i++) ov[i] = pk2(o[2 * i], o[2 * i + 1]);
      publish20(ov, rW, l16, WS_OB + ((p * 4 + mt) * 20 + 5 * (j >> 1)) * 1024, j);
    }
    waitv<0>();
    setflag(ws, WS_FO + ((p * 4 + mt) * 8 + j) * 8, tag, lane);
    if (b == 0 && tid == 0) __hip_atomic_store((GAS u64*)ws, tag, __ATOMIC_RELAXED, __HIP_MEMORY_SCOPE_AGENT);
    BAR();
    STAMP(7);
    {
      u4 of[20], wf[5];
      u2 oh[4][2];
      const u32 so = WS_OB + (p * 4 + mt) * 20480;
      auto ldo = [&](auto S) {
        constexpr int s = S;
        if constexpr (s % 5 == 2) { ldv2<(s & 3) * 1024>(oh[s / 5][0], rW, l8, so + (s & ~3) * 1024); ldv2<(s & 3) * 1024 + 512>(oh[s / 5][1], rW, l8, so + (s & ~3) * 1024); }
        else ldv<(s & 3) * 1024>(of[s], rW, l16, so + (s & ~3) * 1024);
      };
      const u32 a0 = opaque(L_WO + l16), a1 = opaque(L_WO + 51200 + l16);
      poll(ws, WS_FO + ((p * 4 + mt) * 8 + (lane & 7)) * 8, tag);
      STAMP(8);
      sfor<0, 10>(ldo);
      BAR();
#pragma unroll
      for (int n = 0; n < 5; n++) wf[n] = lds128(a0 + n * 10240);
      f4 ha[5];
      sfor<0, 20>([&](auto S) {
        constexpr int s = S, e = s + 10 < 20 ? s + 10 : 20;
        constexpr int yo = (e - s - 1) + ((e + 2) / 5 - (s + 3) / 5);
        if constexpr (s == 9) BAR();
        waitvl<yo, 2>();
        sfor<0, 5>([&](auto N) {
          constexpr int n = N, t = s + 1;
          if constexpr (n == 3) waitl<(s < 19 ? 3 : 0)>();
          if constexpr (s == 0) mm0(ha[n], wf[n], of[s]);
          else if constexpr (s % 5 == 2) mmh(ha[n], wf[n], oh[s / 5][0], oh[s / 5][1]);
          else mm(ha[n], wf[n], of[s]);
          if constexpr (s < 19) wf[n] = lds128((t < 10 ? a0 : a1) + n * 10240 + (t % 10) * 1024);
        });
        if constexpr (s < 10) ldo(IC<s + 10>{});
      });
      MM_DONE();
      STAMP(9);
      u32 hres[10];
      float sum = 0.f, sq = 0.f;
#pragma unroll
      for (int n = 0; n < 5; n++) {
        pin(ha[n]);
        hres[2 * n] = pk2(unp(xres[n].x) + f2{ha[n][0], ha[n][1]});
        hres[2 * n + 1] = pk2(unp(xres[n].y) + f2{ha[n][2], ha[n][3]});
      }
      publish20(hres, rW, l16, WS_HB + ((p * 4 + mt) * 20 + 5 * (j >> 1)) * 1024, j);
#pragma unroll
      for (int i = 0; i < 10; i++) { sum = dot2c(hres[i], ONE2, sum); sq = dot2c(hres[i], hres[i], sq); }
      st64(u2{asu(xsum16_32(sum)), asu(xsum16_32(sq))}, rW, g == 0 ? l8 : OOB, WS_ST + ((p * 4 + mt) * 8 + j) * 128);
#pragma unroll
      for (int n = 0; n < 5; n++) sts64(L_HR + (mt * 5 + n) * 512 + l8, u2{hres[2 * n], hres[2 * n + 1]});
      waitv<0>();
      setflag(ws, WS_FH + ((p * 4 + mt) * 8 + j) * 8, tag, lane);
      BAR();
      STAMP(10);
    }
    {
      constexpr int HD = 6;
      const u32 pc = 2 * P + (mt >> 1), mc = 2 * (mt & 1);
      f4 ca[2][7];
      u4 hf[2][20], wf[7];
      u2 hh[2][4][2], stv[2][2];
      const u32 so = WS_HB + (pc * 4 + mc) * 20480;
      auto ldh = [&](auto S) {
        constexpr int s = S;
        sfor<0, 2>([&](auto M) {
          if constexpr (s % 5 == 2) {
            ldv2<(s & 3) * 1024>(hh[M][s / 5][0], rW, l8, so + M * 20480 + (s & ~3) * 1024);
            ldv2<(s & 3) * 1024 + 512>(hh[M][s / 5][1], rW, l8, so + M * 20480 + (s & ~3) * 1024);
          } else ldv<(s & 3) * 1024>(hf[M][s], rW, l16, so + M * 20480 + (s & ~3) * 1024);
        });
      };
      const u32 a0 = opaque(L_W1A + l16), a1 = opaque(L_W1B + l16), a2 = opaque(L_W1B + 35840 + l16), a3 = opaque(L_W1B + 71680 + l16);
#pragma unroll
      for (int n = 0; n < 7; n++) wf[n] = lds128(a0 + n * 5120);
      poll(ws, WS_FH + ((pc * 4 + mc) * 8 + lr) * 8, tag);
      STAMP(11);
      sfor<0, 2>([&](auto M) {
        ldv2<M * 1024>(stv[M][0], rW, g * 256 + lr * 8, WS_ST + (pc * 4 + mc) * 1024); ldv2<M * 1024 + 128>(stv[M][1], rW, g * 256 + lr * 8, WS_ST + (pc * 4 + mc) * 1024);
      });
      sfor<0, HD>(ldh);
      float sc[2], sm[2];
      sfor<0, 20>([&](auto S) {
        constexpr int s = S, e = s + HD < 20 ? s + HD : 20, NW = CW;
        constexpr int yo = 2 * ((e - s - 1) + ((e + 2) / 5 - (s + 3) / 5)) + (s > 20 - HD ? NW * (s - (20 - HD)) : 0);
        if constexpr (s == 9) BAR();
        waitvl<yo, 3>();
        if constexpr (s == 1) {
          sfor<0, 2>([&](auto M) {
            landed(stv[M][0]); landed(stv[M][1]);
            const float mean = xsum16_32(asf(stv[M][0].x) + asf(stv[M][1].x)) * (1.f / 640.f);
            const float var = fmaxf(xsum16_32(asf(stv[M][0].y) + asf(stv[M][1].y)) * (1.f / 640.f) - mean * mean, 0.f);
            const float m1 = rbf(mean), sd1 = rbf(__builtin_amdgcn_sqrtf(rbf(rbf(var) + 1e-5f))), r1 = __builtin_amdgcn_rcpf(sd1);
            const float m2 = rbf((mean - m1) * r1), sd2 = rbf(__builtin_amdgcn_sqrtf(rbf(rbf(var * r1 * r1) + 1e-5f)));
            sc[M] = r1 * __builtin_amdgcn_rcpf(sd2);
            sm[M] = sc[M] * (m1 + m2 * sd1);
            landed(sc[M]); landed(sm[M]);
          });
        }
        sfor<0, 7>([&](auto N) {
          constexpr int n = N, t = s + 1;
          if constexpr (n == 4) waitl<(s < 19 ? 4 : 0)>();
          sfor<0, 2>([&](auto M) {
            if constexpr (s == 0) mm0(ca[M][n], wf[n], hf[M][s]);
            else if constexpr (s % 5 == 2) mmh(ca[M][n], wf[n], hh[M][s / 5][0], hh[M][s / 5][1]);
            else mm(ca[M][n], wf[n], hf[M][s]);
          });
          if constexpr (s < 19) wf[n] = lds128((t < 5 ? a0 : t < 10 ? a1 : t < 15 ? a2 : a3) + n * 5120 + (t % 5) * 1024);
          if constexpr (n == 2 && s + HD < 20) ldh(IC<s + HD>{});
          if constexpr (s + HD >= 20 && n < NW) ldw2(IC<NW * (s - (20 - HD)) + n>{});
        });
      });
      MM_DONE();
      BAR();
      STAMP(12);
      const u32 sh = WS_HID + (pc * 4 + mc) * 54 * 1024;
      u32 hp[2][2][2];
      sfor<0, 7>([&](auto N) {
        constexpr int n = N;
        const f4 c4 = __builtin_bit_cast(f4, lds128(L_CB + n * 64 + g * 16)), b4 = __builtin_bit_cast(f4, lds128(L_CB + 512 + n * 64 + g * 16));
        sfor<0, 2>([&](auto M) {
          pin(ca[M][n]);
          constexpr int wi = CW * HD + 2 * n + M;
          if constexpr (wi < CG) ldw2(IC<wi>{});
#pragma unroll
          for (int i = 0; i < 2; i++) {
            const f2 v = f2{ca[M][n][2 * i], ca[M][n][2 * i + 1]} * sc[M] + (f2{b4[2 * i], b4[2 * i + 1]} - sm[M] * f2{c4[2 * i], c4[2 * i + 1]});
            const f2 u = v * (-2.302208198f - 0.1029432397f * v * v);
            const f2 d = f2{__builtin_amdgcn_exp2f(u[0]), __builtin_amdgcn_exp2f(u[1])} + 1.f;
            hp[M][n & 1][i] = pk2(v * f2{__builtin_amdgcn_rcpf(d[0]), __builtin_amdgcn_rcpf(d[1])});
          }
          if constexpr (n & 1) st128(u4{hp[M][0][0], hp[M][0][1], hp[M][1][0], hp[M][1][1]}, rW, l16, sh + M * 54 * 1024 + ((t0 >> 1) + n / 2) * 1024);
          if constexpr (n == 6) st64(u2{hp[M][0][0], hp[M][0][1]}, rW, rho < 6 ? l8 + odd * 512 : OOB, sh + M * 54 * 1024 + (tl >> 1) * 1024);
        });
      });
      STAMP(29);
      waitv<0>();
      setflag(ws, WS_FHID + ((pc * 2 + (mt & 1)) * 16 + c) * 8, tag, lane);
      __builtin_amdgcn_s_setprio(0);
      STAMP(13);
      sfor<CG, 35>(ldw2);
      poll(ws, WS_FHID + (p * 32 + (lane & 31)) * 8, tag);
    }
  } else {
    STAMP(20);
    LD_EPOCH();
    if (mt < 3) {
      const rsrc_t rQ = __builtin_amdgcn_make_buffer_rsrc((void*)((const char*)wqkv - 4096), 0, 640 * 3840 + 4096, 0x00020000);
      u32 voffW[7];
#pragma unroll
      for (int i = 0; i < 7; i++) {
        const u32 pi = i < 5 ? i : (i == 5 ? mt : (mt < 2 ? mt + 3 : 4u));
        const u32 f = 64 * pi + lane, sg = (f * 6554) >> 16, ch = f - sg * 10;
        voffW[i] = (((sg & 12) << 1) + ((sg >> 4) << 2) + (sg & 3)) * 3840 + ch * 16 + 4096 - (i < 4 ? i * 1024 : 0);
      }
      const u32 soW = (mt >> 1) * 32 * 3840 + ((mt & 1) + 1) * 1280 + j * 160, soW3 = 32 * 3840 + 2560 + j * 160;
      const u32 soQ = mt * 32 * 3840 + j * 160, soQ3 = 96 * 3840 + j * 160;
      const u32 l5 = 15360 + mt * 1024, l6 = 15360 + (mt < 2 ? mt + 3 : 4u) * 1024;
      u32 st = rot, qs = rotq;
      auto issueW = [&](u32 so, u32 so3, u32 slot) {
        const u32 ld = L_RING + slot * 20480;
        dma<0>(rQ, voffW[0], so, ld + mt * 5120); dma_c<1024>(rQ, voffW[1], so); dma_c<2048>(rQ, voffW[2], so); dma_c<3072>(rQ, voffW[3], so);
        dma<0>(rQ, voffW[4], so, ld + mt * 5120 + 4096); dma<0>(rQ, voffW[5], so3, ld + l5);
        if (mt < 2) dma<0>(rQ, voffW[6], so3, ld + l6);
      };
      auto issueKV = [&](u32 slot) { issueW(soW + st * 245760, soW3 + st * 245760, slot); st = st + 1 >= 10 ? 0 : st + 1; };
      auto issueQ = [&](u32 slot) { issueW(soQ + qs * 491520, soQ3 + qs * 491520, slot); qs = qs + 1 >= 5 ? 0 : qs + 1; };
#pragma nounroll
      for (u32 i = 0; i < 3; i++) issueKV(i);
      STAMP(21);
      waitv<12>();
      BAR();
      issueKV(3);
#pragma nounroll
      for (u32 i = 0; i < 6; i++) { waitv<12>(); asm volatile("s_barrier" ::: "memory"); issueKV(i & 3); }
      waitv<12>(); BAR();
      waitv<6>(); BAR();
      waitv<0>(); BAR();
      issueQ(2);
      BAR();
      issueQ(3);
      BAR();
      waitv<0>();
      BAR();
      issueQ(1); issueQ(0);
      BAR();
      issueQ(2);
      GET_TAG();
    } else {
      const bool m1 = b < 108, mo = b >= 216;
      const u32 v = m1 ? b : (mo ? b - 216 : b - 108), vq = m1 ? (v * 2428) >> 16 : (v * 6554) >> 16;
      const u32 sA = uni((m1 || mo) ? 5 * vq : v >> 1), cA = uni(m1 ? v - 27 * vq : (mo ? v - 10 * vq : 5 * (v & 1))), sQ = uni((m1 || mo) ? 1u : 0u);
      const u32 pitch = uni(m1 ? 3456 : 1280);
      const u64 src = m1 ? (u64)w1 : (mo ? (u64)wo : (u64)w2);
      const rsrc_t rS = __builtin_amdgcn_make_buffer_rsrc((void*)src, 0, mo ? 640 * 1280 : 640 * 3456, 0x00020000);
      const u32 vo = (lane >> 3) * pitch + (((lane & 7) ^ (((lane >> 4) & 3) << 1)) << 4);
      const u32 step = uni(sQ ? 32 * pitch : 128u);
      u32 so = 32 * sA * pitch + cA * 128;
#pragma nounroll
      for (u32 q = 0; q < 5; q++, so += step) {
#pragma unroll
        for (u32 i = 0; i < 4; i++) dma<0>(rS, vo, so + i * 8 * pitch, L_RING + 61440 + q * 4096 + i * 1024);
      }
      u4 ONES = {0x3F803F80u, 0x3F803F80u, 0x3F803F80u, 0x3F803F80u};
      landed(ONES);
      u32 ta[5][2];
#pragma unroll
      for (int q = 0; q < 5; q++)
#pragma unroll
        for (int t = 0; t < 2; t++) {
          const u32 i40 = 8 * q + 4 * t + (lr >> 2), hf = i40 >= 20 ? 1u : 0u, id = i40 - 20 * hf;
          const u32 rp = opaque(80 * hf + 15 * ((id * 13) >> 6) + 5 * g + id), row = mo ? rp : 32 * q + 16 * t + 4 * g + (lr >> 2);
          ta[q][t] = opaque(L_RING + 61440 + row * 128 + (lr & 1) * 8 + ((((lr >> 1) & 1) ^ (((row >> 1) & 3) << 1)) << 4));
        }
      const u32 dT = uni(m1 ? 20480u : (mo ? 10240u : 55296u)), dQ = uni(sQ ? 1024u : 4 * 55296u);
      const u32 dA = uni((m1 ? WS_W1T : (mo ? WS_WOT + (sA >= 10 ? 40960u : 0u) : WS_W2T)) + 4 * cA * dT + sA * 1024);
      u32 dJ[4];
#pragma unroll
      for (int n = 0; n < 4; n++) dJ[n] = uni(dA + n * dT + (mo ? (((4 * cA + n) * 52) >> 8) * 51200u : 0u));
      waitv<0>();
      GET_TAG();
      u4 f[5][4];
      f4 cs[4];
#pragma unroll
      for (int q = 0; q < 5; q++)
#pragma unroll
        for (int n = 0; n < 4; n++) f[q][n] = ldstr(ta[q][0] ^ (n << 5), ta[q][1] ^ (n << 5));
      STAMP(21);
      BAR();
#pragma unroll
      for (int q = 0; q < 5; q++) {
#pragma unroll
        for (int n = 0; n < 4; n++) {
          cs[n] = mfma(f[q][n], ONES, q == 0 ? f4{0, 0, 0, 0} : cs[n]);
          st128(f[q][n], rW, l16, dJ[n] + q * dQ);
        }
        BAR();
      }
#pragma unroll
      for (int n = 0; n < 4; n++) st128(__builtin_bit_cast(u4, cs[n]), rW, (m1 && lr == 0) ? g * 16 : OOB, WS_CS1 + vq * 6912 + (4 * cA + n) * 64);
      BAR();
      waitv<0>();
      setflag(ws, WS_FRT + b * 8, tag, lane);
      STAMP(22);
#pragma nounroll
      for (u32 i = 0; i < 7; i++) BAR();
    }
    STAMP(24);
    u4 fr[2];
    ldv<0>(fr[0], rW, lane * 32, WS_FRT); ldv<16>(fr[1], rW, lane * 32, WS_FRT);
    u4 sel[2];
#pragma unroll
    for (int q = 0; q < 2; q++) {
      const u32 k = lr + 16 * q - 8 * g, one = (k & 1) ? 0x3F800000u : 0x00003F80u;
      sel[q] = u4{k < 2 ? one : 0u, (k >> 1) == 1 ? one : 0u, (k >> 1) == 2 ? one : 0u, (k >> 1) == 3 ? one : 0u};
      landed(sel[q]);
    }
    u32 dst[2];
#pragma unroll
    for (int h = 0; h < 2; h++) {
      const u32 qd = 2 * (lr + 16 * (2 * h + (g >> 1))) + (g & 1), e = (qd * 205) >> 10, dq = qd - e * 5;
      const u32 gs = dq + mt, grp = (e * 13) >> 6, ii = e - grp * 5;
      const u32 base = L_IMG + mt * 2048 + (gs & 3) * 256 + (gs >> 2) * 8;
      dst[h] = opaque(e < 20 ? base + (ii < 4 ? (4 * grp + ii) * 16 : 1024 + 4 * grp * 16) : (e == 20 ? base + 1024 + 16 : L_CB + 2048));
    }
    const bool zrow = (dst[1] & 0x430) == 0x410 && dst[1] < L_CB;
    waitv<0>();
    BAR();
    {
      landed(fr[0]); landed(fr[1]);
      const u32 tl32 = (u32)tag, th32 = (u32)(tag >> 32);
      const u32 bad = ((fr[0].x ^ tl32) | (fr[0].z ^ tl32) | (fr[1].x ^ tl32) | (fr[1].z ^ tl32)) | ((fr[0].y ^ th32) | (fr[0].w ^ th32) | (fr[1].y ^ th32) | (fr[1].w ^ th32));
      if (__any(bad != 0)) {
#pragma nounroll
        for (u32 q = 0; q < 4; q++) poll(ws, WS_FRT + (64 * q + lane) * 8, tag);
      }
    }
    sts128(L_IMG + mt * 2048 + l16, u4{0, 0, 0, 0}); sts128(L_IMG + mt * 2048 + 1024 + l16, u4{0, 0, 0, 0});
    STAMP(4);
    poll(ws, WS_FKV + ((j * 4 + mt) * 32 + (lane & 31)) * 8, tag);
    STAMP(5);
    {
      const u32 so = WS_KVP + (j * 32 * 4 + mt) * 896;
      const u32 v0 = lane < 53 ? l16 : OOB;
      u4 a[32];
      sfor<0, 32>([&](auto Q) { ldv<(Q & 1) * 3584>(a[Q], rW, v0, so + (Q & ~1) * 3584); });
      BAR();
      f4 s0, s1, s2, s3;
      sfor<0, 16>([&](auto Q) {
        if constexpr ((Q & 1) == 0) waitv<28 - 2 * Q>();
        if constexpr (Q == 0) { mm0(s0, sel[0], a[0]); mm0(s1, sel[1], a[0]); mm0(s2, sel[0], a[1]); mm0(s3, sel[1], a[1]); }
        else { mm(s0, sel[0], a[2 * Q]); mm(s1, sel[1], a[2 * Q]); mm(s2, sel[0], a[2 * Q + 1]); mm(s3, sel[1], a[2 * Q + 1]); }
      });
      MM_DONE();
      pin(s0); pin(s1); pin(s2); pin(s3);
      s0 += s2; s1 += s3;
      const u2 v1 = {pk2(s1[0], s1[1]), pk2(s1[2], s1[3])};
      sts64(dst[0], u2{pk2(s0[0], s0[1]), pk2(s0[2], s0[3])});
      sts64(dst[1], v1);
      if (zrow) { sts64(dst[1] + 64, v1); sts64(dst[1] + 128, v1); sts64(dst[1] + 192, v1); }
      STAMP(6);
      BAR();
    }
    auto issueW1 = [&](u32 it, u32 n, u32 ld) {
      const u32 so = WS_W1T + (n < 6 ? t0 + n : tl) * 20480 + it * 5120;
      dma4_sc1(rW, l16, so, ld + n * 5120); dma1_sc1(rW, l16, so + 4096, ld + n * 5120 + 4096);
    };
    const u32 o = 51200 + mt * 12288, o2 = 51200 + 49152 + (mt & 1) * 1024, sw = WS_WOT + j * 102400;
    {
      f4 csum;
      u32 ci = lane + 64 * (mt & 1); ci = ci > 111 ? 111 : ci;
      const u32 n = ci >> 4, col = (n < 6 ? t0 + n : tl) * 16 + (ci & 15);
#pragma unroll
      for (int q = 0; q < 4; q++) csum[q] = asf(__builtin_amdgcn_raw_buffer_load_b32(rW, col * 4, WS_CS1 + q * 6912, SC1));
      const u32 bv = b1[col], b2v = ((const u32*)b2)[40 * j + (lane < 40 ? lane : 0)];
      sts32(L_CB + ci * 4, (csum[0] + csum[1]) + (csum[2] + csum[3]));
      sts32(L_CB + 512 + ci * 4, blo(bv));
      if (lane < 40) *(LAS u32*)(L_CB + 1024 + lane * 4) = b2v;
    }
    BAR();
    STAMP(9);
    dma4_sc1(rW, l16, sw + o, L_WO + o); dma4_sc1(rW, l16, sw + o + 4096, L_WO + o + 4096); dma4_sc1(rW, l16, sw + o + 8192, L_WO + o + 8192);
    dma1_sc1(rW, l16, sw + o2, L_WO + o2);
    issueW1(0, mt, L_W1A); issueW1(0, mt < 3 ? mt + 4 : 6, L_W1A);
    sfor<0, HA>(ldw2);
    STAMP(10);
    BAR();
    waitv<10 + HA>();
    STAMP(7);
    BAR();
    issueW1(1, mt, L_W1B); if (mt < 3) issueW1(1, mt + 4, L_W1B);
    sfor<HA, HB2>(ldw2);
    waitv<HB2 - HA>();
    STAMP(25);
    BAR();
    STAMP(8);
#pragma nounroll
    for (u32 u = mt; u < 14; u += 4) { const u32 it = u >= 7 ? 1u : 0u; issueW1(it + 2, u - 7 * it, L_W1B + 35840 + it * 35840); }
    sfor<HB2, 35>(ldw2);
    waitv<35 - HB2>();
    STAMP(28);
    BAR();
    STAMP(26);
    BAR();
    poll(ws, WS_FHID + (p * 32 + (lane & 31)) * 8, tag);
  }
  STAMP(14);
  {
    f4 da[4][5];
    u4 xf[7][4];
    u2 xh[4][2];
    const u32 so = WS_HID + (p * 4 * 54 + s0) * 1024;
    const u32 vh0 = w < 6 ? l8 : l16, vh1 = w < 6 ? l8 + 512 : l16 + 8;
    auto ldx = [&](auto S, auto A) {
      constexpr int s = S, a = A;
      if constexpr (s == 3) { ldv2<3072>(xh[a][0], rW, vh0, so + a * 54 * 1024); ldv2<3072>(xh[a][1], rW, vh1, so + a * 54 * 1024); }
      else ldv<(s & 3) * 1024>(xf[s][a], rW, s == 6 ? vlast : l16, so + (a * 54 + (s & ~3)) * 1024);
    };
    constexpr int XD = 7;
    sfor<0, XD>([&](auto U) { ldx(IC<U / 4>{}, IC<U % 4>{}); });
    sfor<0, 28>([&](auto U) {
      constexpr int u = U, s = u / 4, a = u % 4, e = u + XD + u / 2 < 28 ? u + XD + u / 2 : 28;
      constexpr int lo3 = u + 1 > 12 ? u + 1 : 12, hi3 = e < 16 ? e : 16;
      waitv<(e - u - 1) + (hi3 > lo3 ? hi3 - lo3 : 0)>();
      sfor<0, 5>([&](auto N) {
        if constexpr (s == 0) mm0(da[a][N], w2f[N], xf[s][a]);
        else if constexpr (s == 3) mmh(da[a][N], w2f[5 * s + N], xh[a][0], xh[a][1]);
        else mm(da[a][N], w2f[5 * s + N], xf[s][a]);
      });
      constexpr int e2 = u + 1 + XD + (u + 1) / 2 < 28 ? u + 1 + XD + (u + 1) / 2 : 28;
      sfor<e, e2>([&](auto V) { ldx(IC<V / 4>{}, IC<V % 4>{}); });
    });
    MM_DONE();
    STAMP(17);
#pragma unroll
    for (int a = 0; a < 4; a++)
#pragma unroll
      for (int n = 0; n < 5; n++) {
        pin(da[a][n]);
        sts64(L_RED + ((a * 5 + n) * 8 + w) * 512 + l8, u2{pk2(da[a][n][0], da[a][n][1]), pk2(da[a][n][2], da[a][n][3])});
      }
    BAR();
    STAMP(18);
    const rsrc_t rOut = __builtin_amdgcn_make_buffer_rsrc((void*)out, 0, 2048 * 1280, 0x00020000);
#pragma nounroll
    for (u32 n = 3 * nh; n < 3 + 2 * nh; n++) {
      const u32 ra = L_RED + (mt * 5 + n) * 4096 + l8;
      f2 lo = {0, 0}, hi = {0, 0};
#pragma unroll
      for (int q = 0; q < 8; q++) { u2 v = lds64(ra + q * 512); lo += unp(v.x); hi += unp(v.y); }
      const u2 bb = lds64(L_CB + 1024 + n * 32 + g * 8), hr = lds64(L_HR + (mt * 5 + n) * 512 + l8);
      const u32 o0 = pk2(unp(hr.x) + unp(pk2(unp(pk2(lo)) + unp(bb.x)))), o1 = pk2(unp(hr.y) + unp(pk2(unp(pk2(hi)) + unp(bb.y))));
      __builtin_amdgcn_raw_buffer_store_b64(u2{o0, o1}, rOut, lr * 1280 + g * 8, (p * 64 + mt * 16) * 1280 + j * 160 + n * 32, 0);
    }
  }
}

extern "C" void kernel_launch(void* const* d_in, const int* in_sizes, int n_in, void* d_out, int out_size, void* d_ws, size_t ws_size,
                              hipStream_t stream) {
  fused_block<<<256, 512, 0, stream>>>((const u16*)d_in[0], (const u16*)d_in[1], (const u16*)d_in[2], (const u16*)d_in[4], (const u16*)d_in[5],
                                       (const u16*)d_in[6], (const u16*)d_in[7], (u16*)d_out, (char*)d_ws);
}
```

```cpp
#include <hip/hip_runtime.h>
#include <stdint.h>
#define PROF
#define PROBE 0
#pragma clang diagnostic ignored "-Wint-to-pointer-cast"

typedef unsigned short u16;
typedef unsigned u32;
typedef unsigned long long u64;
typedef float f2 __attribute__((ext_vector_type(2)));
typedef float f4 __attribute__((ext_vector_type(4)));
typedef unsigned u2 __attribute__((ext_vector_type(2)));
typedef unsigned u4 __attribute__((ext_vector_type(4)));
typedef short s4 __attribute__((ext_vector_type(4)));
typedef __bf16 bf2 __attribute__((ext_vector_type(2)));
typedef __bf16 bf8 __attribute__((ext_vector_type(8)));
typedef __amdgpu_buffer_rsrc_t rsrc_t;
#define LAS __attribute__((address_space(3)))
#define GAS __attribute__((address_space(1)))
#define DI __device__ __forceinline__

template <int I> struct IC { static constexpr int v = I; constexpr operator int() const { return I; } };
template <int B, int E, class F> DI void sfor(F&& f) {
  if constexpr (B < E) { f(IC<B>{}); sfor<B + 1, E>(f); }
}

constexpr u32 WS_FRT = 4096, WS_FKV = 8192, WS_FO = 16384, WS_FH = 24576, WS_FHID = 32768;
constexpr u32 WS_KVP = 65536;
constexpr u32 WS_OB = 1u << 20;
constexpr u32 WS_HB = WS_OB + 32 * 81920;
constexpr u32 WS_ST = WS_HB + 32 * 81920;
constexpr u32 WS_CS1 = WS_ST + 2048 * 64;
constexpr u32 WS_WOT = WS_CS1 + 32768;
constexpr u32 WS_W1T = WS_WOT + 40 * 20480;
constexpr u32 WS_W2T = WS_W1T + 108 * 20480;
constexpr u32 WS_HID = WS_W2T + 8 * 54 * 5120;
constexpr u32 WS_END = WS_HID + 32 * 4 * 54 * 1024;
constexpr u32 WS_PROF = WS_END;
constexpr u32 OOB = 0x7FFFFF00u;
constexpr int SC1 = 16;
#define MAP 1
#define ROT 2
#define HA 10
#define HB2 20
#define CW 3
#define CG 32

constexpr u32 L_X = 0, L_RING = 81920;
constexpr u32 L_WO = 0, L_IMG = 147456, L_W1A = 110592, L_HR = 147456, L_CB = 160768, L_W1B = 0, L_RED = 0;

DI f4 mfma(u4 a, u4 b, f4 c) {
  return __builtin_amdgcn_mfma_f32_16x16x32_bf16(__builtin_bit_cast(bf8, a), __builtin_bit_cast(bf8, b), c, 0, 0, 0);
}
DI void mm(f4& c, u4 a, u4 b) { asm volatile("v_mfma_f32_16x16x32_bf16 %0, %1, %2, %0" : "+v"(c) : "v"(a), "v"(b) : "memory"); }
DI void mm0(f4& c, u4 a, u4 b) { asm volatile("v_mfma_f32_16x16x32_bf16 %0, %1, %2, 0" : "=&v"(c) : "v"(a), "v"(b) : "memory"); }
#define MM_DONE() asm volatile("s_nop 7\n\ts_nop 1" ::: "memory")
DI void pin(f4& c) { asm volatile("" : "+v"(c)); }
DI void zero(f4& c) { c = f4{0, 0, 0, 0}; asm volatile("" : "+v"(c)); }
DI void undef(u4& d) { asm volatile("" : "=v"(d)); }
DI u32 uni(u32 v) { v = __builtin_amdgcn_readfirstlane(v); asm volatile("" : "+s"(v)); return v; }
DI u32 opaque(u32 v) { asm volatile("" : "+v"(v)); return v; }

DI u32 pk2(float a, float b) { f2 v = {a, b}; return __builtin_bit_cast(u32, __builtin_convertvector(v, bf2)); }
DI u32 pk2(f2 v) { return __builtin_bit_cast(u32, __builtin_convertvector(v, bf2)); }
DI float rbf(float a) { return __builtin_bit_cast(float, pk2(a, a) << 16); }
DI float blo(u32 v) { return __builtin_bit_cast(float, v << 16); }
DI float bhi(u32 v) { return __builtin_bit_cast(float, v & 0xFFFF0000u); }
DI f2 unp(u32 v) { return f2{blo(v), bhi(v)}; }
DI float asf(u32 v) { return __builtin_bit_cast(float, v); }
DI u32 asu(float v) { return __builtin_bit_cast(u32, v); }

template <int OFF> DI void dma(rsrc_t r, u32 voff, u32 soff, u32 lds) {
  asm volatile("s_mov_b32 m0, %0\n\ts_nop 0\n\tbuffer_load_dwordx4 %1, %2, %3 offen offset:%4 lds" ::"s"(lds), "v"(voff), "s"(r), "s"(soff), "n"(OFF) : "memory");
}
template <int OFF> DI void dma_c(rsrc_t r, u32 voff, u32 soff) {
  asm volatile("buffer_load_dwordx4 %0, %1, %2 offen offset:%3 lds" ::"v"(voff), "s"(r), "s"(soff), "n"(OFF) : "memory");
}
DI void dma4_sc1(rsrc_t r, u32 voff, u32 soff, u32 lds) {
  asm volatile("s_mov_b32 m0, %0\n\ts_nop 0\n\tbuffer_load_dwordx4 %1, %2, %3 offen sc1 lds\n\tbuffer_load_dwordx4 %1, %2, %3 offen offset:1024 sc1 lds\n\t"
               "buffer_load_dwordx4 %1, %2, %3 offen offset:2048 sc1 lds\n\tbuffer_load_dwordx4 %1, %2, %3 offen offset:3072 sc1 lds" ::"s"(lds), "v"(voff), "s"(r), "s"(soff) : "memory");
}
DI void dma2_sc1(rsrc_t r, u32 voff, u32 soff, u32 lds) {
  asm volatile("s_mov_b32 m0, %0\n\ts_nop 0\n\tbuffer_load_dwordx4 %1, %2, %3 offen sc1 lds\n\tbuffer_load_dwordx4 %1, %2, %3 offen offset:1024 sc1 lds" ::"s"(lds), "v"(voff), "s"(r), "s"(soff) : "memory");
}
DI void dma1_sc1(rsrc_t r, u32 voff, u32 soff, u32 lds) {
  asm volatile("s_mov_b32 m0, %0\n\ts_nop 0\n\tbuffer_load_dwordx4 %1, %2, %3 offen sc1 lds" ::"s"(lds), "v"(voff), "s"(r), "s"(soff) : "memory");
}
template <int OFF> DI void ldv(u4& d, rsrc_t r, u32 voff, u32 soff) {
  asm volatile("buffer_load_dwordx4 %0, %1, %2, %3 offen offset:%4 sc1" : "=v"(d) : "v"(voff), "s"(r), "s"(soff), "n"(OFF) : "memory");
}
template <int OFF> DI void ldv_rw(u4& d, rsrc_t r, u32 voff, u32 soff) {
  asm volatile("buffer_load_dwordx4 %0, %1, %2, %3 offen offset:%4 sc1" : "+v"(d) : "v"(voff), "s"(r), "s"(soff), "n"(OFF) : "memory");
}
template <int OFF> DI void ldp(u4& d, rsrc_t r, u32 voff, u32 soff) {
  asm volatile("buffer_load_dwordx4 %0, %1, %2, %3 offen offset:%4" : "=v"(d) : "v"(voff), "s"(r), "s"(soff), "n"(OFF) : "memory");
}
template <int OFF> DI void ldp2(u2& d, rsrc_t r, u32 voff, u32 soff) {
  asm volatile("buffer_load_dwordx2 %0, %1, %2, %3 offen offset:%4" : "=v"(d) : "v"(voff), "s"(r), "s"(soff), "n"(OFF) : "memory");
}
template <int N> DI void waitv() { asm volatile("s_waitcnt vmcnt(%0)" ::"n"(N) : "memory"); }
template <class T> DI void landed(T& d) { asm volatile("" : "+v"(d)); }
#define BAR() do { asm volatile("" ::: "memory"); __builtin_amdgcn_s_waitcnt(0xC07F); asm volatile("s_barrier" ::: "memory"); } while (0)
DI u4 lds128(u32 a) { return *(LAS u4*)(a); }
DI u2 lds64(u32 a) { return *(LAS u2*)(a); }
DI void sts128(u32 a, u4 v) { *(LAS u4*)(a) = v; }
DI void sts64(u32 a, u2 v) { *(LAS u2*)(a) = v; }
DI void sts32(u32 a, float v) { *(LAS float*)(a) = v; }
DI u4 ldstr(u32 a0, u32 a1) {
  u2 x = __builtin_bit_cast(u2, __builtin_amdgcn_ds_read_tr16_b64_v4i16((LAS s4*)(a0)));
  u2 y = __builtin_bit_cast(u2, __builtin_amdgcn_ds_read_tr16_b64_v4i16((LAS s4*)(a1)));
  return u4{x.x, x.y, y.x, y.y};
}
DI u4 ldstr(u32 a) { return ldstr(a, a + 2560); }
DI float xsum16_32(float v) {
  auto r = __builtin_amdgcn_permlane16_swap(asu(v), asu(v), false, false);
  v = asf((u32)r[0]) + asf((u32)r[1]);
  auto q = __builtin_amdgcn_permlane32_swap(asu(v), asu(v), false, false);
  return asf((u32)q[0]) + asf((u32)q[1]);
}
DI float xsum8(float v) {
  v += asf(__builtin_amdgcn_update_dpp(0, asu(v), 0xB1, 0xf, 0xf, true));
  v += asf(__builtin_amdgcn_update_dpp(0, asu(v), 0x4E, 0xf, 0xf, true));
  return v + asf(__builtin_amdgcn_update_dpp(0, asu(v), 0x141, 0xf, 0xf, true));
}
DI void poll(const char* ws, u32 off, u64 tag) {
  const GAS u64* f = (const GAS u64*)(ws + off);
#pragma nounroll
  for (u32 it = 0; it < (1u << 18); ++it) {
    u64 v = __hip_atomic_load(f, __ATOMIC_RELAXED, __HIP_MEMORY_SCOPE_AGENT);
    if (__all(v == tag)) break;
    __builtin_amdgcn_s_sleep(2);
  }
  __builtin_amdgcn_fence(__ATOMIC_ACQUIRE, "wavefront");
  asm volatile("" ::: "memory");
}
DI void setflag(char* ws, u32 off, u64 tag, u32 lane) {
  if (lane == 0) __hip_atomic_store((GAS u64*)(ws + off), tag, __ATOMIC_RELAXED, __HIP_MEMORY_SCOPE_AGENT);
  asm volatile("" ::: "memory");
}
template <bool PHI> DI u32 phi2(float a0, float a1, float r0, float r1) {
  const f2 a = {a0, a1}, r = {r0, r1};
  if constexpr (!PHI) return pk2(a * r);
  const f2 t = a * r + 1e-6f, u = a * (r * 1.4426950408889634f);
  return pk2(fmaxf(t[0], 1e-6f) + __builtin_amdgcn_fmed3f(__builtin_amdgcn_exp2f(u[0]), 0.f, 1.f),
             fmaxf(t[1], 1e-6f) + __builtin_amdgcn_fmed3f(__builtin_amdgcn_exp2f(u[1]), 0.f, 1.f));
}
DI float dot2c(u32 a, u32 b, float c) { return __builtin_amdgcn_fdot2_f32_bf16(__builtin_bit_cast(bf2, a), __builtin_bit_cast(bf2, b), c, false); }
DI u4 center8(u4 x, u32 lo, u32 hi, float nm) {
  u4 r; float t0, t1, t2, t3, t4, t5, t6, t7;
  asm("v_dot2_f32_bf16 %4, %12, %16, %18\n\tv_dot2_f32_bf16 %5, %12, %17, %18\n\tv_dot2_f32_bf16 %6, %13, %16, %18\n\tv_dot2_f32_bf16 %7, %13, %17, %18\n\t"
      "v_dot2_f32_bf16 %8, %14, %16, %18\n\tv_dot2_f32_bf16 %9, %14, %17, %18\n\tv_dot2_f32_bf16 %10, %15, %16, %18\n\tv_dot2_f32_bf16 %11, %15, %17, %18\n\t"
      "v_cvt_pk_bf16_f32 %0, %4, %5\n\tv_cvt_pk_bf16_f32 %1, %6, %7\n\tv_cvt_pk_bf16_f32 %2, %8, %9\n\tv_cvt_pk_bf16_f32 %3, %10, %11"
      : "=v"(r.x), "=v"(r.y), "=v"(r.z), "=v"(r.w), "=&v"(t0), "=&v"(t1), "=&v"(t2), "=&v"(t3), "=&v"(t4), "=&v"(t5), "=&v"(t6), "=&v"(t7)
      : "v"(x.x), "v"(x.y), "v"(x.z), "v"(x.w), "v"(lo), "v"(hi), "v"(nm));
  return r;
}
template <int N> DI void waitl() { asm volatile("" ::: "memory"); __builtin_amdgcn_s_waitcnt(0xC07F | (N << 8)); asm volatile("" ::: "memory"); }
DI void st128(u4 v, rsrc_t r, u32 voff, u32 soff) {
  __builtin_amdgcn_raw_buffer_store_b128(v, r, voff, soff, SC1);
  asm volatile("s_nop 1" ::"v"(v) : "memory");
}
DI void st64(u2 v, rsrc_t r, u32 voff, u32 soff) { __builtin_amdgcn_raw_buffer_store_b64(v, r, voff, soff, SC1); }
DI void publish20(const u32* v, rsrc_t r, u32 l16, u32 so, u32 j) {
  if ((j & 1) == 0) {
    st128(u4{v[0], v[1], v[2], v[3]}, r, l16, so);
    st128(u4{v[4], v[5], v[6], v[7]}, r, l16, so + 1024);
    st64(u2{v[8], v[9]}, r, l16 >> 1, so + 2048);
  } else {
    st64(u2{v[0], v[1]}, r, l16 >> 1, so + 2560);
    st128(u4{v[2], v[3], v[4], v[5]}, r, l16, so + 3072);
    st128(u4{v[6], v[7], v[8], v[9]}, r, l16, so + 4096);
  }
}
template <int OFF> DI void ldv2(u2& d, rsrc_t r, u32 voff, u32 soff) {
  asm volatile("buffer_load_dwordx2 %0, %1, %2, %3 offen offset:%4 sc1" : "=v"(d) : "v"(voff), "s"(r), "s"(soff), "n"(OFF) : "memory");
}
DI void mmh(f4& c, u4 a, u2 b0, u2 b1) { mm(c, a, u4{b0.x, b0.y, b1.x, b1.y}); }
template <int V, int L> DI void waitvl() {
  asm volatile("" ::: "memory"); __builtin_amdgcn_s_waitcnt(0x0070 | (V & 15) | ((V >> 4) << 14) | (L << 8)); asm volatile("" ::: "memory");
}

#define PROFW 3
#define TIMEOP "s_memrealtime"
#define STAMP(i) do { u64 t_; asm volatile(TIMEOP " %0\n\ts_waitcnt lgkmcnt(0)" : "=s"(t_) :: "memory"); asm volatile("s_nop 0\n\tv_writelane_b32 %0, %1, %2" : "+v"(tsv) : "s"((u32)t_), "n"(i)); } while (0)

__global__ __launch_bounds__(512, 2) void fused_block(const u16* __restrict__ x, const u16* __restrict__ wqkv, const u16* __restrict__ wo,
                                                      const u16* __restrict__ w1, const u16* __restrict__ b1, const u16* __restrict__ w2,
                                                      const u16* __restrict__ b2, u16* __restrict__ out, char* __restrict__ ws) {
  __shared__ __attribute__((aligned(1024))) char smem[163840];
  asm volatile("" ::"v"((u32)(uintptr_t)(LAS char*)smem));
  u32 tsv = 0;
  const u32 tid = threadIdx.x, lane = tid & 63, lr = lane & 15, g = lane >> 4;
  const u32 w = __builtin_amdgcn_readfirstlane(tid >> 6), mt = w & 3, nh = w >> 2;
  const bool helper = nh != 0;
  const u32 b = blockIdx.x, xg = b & 7, rr = b >> 3, p = 8 * (xg >> 1) + (rr >> 2), j = 4 * (xg & 1) + (rr & 3);
  asm volatile("" ::"s"(x), "s"(wqkv), "s"(wo), "s"(w1), "s"(b1), "s"(w2), "s"(b2), "s"(out), "s"(ws));
  STAMP(0);
  const rsrc_t rW = __builtin_amdgcn_make_buffer_rsrc((void*)ws, 0, WS_END, 0x00020000);
  const u32 l16 = lane * 16, l8 = lane * 8;
  u2 ep;
  u64 tag;
#define LD_EPOCH() asm volatile("buffer_load_dwordx2 %0, off, %1, 0 sc1" : "=v"(ep) : "s"(rW) : "memory")
#define GET_TAG() do { u32 lo_, hi_; asm volatile("v_readfirstlane_b32 %0, %2\n\tv_readfirstlane_b32 %1, %3\n\ts_nop 3" : "=s"(lo_), "=s"(hi_) : "v"(ep.x), "v"(ep.y) : "memory"); \
    tag = (((u64)hi_ << 32) | lo_) + 1; } while (0)
  u32 rot = xg + 3 * (p & 3), rotq = xg + (p & 3);
  rot = rot >= 10 ? rot - 10 : rot; rotq = rotq >= 10 ? rotq - 10 : (rotq >= 5 ? rotq - 5 : rotq);
  const u32 P = p >> 1, c = ((p & 1) << 3) | j, rho = c >> 1, odd = c & 1;
  const u32 t0 = rho < 6 ? 14 * rho + 8 * odd : 12 * rho + 12 + 6 * odd, tl = rho < 6 ? 14 * rho + 6 + odd : t0;
  const u32 s0 = w < 6 ? 7 * w : 6 * w + 6;
  const u32 vlast = w >= 6 ? OOB : l16, soW2 = WS_W2T + (j * 5 * 54 + s0) * 1024;
  u4 w2f[35];
  auto ldw2 = [&](auto I) {
    constexpr int s = I / 5, n = I % 5;
    ldv<(s & 3) * 1024>(w2f[I], rW, s == 6 ? vlast : l16, soW2 + (n * 54 + (s & ~3)) * 1024);
  };
  constexpr u32 L_RS = L_RING + 4096;

  if (!helper) {
    const rsrc_t rX = __builtin_amdgcn_make_buffer_rsrc((void*)x, 0, 2048 * 1280, 0x00020000);
    float rsd[2];
    const u32 ONE2 = opaque(0x3F803F80u);
    {
      u4 xr[2][10];
      const u32 vx = (lane >> 3) * 1280 + (lane & 7) * 16, sx = (p * 64 + 16 * mt) * 1280;
      sfor<0, 10>([&](auto I) { ldp<I * 128>(xr[0][I], rX, vx, sx); });
      sfor<0, 10>([&](auto I) { ldp<I * 128>(xr[1][I], rX, vx, sx + 8 * 1280); });
      LD_EPOCH();
      const u32 wa = L_X + mt * 2048 + (lane >> 3) * 128 + (((lane & 7) ^ (lane >> 3)) << 4);
      const u32 lo = opaque(0x00003F80u), hi = opaque(0x3F800000u);
      float dm[2];
      sfor<0, 2>([&](auto G) {
        waitv<(G == 0 ? 11 : 1)>();
        STAMP(20 + G);
        float s0 = 0.f, s1 = 0.f;
#pragma unroll
        for (int i = 0; i < 10; i++) {
          landed(xr[G][i]);
          s0 = dot2c(xr[G][i].x, ONE2, s0); s1 = dot2c(xr[G][i].y, ONE2, s1); s0 = dot2c(xr[G][i].z, ONE2, s0); s1 = dot2c(xr[G][i].w, ONE2, s1);
        }
        const float m = xsum8(s0 + s1) * (1.f / 640.f), nm = -rbf(m);
        dm[G] = m + nm;
#pragma unroll
        for (int i = 0; i < 10; i++) { xr[G][i] = center8(xr[G][i], lo, hi, nm); sts128(wa + G * 1024 + i * 8192, xr[G][i]); }
      });
      sfor<0, 2>([&](auto G) {
        float s0 = 0.f, s1 = 0.f;
#pragma unroll
        for (int i = 0; i < 10; i++) {
          const u4 v = xr[G][i];
          s0 = dot2c(v.x, v.x, s0); s1 = dot2c(v.y, v.y, s1); s0 = dot2c(v.z, v.z, s0); s1 = dot2c(v.w, v.w, s1);
        }
        const float var = fmaxf(xsum8(s0 + s1) * (1.f / 640.f) - dm[G] * dm[G], 0.f);
        rsd[G] = __builtin_amdgcn_rcpf(rbf(__builtin_amdgcn_sqrtf(rbf(rbf(var) + 1e-5f))));
        landed(rsd[G]);
      });
    }
    STAMP(22);
    waitv<0>();
    GET_TAG();
    const u32 xa0 = L_X + (lr >> 3) * 1024 + (lr & 7) * 128 + ((g ^ (lr & 7)) << 4);
    const u32 tr0 = L_RING + (4 * g + (lr >> 2)) * 160 + (lr & 3) * 8;
    const u32 mh = mt & 1, nq = mt >> 1;
    float qrs;
    {
      f4 acc[2][5];
#pragma unroll
      for (int i = 0; i < 5; i++) { zero(acc[0][i]); zero(acc[1][i]); }
      const u32 bA = tr0 + nq * 96, bC = tr0 + 64 + nq * 5120;
      const u32 xs0 = xa0 + mh * 4096;
      auto half = [&](u4* cur, u4* nxt, u32 xa, u32 a, u32 c, auto KS, auto LD) {
        constexpr int o = KS * 10240;
        mm(acc[0][0], cur[5], cur[0]);
        if constexpr (LD) nxt[5] = lds128(xa);
        mm(acc[1][0], cur[6], cur[0]);
        if constexpr (LD) nxt[6] = lds128(xa + 2048);
        mm(acc[0][1], cur[5], cur[1]);
        if constexpr (LD) nxt[0] = ldstr(a + o);
        mm(acc[1][1], cur[6], cur[1]);
        if constexpr (LD) nxt[1] = ldstr(a + o + 32);
        mm(acc[0][2], cur[5], cur[2]);
        if constexpr (LD) nxt[2] = ldstr(c + o);
        mm(acc[1][2], cur[6], cur[2]);
        if constexpr (LD) nxt[3] = ldstr(a + o + 5120);
        mm(acc[0][3], cur[5], cur[3]);
        if constexpr (LD) nxt[4] = ldstr(a + o + 5152);
        mm(acc[1][3], cur[6], cur[3]);
        mm(acc[0][4], cur[5], cur[4]); mm(acc[1][4], cur[6], cur[4]);
      };
      u4 fa[7], fb[7];
      u32 st = rot, xa = xs0 + st * 8192, a = bA, cc = bC;
      BAR();
      STAMP(1);
      fa[5] = lds128(xa); fa[6] = lds128(xa + 2048); fa[0] = ldstr(a); fa[1] = ldstr(a + 32); fa[2] = ldstr(cc);
      fa[3] = ldstr(a + 5120); fa[4] = ldstr(a + 5152);
      u32 lkv[6];
#pragma unroll
      for (int q = 0; q < 2; q++) {
        const u32 dq = 4 * q + g - mt, e0 = lr - 4 * mt, e1 = e0 + 16, sb = L_RING + mt * 5120;
        lkv[3 * q] = opaque(sb + ((dq < 5 && e0 < 20) ? e0 * 40 + dq * 8 : 1024));
        lkv[3 * q + 1] = opaque(sb + ((dq < 5 && e1 < 20) ? e1 * 40 + dq * 8 : 1024));
        lkv[3 * q + 2] = opaque(sb + ((dq < 5 && lr == 0) ? 800 + dq * 8 : (dq == 0 && lr == 1) ? 840 : 1024));
      }
#pragma nounroll
      for (u32 i = 1; i < 10; i++) {
        waitl<0>();
        half(fa, fb, xa ^ 64, a, cc, IC<1>{}, IC<1>{});
        st = st + 1 >= 10 ? 0 : st + 1;
        xa = opaque(xs0 + st * 8192); a = opaque(bA + (i & 3) * 20480); cc = opaque(bC + (i & 3) * 20480);
        BAR();
        half(fb, fa, xa, a, cc, IC<0>{}, IC<1>{});
      }
      if ((lane & 7) == 0) { sts32(L_RS + mt * 64 + (lane >> 3) * 4, rsd[0]); sts32(L_RS + mt * 64 + 32 + (lane >> 3) * 4, rsd[1]); }
      waitl<0>();
      half(fa, fb, xa ^ 64, a, cc, IC<1>{}, IC<1>{});
      BAR();
      const f4 r0 = __builtin_bit_cast(f4, lds128(L_RS + mh * 128 + g * 16)), r1 = __builtin_bit_cast(f4, lds128(L_RS + mh * 128 + 64 + g * 16));
      qrs = *(LAS float*)(L_RS + mt * 64 + lr * 4);
      half(fb, fa, 0, 0, 0, IC<0>{}, IC<0>{});
      MM_DONE();
      STAMP(2);
      const u32 EX = L_RING + 20480;
      const u32 ew = EX + mh * 10240 + nq * 3072 + l16;
      auto put = [&](auto N, auto PHI, u32 a) {
        constexpr int n = N;
        pin(acc[0][n]); pin(acc[1][n]);
        sts128(a, u4{phi2<PHI>(acc[0][n][0], acc[0][n][1], r0[0], r0[1]), phi2<PHI>(acc[0][n][2], acc[0][n][3], r0[2], r0[3]),
                     phi2<PHI>(acc[1][n][0], acc[1][n][1], r1[0], r1[1]), phi2<PHI>(acc[1][n][2], acc[1][n][3], r1[2], r1[3])});
      };
      put(IC<0>{}, IC<1>{}, ew); put(IC<1>{}, IC<1>{}, ew + 1024); put(IC<3>{}, IC<0>{}, ew + 5120); put(IC<4>{}, IC<0>{}, ew + 6144);
      if (nq == 0) put(IC<2>{}, IC<1>{}, ew + 2048); else put(IC<2>{}, IC<0>{}, ew + 4096);
      BAR();
      const u32 ex = EX + mt * 1024 + l16;
      u4 kf[2][2], vf[2][2];
#pragma unroll
      for (int m = 0; m < 2; m++) {
        kf[m][0] = lds128(ex + m * 10240); vf[m][0] = lds128(ex + m * 10240 + 5120);
        vf[m][1] = lds128(ex + m * 10240 + 6144); kf[m][1] = lds128(ex + m * 10240 + 1024);
      }
      const u32 o0 = lr == 0 ? ONE2 : 0u;
      const u4 ONE0 = {o0, o0, o0, o0};
#pragma unroll
      for (int q = 0; q < 2; q++) {
        f4 z4 = {0, 0, 0, 0};
        f4 kv0 = mfma(kf[1][q], vf[1][0], mfma(kf[0][q], vf[0][0], z4)), kv1 = mfma(kf[1][q], vf[1][1], mfma(kf[0][q], vf[0][1], z4));
        f4 zz = mfma(kf[1][q], ONE0, mfma(kf[0][q], ONE0, z4));
        sts64(lkv[3 * q], u2{pk2(kv0[0], kv0[1]), pk2(kv0[2], kv0[3])});
        sts64(lkv[3 * q + 1], u2{pk2(kv1[0], kv1[1]), pk2(kv1[2], kv1[3])});
        sts64(lkv[3 * q + 2], u2{pk2(zz[0], zz[1]), pk2(zz[2], zz[3])});
      }
      asm volatile("" ::: "memory");
      st128(lds128(L_RING + mt * 5120 + l16), rW, lane < 56 ? l16 : OOB, WS_KVP + ((j * 32 + p) * 4 + mt) * 896);
    }
    BAR();
    waitv<0>();
    setflag(ws, WS_FKV + ((j * 4 + mt) * 32 + p) * 8, tag, lane);
    STAMP(3);
    u2 xres[5];
    sfor<0, 5>([&](auto N) { ldp2<N * 32>(xres[N], rX, lr * 1280 + g * 8, (p * 64 + mt * 16) * 1280 + j * 160); });
    u32 pq[5][2];
    {
      f4 qa[5];
#pragma unroll
      for (int n = 0; n < 5; n++) zero(qa[n]);
      u32 qs = rotq, xb = xa0 + mt * 2048 + qs * 16384, a0 = tr0 + 2 * 20480;
      u4 fa[6], fb[6];
      fa[5] = lds128(xb); fa[0] = ldstr(a0); fa[1] = ldstr(a0 + 32);
      asm volatile("" ::: "memory");
      fa[2] = ldstr(a0 + 64); fa[3] = ldstr(a0 + 96); fa[4] = ldstr(a0 + 128);
      auto step = [&](u4* cur, u4* nxt, u32 xa, u32 a) {
        waitl<6>();
        mm(qa[0], cur[0], cur[5]);
        nxt[5] = lds128(xa); nxt[0] = ldstr(a);
        mm(qa[1], cur[1], cur[5]);
        nxt[1] = ldstr(a + 32);
        waitl<5>();
        mm(qa[2], cur[2], cur[5]);
        nxt[2] = ldstr(a + 64);
        mm(qa[3], cur[3], cur[5]);
        nxt[3] = ldstr(a + 96);
        mm(qa[4], cur[4], cur[5]);
        nxt[4] = ldstr(a + 128);
      };
#pragma nounroll
      for (u32 qi = 0; qi < 5; qi++) {
        const u32 xc = opaque(xb ^ 64);
        step(fa, fb, xc, a0 + 5120);
        step(fb, fa, xb + 8192, a0 + 10240);
        step(fa, fb, xc + 8192, a0 + 15360);
        qs = qs + 1 >= 5 ? 0 : qs + 1;
        xb = opaque(xa0 + mt * 2048 + qs * 16384); a0 = opaque(tr0 + ((0x02013u >> (4 * qi)) & 3) * 20480);
        if (qi < 2) BAR();
        step(fb, fa, xb, a0);
      }
      MM_DONE();
      STAMP(4);
#pragma unroll
      for (int n = 0; n < 5; n++) {
        pin(qa[n]);
        pq[n][0] = opaque(phi2<1>(qa[n][0], qa[n][1], qrs, qrs)); pq[n][1] = opaque(phi2<1>(qa[n][2], qa[n][3], qrs, qrs));
      }
    }
    STAMP(5);
    BAR();
    {
      const u32 o = mt * 12288, o2 = 49152 + (mt & 1) * 1024, sw = WS_WOT + j * 102400;
      dma4_sc1(rW, l16, sw + o, L_WO + o); dma4_sc1(rW, l16, sw + o + 4096, L_WO + o + 4096); dma4_sc1(rW, l16, sw + o + 8192, L_WO + o + 8192);
      dma1_sc1(rW, l16, sw + o2, L_WO + o2);
    }
    __builtin_amdgcn_s_setprio(2);
    STAMP(23);
    BAR();
    STAMP(6);
    {
      float o[20];
#pragma unroll
      for (int h = 0; h < 4; h++) {
        u4 bq = {pq[h][0], pq[h][1], pq[h + 1][0], pq[h + 1][1]};
        f4 z4 = {0, 0, 0, 0};
        f4 d0 = mfma(lds128(L_IMG + h * 2048 + l16), bq, z4);
        f4 d1 = mfma(lds128(L_IMG + h * 2048 + 1024 + l16), bq, z4);
        float rd = __builtin_amdgcn_rcpf(d1[1] + 1e-6f);
#pragma unroll
        for (int i = 0; i < 4; i++) o[5 * h + i] = d0[i] * rd;
        o[5 * h + 4] = d1[0] * rd;
      }
      u32 ov[10];
#pragma unroll
      for (int i = 0; i < 10; i++) ov[i] = pk2(o[2 * i], o[2 * i + 1]);
      publish20(ov, rW, l16, WS_OB + ((p * 4 + mt) * 20 + 5 * (j >> 1)) * 1024, j);
    }
    waitv<0>();
    setflag(ws, WS_FO + ((p * 4 + mt) * 8 + j) * 8, tag, lane);
    if (b == 0 && tid == 0) __hip_atomic_store((GAS u64*)ws, tag, __ATOMIC_RELAXED, __HIP_MEMORY_SCOPE_AGENT);
    BAR();
    STAMP(7);
    {
      u4 of[20], wf[5];
      u2 oh[4][2];
      const u32 so = WS_OB + (p * 4 + mt) * 20480;
      auto ldo = [&](auto S) {
        constexpr int s = S;
        if constexpr (s % 5 == 2) { ldv2<(s & 3) * 1024>(oh[s / 5][0], rW, l8, so + (s & ~3) * 1024); ldv2<(s & 3) * 1024 + 512>(oh[s / 5][1], rW, l8, so + (s & ~3) * 1024); }
        else ldv<(s & 3) * 1024>(of[s], rW, l16, so + (s & ~3) * 1024);
      };
      const u32 a0 = opaque(L_WO + l16), a1 = opaque(L_WO + 51200 + l16);
      poll(ws, WS_FO + ((p * 4 + mt) * 8 + (lane & 7)) * 8, tag);
      STAMP(8);
      sfor<0, 10>(ldo);
      BAR();
#pragma unroll
      for (int n = 0; n < 5; n++) wf[n] = lds128(a0 + n * 10240);
      f4 ha[5];
      sfor<0, 20>([&](auto S) {
        constexpr int s = S, e = s + 10 < 20 ? s + 10 : 20;
        constexpr int yo = (e - s - 1) + ((e + 2) / 5 - (s + 3) / 5);
        if constexpr (s == 9) BAR();
        waitvl<yo, 2>();
        sfor<0, 5>([&](auto N) {
          constexpr int n = N, t = s + 1;
          if constexpr (n == 3) waitl<(s < 19 ? 3 : 0)>();
          if constexpr (s == 0) mm0(ha[n], wf[n], of[s]);
          else if constexpr (s % 5 == 2) mmh(ha[n], wf[n], oh[s / 5][0], oh[s / 5][1]);
          else mm(ha[n], wf[n], of[s]);
          if constexpr (s < 19) wf[n] = lds128((t < 10 ? a0 : a1) + n * 10240 + (t % 10) * 1024);
        });
        if constexpr (s < 10) ldo(IC<s + 10>{});
      });
      MM_DONE();
      STAMP(9);
      u32 hres[10];
      float sum = 0.f, sq = 0.f;
#pragma unroll
      for (int n = 0; n < 5; n++) {
        pin(ha[n]);
        hres[2 * n] = pk2(unp(xres[n].x) + f2{ha[n][0], ha[n][1]});
        hres[2 * n + 1] = pk2(unp(xres[n].y) + f2{ha[n][2], ha[n][3]});
      }
      publish20(hres, rW, l16, WS_HB + ((p * 4 + mt) * 20 + 5 * (j >> 1)) * 1024, j);
#pragma unroll
      for (int i = 0; i < 10; i++) { sum = dot2c(hres[i], ONE2, sum); sq = dot2c(hres[i], hres[i], sq); }
      st64(u2{asu(xsum16_32(sum)), asu(xsum16_32(sq))}, rW, g == 0 ? l8 : OOB, WS_ST + ((p * 4 + mt) * 8 + j) * 128);
#pragma unroll
      for (int n = 0; n < 5; n++) sts64(L_HR + (mt * 5 + n) * 512 + l8, u2{hres[2 * n], hres[2 * n + 1]});
      waitv<0>();
      setflag(ws, WS_FH + ((p * 4 + mt) * 8 + j) * 8, tag, lane);
      BAR();
      STAMP(10);
    }
    {
      constexpr int HD = 6;
      const u32 pc = 2 * P + (mt >> 1), mc = 2 * (mt & 1);
      f4 ca[2][7];
      u4 hf[2][20], wf[7];
      u2 hh[2][4][2], stv[2][2];
      const u32 so = WS_HB + (pc * 4 + mc) * 20480;
      auto ldh = [&](auto S) {
        constexpr int s = S;
        sfor<0, 2>([&](auto M) {
          if constexpr (s % 5 == 2) {
            ldv2<(s & 3) * 1024>(hh[M][s / 5][0], rW, l8, so + M * 20480 + (s & ~3) * 1024);
            ldv2<(s & 3) * 1024 + 512>(hh[M][s / 5][1], rW, l8, so + M * 20480 + (s & ~3) * 1024);
          } else ldv<(s & 3) * 1024>(hf[M][s], rW, l16, so + M * 20480 + (s & ~3) * 1024);
        });
      };
      const u32 a0 = opaque(L_W1A + l16), a1 = opaque(L_W1B + l16), a2 = opaque(L_W1B + 35840 + l16), a3 = opaque(L_W1B + 71680 + l16);
#pragma unroll
      for (int n = 0; n < 7; n++) wf[n] = lds128(a0 + n * 5120);
      poll(ws, WS_FH + ((pc * 4 + mc) * 8 + lr) * 8, tag);
      STAMP(11);
      sfor<0, 2>([&](auto M) {
        ldv2<M * 1024>(stv[M][0], rW, g * 256 + lr * 8, WS_ST + (pc * 4 + mc) * 1024); ldv2<M * 1024 + 128>(stv[M][1], rW, g * 256 + lr * 8, WS_ST + (pc * 4 + mc) * 1024);
      });
      sfor<0, HD>(ldh);
      float sc[2], sm[2];
      sfor<0, 20>([&](auto S) {
        constexpr int s = S, e = s + HD < 20 ? s + HD : 20, NW = CW;
        constexpr int yo = 2 * ((e - s - 1) + ((e + 2) / 5 - (s + 3) / 5)) + (s > 20 - HD ? NW * (s - (20 - HD)) : 0);
        if constexpr (s == 9) BAR();
        waitvl<yo, 3>();
        if constexpr (s == 1) {
          sfor<0, 2>([&](auto M) {
            landed(stv[M][0]); landed(stv[M][1]);
            const float mean = xsum16_32(asf(stv[M][0].x) + asf(stv[M][1].x)) * (1.f / 640.f);
            const float var = fmaxf(xsum16_32(asf(stv[M][0].y) + asf(stv[M][1].y)) * (1.f / 640.f) - mean * mean, 0.f);
            const float m1 = rbf(mean), sd1 = rbf(__builtin_amdgcn_sqrtf(rbf(rbf(var) + 1e-5f))), r1 = __builtin_amdgcn_rcpf(sd1);
            const float m2 = rbf((mean - m1) * r1), sd2 = rbf(__builtin_amdgcn_sqrtf(rbf(rbf(var * r1 * r1) + 1e-5f)));
            sc[M] = r1 * __builtin_amdgcn_rcpf(sd2);
            sm[M] = sc[M] * (m1 + m2 * sd1);
            landed(sc[M]); landed(sm[M]);
          });
        }
        sfor<0, 7>([&](auto N) {
          constexpr int n = N, t = s + 1;
          if constexpr (n == 4) waitl<(s < 19 ? 4 : 0)>();
          sfor<0, 2>([&](auto M) {
            if constexpr (s == 0) mm0(ca[M][n], wf[n], hf[M][s]);
            else if constexpr (s % 5 == 2) mmh(ca[M][n], wf[n], hh[M][s / 5][0], hh[M][s / 5][1]);
            else mm(ca[M][n], wf[n], hf[M][s]);
          });
          if constexpr (s < 19) wf[n] = lds128((t < 5 ? a0 : t < 10 ? a1 : t < 15 ? a2 : a3) + n * 5120 + (t % 5) * 1024);
          if constexpr (n == 2 && s + HD < 20) ldh(IC<s + HD>{});
          if constexpr (s + HD >= 20 && n < NW) ldw2(IC<NW * (s - (20 - HD)) + n>{});
        });
      });
      MM_DONE();
      BAR();
      STAMP(12);
      const u32 sh = WS_HID + (pc * 4 + mc) * 54 * 1024;
      u32 hp[2][2][2];
      sfor<0, 7>([&](auto N) {
        constexpr int n = N;
        const f4 c4 = __builtin_bit_cast(f4, lds128(L_CB + n * 64 + g * 16)), b4 = __builtin_bit_cast(f4, lds128(L_CB + 512 + n * 64 + g * 16));
        sfor<0, 2>([&](auto M) {
          pin(ca[M][n]);
          constexpr int wi = CW * HD + 2 * n + M;
          if constexpr (wi < CG) ldw2(IC<wi>{});
#pragma unroll
          for (int i = 0; i < 2; i++) {
            const f2 v = f2{ca[M][n][2 * i], ca[M][n][2 * i + 1]} * sc[M] + (f2{b4[2 * i], b4[2 * i + 1]} - sm[M] * f2{c4[2 * i], c4[2 * i + 1]});
            const f2 u = v * (-2.302208198f - 0.1029432397f * v * v);
            const f2 d = f2{__builtin_amdgcn_exp2f(u[0]), __builtin_amdgcn_exp2f(u[1])} + 1.f;
            hp[M][n & 1][i] = pk2(v * f2{__builtin_amdgcn_rcpf(d[0]), __builtin_amdgcn_rcpf(d[1])});
          }
          if constexpr (n & 1) st128(u4{hp[M][0][0], hp[M][0][1], hp[M][1][0], hp[M][1][1]}, rW, l16, sh + M * 54 * 1024 + ((t0 >> 1) + n / 2) * 1024);
          if constexpr (n == 6) st64(u2{hp[M][0][0], hp[M][0][1]}, rW, rho < 6 ? l8 + odd * 512 : OOB, sh + M * 54 * 1024 + (tl >> 1) * 1024);
        });
      });
      STAMP(29);
      waitv<0>();
      setflag(ws, WS_FHID + ((pc * 2 + (mt & 1)) * 16 + c) * 8, tag, lane);
      __builtin_amdgcn_s_setprio(0);
      STAMP(13);
      sfor<CG, 35>(ldw2);
      poll(ws, WS_FHID + (p * 32 + (lane & 31)) * 8, tag);
    }
  } else {
    STAMP(20);
    LD_EPOCH();
    if (mt < 3) {
      const rsrc_t rQ = __builtin_amdgcn_make_buffer_rsrc((void*)((const char*)wqkv - 4096), 0, 640 * 3840 + 4096, 0x00020000);
      u32 voffW[7];
#pragma unroll
      for (int i = 0; i < 7; i++) {
        const u32 pi = i < 5 ? i : (i == 5 ? mt : (mt < 2 ? mt + 3 : 4u));
        const u32 f = 64 * pi + lane, sg = (f * 6554) >> 16, ch = f - sg * 10;
        voffW[i] = (((sg & 12) << 1) + ((sg >> 4) << 2) + (sg & 3)) * 3840 + ch * 16 + 4096 - (i < 4 ? i * 1024 : 0);
      }
      const u32 soW = (mt >> 1) * 32 * 3840 + ((mt & 1) + 1) * 1280 + j * 160, soW3 = 32 * 3840 + 2560 + j * 160;
      const u32 soQ = mt * 32 * 3840 + j * 160, soQ3 = 96 * 3840 + j * 160;
      const u32 l5 = 15360 + mt * 1024, l6 = 15360 + (mt < 2 ? mt + 3 : 4u) * 1024;
      u32 st = rot, qs = rotq;
      auto issueW = [&](u32 so, u32 so3, u32 slot) {
        const u32 ld = L_RING + slot * 20480;
        dma<0>(rQ, voffW[0], so, ld + mt * 5120); dma_c<1024>(rQ, voffW[1], so); dma_c<2048>(rQ, voffW[2], so); dma_c<3072>(rQ, voffW[3], so);
        dma<0>(rQ, voffW[4], so, ld + mt * 5120 + 4096); dma<0>(rQ, voffW[5], so3, ld + l5);
        if (mt < 2) dma<0>(rQ, voffW[6], so3, ld + l6);
      };
      auto issueKV = [&](u32 slot) { issueW(soW + st * 245760, soW3 + st * 245760, slot); st = st + 1 >= 10 ? 0 : st + 1; };
      auto issueQ = [&](u32 slot) { issueW(soQ + qs * 491520, soQ3 + qs * 491520, slot); qs = qs + 1 >= 5 ? 0 : qs + 1; };
#pragma nounroll
      for (u32 i = 0; i < 3; i++) issueKV(i);
      STAMP(21);
      waitv<12>();
      BAR();
      issueKV(3);
#pragma nounroll
      for (u32 i = 0; i < 6; i++) { waitv<12>(); asm volatile("s_barrier" ::: "memory"); issueKV(i & 3); }
      waitv<12>(); BAR();
      waitv<6>(); BAR();
      waitv<0>(); BAR();
      issueQ(2);
      BAR();
      issueQ(3);
      BAR();
      waitv<0>();
      BAR();
      issueQ(1); issueQ(0);
      BAR();
      issueQ(2);
      GET_TAG();
    } else {
      const bool m1 = b < 108, mo = b >= 216;
      const u32 v = m1 ? b : (mo ? b - 216 : b - 108), vq = m1 ? (v * 2428) >> 16 : (v * 6554) >> 16;
      const u32 sA = uni((m1 || mo) ? 5 * vq : v >> 1), cA = uni(m1 ? v - 27 * vq : (mo ? v - 10 * vq : 5 * (v & 1))), sQ = uni((m1 || mo) ? 1u : 0u);
      const u32 pitch = uni(m1 ? 3456 : 1280);
      const u64 src = m1 ? (u64)w1 : (mo ? (u64)wo : (u64)w2);
      const rsrc_t rS = __builtin_amdgcn_make_buffer_rsrc((void*)src, 0, mo ? 640 * 1280 : 640 * 3456, 0x00020000);
      const u32 vo = (lane >> 3) * pitch + (((lane & 7) ^ (((lane >> 4) & 3) << 1)) << 4);
      const u32 step = uni(sQ ? 32 * pitch : 128u);
      u32 so = 32 * sA * pitch + cA * 128;
#pragma nounroll
      for (u32 q = 0; q < 5; q++, so += step) {
#pragma unroll
        for (u32 i = 0; i < 4; i++) dma<0>(rS, vo, so + i * 8 * pitch, L_RING + 61440 + q * 4096 + i * 1024);
      }
      u4 ONES = {0x3F803F80u, 0x3F803F80u, 0x3F803F80u, 0x3F803F80u};
      landed(ONES);
      u32 ta[5][2];
#pragma unroll
      for (int q = 0; q < 5; q++)
#pragma unroll
        for (int t = 0; t < 2; t++) {
          const u32 i40 = 8 * q + 4 * t + (lr >> 2), hf = i40 >= 20 ? 1u : 0u, id = i40 - 20 * hf;
          const u32 rp = opaque(80 * hf + 15 * ((id * 13) >> 6) + 5 * g + id), row = mo ? rp : 32 * q + 16 * t + 4 * g + (lr >> 2);
          ta[q][t] = opaque(L_RING + 61440 + row * 128 + (lr & 1) * 8 + ((((lr >> 1) & 1) ^ (((row >> 1) & 3) << 1)) << 4));
        }
      const u32 dT = uni(m1 ? 20480u : (mo ? 10240u : 55296u)), dQ = uni(sQ ? 1024u : 4 * 55296u);
      const u32 dA = uni((m1 ? WS_W1T : (mo ? WS_WOT + (sA >= 10 ? 40960u : 0u) : WS_W2T)) + 4 * cA * dT + sA * 1024);
      u32 dJ[4];
#pragma unroll
      for (int n = 0; n < 4; n++) dJ[n] = uni(dA + n * dT + (mo ? (((4 * cA + n) * 52) >> 8) * 51200u : 0u));
      waitv<0>();
      GET_TAG();
      u4 f[5][4];
      f4 cs[4];
#pragma unroll
      for (int q = 0; q < 5; q++)
#pragma unroll
        for (int n = 0; n < 4; n++) f[q][n] = ldstr(ta[q][0] ^ (n << 5), ta[q][1] ^ (n << 5));
      STAMP(21);
      BAR();
#pragma unroll
      for (int q = 0; q < 5; q++) {
#pragma unroll
        for (int n = 0; n < 4; n++) {
          cs[n] = mfma(f[q][n], ONES, q == 0 ? f4{0, 0, 0, 0} : cs[n]);
          st128(f[q][n], rW, l16, dJ[n] + q * dQ);
        }
        BAR();
      }
#pragma unroll
      for (int n = 0; n < 4; n++) st128(__builtin_bit_cast(u4, cs[n]), rW, (m1 && lr == 0) ? g * 16 : OOB, WS_CS1 + vq * 6912 + (4 * cA + n) * 64);
      BAR();
      waitv<0>();
      setflag(ws, WS_FRT + b * 8, tag, lane);
      STAMP(22);
#pragma nounroll
      for (u32 i = 0; i < 7; i++) BAR();
    }
    STAMP(24);
    u4 fr[2];
    ldv<0>(fr[0], rW, lane * 32, WS_FRT); ldv<16>(fr[1], rW, lane * 32, WS_FRT);
    u4 sel[2];
#pragma unroll
    for (int q = 0; q < 2; q++) {
      const u32 k = lr + 16 * q - 8 * g, one = (k & 1) ? 0x3F800000u : 0x00003F80u;
      sel[q] = u4{k < 2 ? one : 0u, (k >> 1) == 1 ? one : 0u, (k >> 1) == 2 ? one : 0u, (k >> 1) == 3 ? one : 0u};
      landed(sel[q]);
    }
    u32 dst[2];
#pragma unroll
    for (int h = 0; h < 2; h++) {
      const u32 qd = 2 * (lr + 16 * (2 * h + (g >> 1))) + (g & 1), e = (qd * 205) >> 10, dq = qd - e * 5;
      const u32 gs = dq + mt, grp = (e * 13) >> 6, ii = e - grp * 5;
      const u32 base = L_IMG + mt * 2048 + (gs & 3) * 256 + (gs >> 2) * 8;
      dst[h] = opaque(e < 20 ? base + (ii < 4 ? (4 * grp + ii) * 16 : 1024 + 4 * grp * 16) : (e == 20 ? base + 1024 + 16 : L_CB + 2048));
    }
    const bool zrow = (dst[1] & 0x430) == 0x410 && dst[1] < L_CB;
    waitv<0>();
    BAR();
    {
      landed(fr[0]); landed(fr[1]);
      const u32 tl32 = (u32)tag, th32 = (u32)(tag >> 32);
      const u32 bad = ((fr[0].x ^ tl32) | (fr[0].z ^ tl32) | (fr[1].x ^ tl32) | (fr[1].z ^ tl32)) | ((fr[0].y ^ th32) | (fr[0].w ^ th32) | (fr[1].y ^ th32) | (fr[1].w ^ th32));
      if (__any(bad != 0)) {
#pragma nounroll
        for (u32 q = 0; q < 4; q++) poll(ws, WS_FRT + (64 * q + lane) * 8, tag);
      }
    }
    sts128(L_IMG + mt * 2048 + l16, u4{0, 0, 0, 0}); sts128(L_IMG + mt * 2048 + 1024 + l16, u4{0, 0, 0, 0});
    STAMP(4);
    poll(ws, WS_FKV + ((j * 4 + mt) * 32 + (lane & 31)) * 8, tag);
    STAMP(5);
    {
      const u32 so = WS_KVP + (j * 32 * 4 + mt) * 896;
      const u32 v0 = lane < 53 ? l16 : OOB;
      u4 a[32];
      sfor<0, 32>([&](auto Q) { ldv<(Q & 1) * 3584>(a[Q], rW, v0, so + (Q & ~1) * 3584); });
      BAR();
      f4 s0, s1, s2, s3;
      sfor<0, 16>([&](auto Q) {
        if constexpr ((Q & 1) == 0) waitv<28 - 2 * Q>();
        if constexpr (Q == 0) { mm0(s0, sel[0], a[0]); mm0(s1, sel[1], a[0]); mm0(s2, sel[0], a[1]); mm0(s3, sel[1], a[1]); }
        else { mm(s0, sel[0], a[2 * Q]); mm(s1, sel[1], a[2 * Q]); mm(s2, sel[0], a[2 * Q + 1]); mm(s3, sel[1], a[2 * Q + 1]); }
      });
      MM_DONE();
      pin(s0); pin(s1); pin(s2); pin(s3);
      s0 += s2; s1 += s3;
      const u2 v1 = {pk2(s1[0], s1[1]), pk2(s1[2], s1[3])};
      sts64(dst[0], u2{pk2(s0[0], s0[1]), pk2(s0[2], s0[3])});
      sts64(dst[1], v1);
      if (zrow) { sts64(dst[1] + 64, v1); sts64(dst[1] + 128, v1); sts64(dst[1] + 192, v1); }
      STAMP(6);
      BAR();
    }
    auto issueW1 = [&](u32 it, u32 n, u32 ld) {
      const u32 so = WS_W1T + (n < 6 ? t0 + n : tl) * 20480 + it * 5120;
      dma4_sc1(rW, l16, so, ld + n * 5120); dma1_sc1(rW, l16, so + 4096, ld + n * 5120 + 4096);
    };
    const u32 o = 51200 + mt * 12288, o2 = 51200 + 49152 + (mt & 1) * 1024, sw = WS_WOT + j * 102400;
    {
      f4 csum;
      u32 ci = lane + 64 * (mt & 1); ci = ci > 111 ? 111 : ci;
      const u32 n = ci >> 4, col = (n < 6 ? t0 + n : tl) * 16 + (ci & 15);
#pragma unroll
      for (int q = 0; q < 4; q++) csum[q] = asf(__builtin_amdgcn_raw_buffer_load_b32(rW, col * 4, WS_CS1 + q * 6912, SC1));
      const u32 bv = b1[col], b2v = ((const u32*)b2)[40 * j + (lane < 40 ? lane : 0)];
      sts32(L_CB + ci * 4, (csum[0] + csum[1]) + (csum[2] + csum[3]));
      sts32(L_CB + 512 + ci * 4, blo(bv));
      if (lane < 40) *(LAS u32*)(L_CB + 1024 + lane * 4) = b2v;
    }
    BAR();
    STAMP(9);
    dma4_sc1(rW, l16, sw + o, L_WO + o); dma4_sc1(rW, l16, sw + o + 4096, L_WO + o + 4096); dma4_sc1(rW, l16, sw + o + 8192, L_WO + o + 8192);
    dma1_sc1(rW, l16, sw + o2, L_WO + o2);
    issueW1(0, mt, L_W1A); issueW1(0, mt < 3 ? mt + 4 : 6, L_W1A);
    sfor<0, HA>(ldw2);
    STAMP(10);
    BAR();
    waitv<10 + HA>();
    STAMP(7);
    BAR();
    issueW1(1, mt, L_W1B); if (mt < 3) issueW1(1, mt + 4, L_W1B);
    sfor<HA, HB2>(ldw2);
    waitv<HB2 - HA>();
    STAMP(25);
    BAR();
    STAMP(8);
#pragma nounroll
    for (u32 u = mt; u < 14; u += 4) { const u32 it = u >= 7 ? 1u : 0u; issueW1(it + 2, u - 7 * it, L_W1B + 35840 + it * 35840); }
    sfor<HB2, 35>(ldw2);
    waitv<35 - HB2>();
    STAMP(28);
    BAR();
    STAMP(26);
    BAR();
    poll(ws, WS_FHID + (p * 32 + (lane & 31)) * 8, tag);
  }
  STAMP(14);
  {
    f4 da[4][5];
    u4 xf[7][4];
    u2 xh[4][2];
    const u32 so = WS_HID + (p * 4 * 54 + s0) * 1024;
    const u32 vh0 = w < 6 ? l8 : l16, vh1 = w < 6 ? l8 + 512 : l16 + 8;
    auto ldx = [&](auto S, auto A) {
      constexpr int s = S, a = A;
      if constexpr (s == 3) { ldv2<3072>(xh[a][0], rW, vh0, so + a * 54 * 1024); ldv2<3072>(xh[a][1], rW, vh1, so + a * 54 * 1024); }
      else ldv<(s & 3) * 1024>(xf[s][a], rW, s == 6 ? vlast : l16, so + (a * 54 + (s & ~3)) * 1024);
    };
    constexpr int XD = 7;
    sfor<0, XD>([&](auto U) { ldx(IC<U / 4>{}, IC<U % 4>{}); });
    sfor<0, 28>([&](auto U) {
      constexpr int u = U, s = u / 4, a = u % 4, e = u + XD + u / 2 < 28 ? u + XD + u / 2 : 28;
      constexpr int lo3 = u + 1 > 12 ? u + 1 : 12, hi3 = e < 16 ? e : 16;
      waitv<(e - u - 1) + (hi3 > lo3 ? hi3 - lo3 : 0)>();
      sfor<0, 5>([&](auto N) {
        if constexpr (s == 0) mm0(da[a][N], w2f[N], xf[s][a]);
        else if constexpr (s == 3) mmh(da[a][N], w2f[5 * s + N], xh[a][0], xh[a][1]);
        else mm(da[a][N], w2f[5 * s + N], xf[s][a]);
      });
      constexpr int e2 = u + 1 + XD + (u + 1) / 2 < 28 ? u + 1 + XD + (u + 1) / 2 : 28;
      sfor<e, e2>([&](auto V) { ldx(IC<V / 4>{}, IC<V % 4>{}); });
    });
    MM_DONE();
    STAMP(17);
#pragma unroll
    for (int a = 0; a < 4; a++)
#pragma unroll
      for (int n = 0; n < 5; n++) {
        pin(da[a][n]);
        sts64(L_RED + ((a * 5 + n) * 8 + w) * 512 + l8, u2{pk2(da[a][n][0], da[a][n][1]), pk2(da[a][n][2], da[a][n][3])});
      }
    BAR();
    STAMP(18);
    const rsrc_t rOut = __builtin_amdgcn_make_buffer_rsrc((void*)out, 0, 2048 * 1280, 0x00020000);
#pragma nounroll
    for (u32 n = 3 * nh; n < 3 + 2 * nh; n++) {
      const u32 ra = L_RED + (mt * 5 + n) * 4096 + l8;
      f2 lo = {0, 0}, hi = {0, 0};
#pragma unroll
      for (int q = 0; q < 8; q++) { u2 v = lds64(ra + q * 512); lo += unp(v.x); hi += unp(v.y); }
      const u2 bb = lds64(L_CB + 1024 + n * 32 + g * 8), hr = lds64(L_HR + (mt * 5 + n) * 512 + l8);
      const u32 o0 = pk2(unp(hr.x) + unp(pk2(unp(pk2(lo)) + unp(bb.x)))), o1 = pk2(unp(hr.y) + unp(pk2(unp(pk2(hi)) + unp(bb.y))));
      __builtin_amdgcn_raw_buffer_store_b64(u2{o0, o1}, rOut, lr * 1280 + g * 8, (p * 64 + mt * 16) * 1280 + j * 160 + n * 32, 0);
    }
  }
  waitv<0>();
  STAMP(19);
  if (b == 77 && w == 0) {
    u32 d = __builtin_amdgcn_readlane(tsv, PROBE) - __builtin_amdgcn_readlane(tsv, 0);
    u64 t0 = __builtin_amdgcn_s_memrealtime();
    while (__builtin_amdgcn_s_memrealtime() - t0 < 20ull * d) __builtin_amdgcn_s_sleep(2);
  }
}

extern "C" void kernel_launch(void* const* d_in, const int* in_sizes, int n_in, void* d_out, int out_size, void* d_ws, size_t ws_size,
                              hipStream_t stream) {
  fused_block<<<256, 512, 0, stream>>>((const u16*)d_in[0], (const u16*)d_in[1], (const u16*)d_in[2], (const u16*)d_in[4], (const u16*)d_in[5],
                                       (const u16*)d_in[6], (const u16*)d_in[7], (u16*)d_out, (char*)d_ws);
}
```

```cpp
#include <hip/hip_runtime.h>
#include <stdint.h>
#pragma clang diagnostic ignored "-Wint-to-pointer-cast"

typedef unsigned short u16;
typedef unsigned u32;
typedef unsigned long long u64;
typedef float f2 __attribute__((ext_vector_type(2)));
typedef float f4 __attribute__((ext_vector_type(4)));
typedef unsigned u2 __attribute__((ext_vector_type(2)));
typedef unsigned u4 __attribute__((ext_vector_type(4)));
typedef short s4 __attribute__((ext_vector_type(4)));
typedef __bf16 bf2 __attribute__((ext_vector_type(2)));
typedef __bf16 bf8 __attribute__((ext_vector_type(8)));
typedef __amdgpu_buffer_rsrc_t rsrc_t;
#define LAS __attribute__((address_space(3)))
#define GAS __attribute__((address_space(1)))
#define DI __device__ __forceinline__

template <int I> struct IC { static constexpr int v = I; constexpr operator int() const { return I; } };
template <int B, int E, class F> DI void sfor(F&& f) {
  if constexpr (B < E) { f(IC<B>{}); sfor<B + 1, E>(f); }
}

constexpr u32 WS_FRT = 4096, WS_FKV = 8192, WS_FO = 16384, WS_FH = 24576, WS_FHID = 32768;
constexpr u32 WS_KVP = 65536;
constexpr u32 WS_OB = 1u << 20;
constexpr u32 WS_HB = WS_OB + 32 * 81920;
constexpr u32 WS_ST = WS_HB + 32 * 81920;
constexpr u32 WS_CS1 = WS_ST + 2048 * 64;
constexpr u32 WS_WOT = WS_CS1 + 32768;
constexpr u32 WS_W1T = WS_WOT + 40 * 20480;
constexpr u32 WS_W2T = WS_W1T + 108 * 20480;
constexpr u32 WS_HID = WS_W2T + 8 * 54 * 5120;
constexpr u32 WS_END = WS_HID + 32 * 4 * 54 * 1024;
constexpr u32 WS_PROF = WS_END;
constexpr u32 OOB = 0x7FFFFF00u;
constexpr int SC1 = 16;
#define MAP 1
#define ROT 2
#define HDV 8
#define TH2 4
#define TH 8
#define HA 4
#define HB2 20
#define CW 5
#define CG 32

constexpr u32 L_X = 0, L_RING = 81920;
constexpr u32 L_WO = 0, L_IMG = 147456, L_W1A = 110592, L_HR = 147456, L_CB = 160768, L_W1B = 0, L_RED = 0;

DI f4 mfma(u4 a, u4 b, f4 c) {
  return __builtin_amdgcn_mfma_f32_16x16x32_bf16(__builtin_bit_cast(bf8, a), __builtin_bit_cast(bf8, b), c, 0, 0, 0);
}
DI void mm(f4& c, u4 a, u4 b) { asm volatile("v_mfma_f32_16x16x32_bf16 %0, %1, %2, %0" : "+v"(c) : "v"(a), "v"(b) : "memory"); }
DI void mm0(f4& c, u4 a, u4 b) { asm volatile("v_mfma_f32_16x16x32_bf16 %0, %1, %2, 0" : "=&v"(c) : "v"(a), "v"(b) : "memory"); }
#define MM_DONE() asm volatile("s_nop 7\n\ts_nop 1" ::: "memory")
DI void pin(f4& c) { asm volatile("" : "+v"(c)); }
DI void zero(f4& c) { c = f4{0, 0, 0, 0}; asm volatile("" : "+v"(c)); }
DI void undef(u4& d) { asm volatile("" : "=v"(d)); }
DI u32 uni(u32 v) { v = __builtin_amdgcn_readfirstlane(v); asm volatile("" : "+s"(v)); return v; }
DI u32 opaque(u32 v) { asm volatile("" : "+v"(v)); return v; }

DI u32 pk2(float a, float b) { f2 v = {a, b}; return __builtin_bit_cast(u32, __builtin_convertvector(v, bf2)); }
DI u32 pk2(f2 v) { return __builtin_bit_cast(u32, __builtin_convertvector(v, bf2)); }
DI float rbf(float a) { return __builtin_bit_cast(float, pk2(a, a) << 16); }
DI float blo(u32 v) { return __builtin_bit_cast(float, v << 16); }
DI float bhi(u32 v) { return __builtin_bit_cast(float, v & 0xFFFF0000u); }
DI f2 unp(u32 v) { return f2{blo(v), bhi(v)}; }
DI float asf(u32 v) { return __builtin_bit_cast(float, v); }
DI u32 asu(float v) { return __builtin_bit_cast(u32, v); }

template <int OFF> DI void dma(rsrc_t r, u32 voff, u32 soff, u32 lds) {
  asm volatile("s_mov_b32 m0, %0\n\ts_nop 0\n\tbuffer_load_dwordx4 %1, %2, %3 offen offset:%4 lds" ::"s"(lds), "v"(voff), "s"(r), "s"(soff), "n"(OFF) : "memory");
}
template <int OFF> DI void dma_c(rsrc_t r, u32 voff, u32 soff) {
  asm volatile("buffer_load_dwordx4 %0, %1, %2 offen offset:%3 lds" ::"v"(voff), "s"(r), "s"(soff), "n"(OFF) : "memory");
}
DI void dma4_sc1(rsrc_t r, u32 voff, u32 soff, u32 lds) {
  asm volatile("s_mov_b32 m0, %0\n\ts_nop 0\n\tbuffer_load_dwordx4 %1, %2, %3 offen sc1 lds\n\tbuffer_load_dwordx4 %1, %2, %3 offen offset:1024 sc1 lds\n\t"
               "buffer_load_dwordx4 %1, %2, %3 offen offset:2048 sc1 lds\n\tbuffer_load_dwordx4 %1, %2, %3 offen offset:3072 sc1 lds" ::"s"(lds), "v"(voff), "s"(r), "s"(soff) : "memory");
}
DI void dma2_sc1(rsrc_t r, u32 voff, u32 soff, u32 lds) {
  asm volatile("s_mov_b32 m0, %0\n\ts_nop 0\n\tbuffer_load_dwordx4 %1, %2, %3 offen sc1 lds\n\tbuffer_load_dwordx4 %1, %2, %3 offen offset:1024 sc1 lds" ::"s"(lds), "v"(voff), "s"(r), "s"(soff) : "memory");
}
DI void dma1_sc1(rsrc_t r, u32 voff, u32 soff, u32 lds) {
  asm volatile("s_mov_b32 m0, %0\n\ts_nop 0\n\tbuffer_load_dwordx4 %1, %2, %3 offen sc1 lds" ::"s"(lds), "v"(voff), "s"(r), "s"(soff) : "memory");
}
template <int OFF> DI void ldv(u4& d, rsrc_t r, u32 voff, u32 soff) {
  asm volatile("buffer_load_dwordx4 %0, %1, %2, %3 offen offset:%4 sc1" : "=v"(d) : "v"(voff), "s"(r), "s"(soff), "n"(OFF) : "memory");
}
template <int OFF> DI void ldv_rw(u4& d, rsrc_t r, u32 voff, u32 soff) {
  asm volatile("buffer_load_dwordx4 %0, %1, %2, %3 offen offset:%4 sc1" : "+v"(d) : "v"(voff), "s"(r), "s"(soff), "n"(OFF) : "memory");
}
template <int OFF> DI void ldp(u4& d, rsrc_t r, u32 voff, u32 soff) {
  asm volatile("buffer_load_dwordx4 %0, %1, %2, %3 offen offset:%4" : "=v"(d) : "v"(voff), "s"(r), "s"(soff), "n"(OFF) : "memory");
}
template <int OFF> DI void ldp2(u2& d, rsrc_t r, u32 voff, u32 soff) {
  asm volatile("buffer_load_dwordx2 %0, %1, %2, %3 offen offset:%4" : "=v"(d) : "v"(voff), "s"(r), "s"(soff), "n"(OFF) : "memory");
}
template <int N> DI void waitv() { asm volatile("s_waitcnt vmcnt(%0)" ::"n"(N) : "memory"); }
template <class T> DI void landed(T& d) { asm volatile("" : "+v"(d)); }
#define BAR() do { asm volatile("" ::: "memory"); __builtin_amdgcn_s_waitcnt(0xC07F); asm volatile("s_barrier" ::: "memory"); } while (0)
DI u4 lds128(u32 a) { return *(LAS u4*)(a); }
DI u2 lds64(u32 a) { return *(LAS u2*)(a); }
DI void sts128(u32 a, u4 v) { *(LAS u4*)(a) = v; }
DI void sts64(u32 a, u2 v) { *(LAS u2*)(a) = v; }
DI void sts32(u32 a, float v) { *(LAS float*)(a) = v; }
DI u4 ldstr(u32 a0, u32 a1) {
  u2 x = __builtin_bit_cast(u2, __builtin_amdgcn_ds_read_tr16_b64_v4i16((LAS s4*)(a0)));
  u2 y = __builtin_bit_cast(u2, __builtin_amdgcn_ds_read_tr16_b64_v4i16((LAS s4*)(a1)));
  return u4{x.x, x.y, y.x, y.y};
}
DI u4 ldstr(u32 a) { return ldstr(a, a + 2560); }
DI float xsum16_32(float v) {
  auto r = __builtin_amdgcn_permlane16_swap(asu(v), asu(v), false, false);
  v = asf((u32)r[0]) + asf((u32)r[1]);
  auto q = __builtin_amdgcn_permlane32_swap(asu(v), asu(v), false, false);
  return asf((u32)q[0]) + asf((u32)q[1]);
}
DI float xsum8(float v) {
  v += asf(__builtin_amdgcn_update_dpp(0, asu(v), 0xB1, 0xf, 0xf, true));
  v += asf(__builtin_amdgcn_update_dpp(0, asu(v), 0x4E, 0xf, 0xf, true));
  return v + asf(__builtin_amdgcn_update_dpp(0, asu(v), 0x141, 0xf, 0xf, true));
}
DI void poll(const char* ws, u32 off, u64 tag) {
  const GAS u64* f = (const GAS u64*)(ws + off);
#pragma nounroll
  for (u32 it = 0; it < (1u << 18); ++it) {
    u64 v = __hip_atomic_load(f, __ATOMIC_RELAXED, __HIP_MEMORY_SCOPE_AGENT);
    if (__all(v == tag)) break;
    __builtin_amdgcn_s_sleep(2);
  }
  __builtin_amdgcn_fence(__ATOMIC_ACQUIRE, "wavefront");
  asm volatile("" ::: "memory");
}
DI void lset(u32 k, u32 mt) { __builtin_amdgcn_s_waitcnt(0xC07F); *(volatile LAS u32*)(L_CB + 1536 + k * 16 + mt * 4) = 1u; asm volatile("" ::: "memory"); }
DI void lwait(u32 k) {
  asm volatile("" ::: "memory");
  for (;;) {
    const u4 v = *(volatile LAS u4*)(L_CB + 1536 + k * 16);
    if (__builtin_amdgcn_readfirstlane(v.x & v.y & v.z & v.w)) break;
    __builtin_amdgcn_s_sleep(1);
  }
  asm volatile("" ::: "memory");
}
DI void setflag(char* ws, u32 off, u64 tag, u32 lane) {
  if (lane == 0) __hip_atomic_store((GAS u64*)(ws + off), tag, __ATOMIC_RELAXED, __HIP_MEMORY_SCOPE_AGENT);
  asm volatile("" ::: "memory");
}
template <bool PHI> DI u32 phi2(float a0, float a1, float r0, float r1) {
  const f2 a = {a0, a1}, r = {r0, r1};
  if constexpr (!PHI) return pk2(a * r);
  const f2 t = a * r + 1e-6f, u = a * (r * 1.4426950408889634f);
  return pk2(fmaxf(t[0], 1e-6f) + __builtin_amdgcn_fmed3f(__builtin_amdgcn_exp2f(u[0]), 0.f, 1.f),
             fmaxf(t[1], 1e-6f) + __builtin_amdgcn_fmed3f(__builtin_amdgcn_exp2f(u[1]), 0.f, 1.f));
}
DI float dot2c(u32 a, u32 b, float c) { return __builtin_amdgcn_fdot2_f32_bf16(__builtin_bit_cast(bf2, a), __builtin_bit_cast(bf2, b), c, false); }
DI u4 center8(u4 x, u32 lo, u32 hi, float nm) {
  u4 r; float t0, t1, t2, t3, t4, t5, t6, t7;
  asm("v_dot2_f32_bf16 %4, %12, %16, %18\n\tv_dot2_f32_bf16 %5, %12, %17, %18\n\tv_dot2_f32_bf16 %6, %13, %16, %18\n\tv_dot2_f32_bf16 %7, %13, %17, %18\n\t"
      "v_dot2_f32_bf16 %8, %14, %16, %18\n\tv_dot2_f32_bf16 %9, %14, %17, %18\n\tv_dot2_f32_bf16 %10, %15, %16, %18\n\tv_dot2_f32_bf16 %11, %15, %17, %18\n\t"
      "v_cvt_pk_bf16_f32 %0, %4, %5\n\tv_cvt_pk_bf16_f32 %1, %6, %7\n\tv_cvt_pk_bf16_f32 %2, %8, %9\n\tv_cvt_pk_bf16_f32 %3, %10, %11"
      : "=v"(r.x), "=v"(r.y), "=v"(r.z), "=v"(r.w), "=&v"(t0), "=&v"(t1), "=&v"(t2), "=&v"(t3), "=&v"(t4), "=&v"(t5), "=&v"(t6), "=&v"(t7)
      : "v"(x.x), "v"(x.y), "v"(x.z), "v"(x.w), "v"(lo), "v"(hi), "v"(nm));
  return r;
}
template <int N> DI void waitl() { asm volatile("" ::: "memory"); __builtin_amdgcn_s_waitcnt(0xC07F | (N << 8)); asm volatile("" ::: "memory"); }
DI void st128(u4 v, rsrc_t r, u32 voff, u32 soff) {
  __builtin_amdgcn_raw_buffer_store_b128(v, r, voff, soff, SC1);
  asm volatile("s_nop 1" ::"v"(v) : "memory");
}
DI void st64(u2 v, rsrc_t r, u32 voff, u32 soff) { __builtin_amdgcn_raw_buffer_store_b64(v, r, voff, soff, SC1); }
DI void publish20(const u32* v, rsrc_t r, u32 l16, u32 so, u32 j) {
  if ((j & 1) == 0) {
    st128(u4{v[0], v[1], v[2], v[3]}, r, l16, so);
    st128(u4{v[4], v[5], v[6], v[7]}, r, l16, so + 1024);
    st64(u2{v[8], v[9]}, r, l16 >> 1, so + 2048);
  } else {
    st64(u2{v[0], v[1]}, r, l16 >> 1, so + 2560);
    st128(u4{v[2], v[3], v[4], v[5]}, r, l16, so + 3072);
    st128(u4{v[6], v[7], v[8], v[9]}, r, l16, so + 4096);
  }
}
template <int OFF> DI void ldv2(u2& d, rsrc_t r, u32 voff, u32 soff) {
  asm volatile("buffer_load_dwordx2 %0, %1, %2, %3 offen offset:%4 sc1" : "=v"(d) : "v"(voff), "s"(r), "s"(soff), "n"(OFF) : "memory");
}
DI void mmh(f4& c, u4 a, u2 b0, u2 b1) { mm(c, a, u4{b0.x, b0.y, b1.x, b1.y}); }
template <int V, int L> DI void waitvl() {
  asm volatile("" ::: "memory"); __builtin_amdgcn_s_waitcnt(0x0070 | (V & 15) | ((V >> 4) << 14) | (L << 8)); asm volatile("" ::: "memory");
}

#define STAMP(i)

__global__ __launch_bounds__(512, 2) void fused_block(const u16* __restrict__ x, const u16* __restrict__ wqkv, const u16* __restrict__ wo,
                                                      const u16* __restrict__ w1, const u16* __restrict__ b1, const u16* __restrict__ w2,
                                                      const u16* __restrict__ b2, u16* __restrict__ out, char* __restrict__ ws) {
  __shared__ __attribute__((aligned(1024))) char smem[163840];
  asm volatile("" ::"v"((u32)(uintptr_t)(LAS char*)smem));
  const u32 tid = threadIdx.x, lane = tid & 63, lr = lane & 15, g = lane >> 4;
  const u32 w = __builtin_amdgcn_readfirstlane(tid >> 6), mt = w & 3, nh = w >> 2;
  const bool helper = nh != 0;
  const u32 b = blockIdx.x, xg = b & 7, rr = b >> 3, p = 8 * (xg >> 1) + (rr >> 2), j = 4 * (xg & 1) + (rr & 3);
  asm volatile("" ::"s"(x), "s"(wqkv), "s"(wo), "s"(w1), "s"(b1), "s"(w2), "s"(b2), "s"(out), "s"(ws));
  STAMP(0);
  const rsrc_t rW = __builtin_amdgcn_make_buffer_rsrc((void*)ws, 0, WS_END, 0x00020000);
  const u32 l16 = lane * 16, l8 = lane * 8;
  u2 ep;
  u64 tag;
#define LD_EPOCH() asm volatile("buffer_load_dwordx2 %0, off, %1, 0 sc1" : "=v"(ep) : "s"(rW) : "memory")
#define GET_TAG() do { u32 lo_, hi_; asm volatile("v_readfirstlane_b32 %0, %2\n\tv_readfirstlane_b32 %1, %3\n\ts_nop 3" : "=s"(lo_), "=s"(hi_) : "v"(ep.x), "v"(ep.y) : "memory"); \
    tag = (((u64)hi_ << 32) | lo_) + 1; } while (0)
  u32 rot = xg + 3 * (p & 3), rotq = xg + (p & 3);
  rot = rot >= 10 ? rot - 10 : rot; rotq = rotq >= 10 ? rotq - 10 : (rotq >= 5 ? rotq - 5 : rotq);
  const u32 P = p >> 1, c = ((p & 1) << 3) | j, rho = c >> 1, odd = c & 1;
  const u32 t0 = rho < 6 ? 14 * rho + 8 * odd : 12 * rho + 12 + 6 * odd, tl = rho < 6 ? 14 * rho + 6 + odd : t0;
  const u32 s0 = w < 6 ? 7 * w : 6 * w + 6;
  const u32 vlast = w >= 6 ? OOB : l16 + 3072, vmid = w >= 6 ? l16 : l16 + 1024, vsp = w >= 6 ? l16 + 2048 : l16, soW2 = WS_W2T + (j * 5 * 54 + s0) * 1024;
  u4 w2f[35];
  auto ldw2 = [&](auto I) {
    constexpr int s = I / 5, n = I % 5;
    if constexpr (s < 2) ldv<s * 1024>(w2f[I], rW, l16, soW2 + n * 54 * 1024);
    else if constexpr (s < 4) ldv<(s - 2) * 1024>(w2f[I], rW, vmid, soW2 + (n * 54 + 3) * 1024);
    else if constexpr (s == 4) ldv<2048>(w2f[I], rW, l16, soW2 + n * 54 * 1024);
    else ldv<0>(w2f[I], rW, s == 5 ? vsp : vlast, soW2 + (n * 54 + 3) * 1024);
  };
  constexpr u32 L_RS = L_RING + 4096;

  if (!helper) {
    const rsrc_t rX = __builtin_amdgcn_make_buffer_rsrc((void*)x, 0, 2048 * 1280, 0x00020000);
    float rsd[2];
    const u32 ONE2 = opaque(0x3F803F80u);
    {
      u4 xr[2][10];
      const u32 vx = (lane >> 3) * 1280 + (lane & 7) * 16, sx = (p * 64 + 16 * mt) * 1280;
      sfor<0, 10>([&](auto I) { ldp<I * 128>(xr[0][I], rX, vx, sx); });
      sfor<0, 10>([&](auto I) { ldp<I * 128>(xr[1][I], rX, vx, sx + 8 * 1280); });
      LD_EPOCH();
      const u32 wa = L_X + mt * 2048 + (lane >> 3) * 128 + (((lane & 7) ^ (lane >> 3)) << 4);
      const u32 lo = opaque(0x00003F80u), hi = opaque(0x3F800000u);
      float dm[2];
      sfor<0, 2>([&](auto G) {
        waitv<(G == 0 ? 11 : 1)>();
        STAMP(20 + G);
        float s0 = 0.f, s1 = 0.f;
#pragma unroll
        for (int i = 0; i < 10; i++) {
          landed(xr[G][i]);
          s0 = dot2c(xr[G][i].x, ONE2, s0); s1 = dot2c(xr[G][i].y, ONE2, s1); s0 = dot2c(xr[G][i].z, ONE2, s0); s1 = dot2c(xr[G][i].w, ONE2, s1);
        }
        const float m = xsum8(s0 + s1) * (1.f / 640.f), nm = -rbf(m);
        dm[G] = m + nm;
#pragma unroll
        for (int i = 0; i < 10; i++) { xr[G][i] = center8(xr[G][i], lo, hi, nm); sts128(wa + G * 1024 + i * 8192, xr[G][i]); }
      });
      sfor<0, 2>([&](auto G) {
        float s0 = 0.f, s1 = 0.f;
#pragma unroll
        for (int i = 0; i < 10; i++) {
          const u4 v = xr[G][i];
          s0 = dot2c(v.x, v.x, s0); s1 = dot2c(v.y, v.y, s1); s0 = dot2c(v.z, v.z, s0); s1 = dot2c(v.w, v.w, s1);
        }
        const float var = fmaxf(xsum8(s0 + s1) * (1.f / 640.f) - dm[G] * dm[G], 0.f);
        rsd[G] = __builtin_amdgcn_rcpf(rbf(__builtin_amdgcn_sqrtf(rbf(rbf(var) + 1e-5f))));
        landed(rsd[G]);
      });
    }
    STAMP(22);
    waitv<0>();
    GET_TAG();
    const u32 xa0 = L_X + (lr >> 3) * 1024 + (lr & 7) * 128 + ((g ^ (lr & 7)) << 4);
    const u32 tr0 = L_RING + (4 * g + (lr >> 2)) * 160 + (lr & 3) * 8;
    const u32 mh = mt & 1, nq = mt >> 1;
    float qrs;
    {
      f4 acc[2][5];
#pragma unroll
      for (int i = 0; i < 5; i++) { zero(acc[0][i]); zero(acc[1][i]); }
      const u32 bA = tr0 + nq * 96, bC = tr0 + 64 + nq * 5120;
      const u32 xs0 = xa0 + mh * 4096;
      auto half = [&](u4* cur, u4* nxt, u32 xa, u32 a, u32 c, auto KS, auto LD) {
        constexpr int o = KS * 10240;
        mm(acc[0][0], cur[5], cur[0]);
        if constexpr (LD) nxt[5] = lds128(xa);
        mm(acc[1][0], cur[6], cur[0]);
        if constexpr (LD) nxt[6] = lds128(xa + 2048);
        mm(acc[0][1], cur[5], cur[1]);
        if constexpr (LD) nxt[0] = ldstr(a + o);
        mm(acc[1][1], cur[6], cur[1]);
        if constexpr (LD) nxt[1] = ldstr(a + o + 32);
        mm(acc[0][2], cur[5], cur[2]);
        if constexpr (LD) nxt[2] = ldstr(c + o);
        mm(acc[1][2], cur[6], cur[2]);
        if constexpr (LD) nxt[3] = ldstr(a + o + 5120);
        mm(acc[0][3], cur[5], cur[3]);
        if constexpr (LD) nxt[4] = ldstr(a + o + 5152);
        mm(acc[1][3], cur[6], cur[3]);
        mm(acc[0][4], cur[5], cur[4]); mm(acc[1][4], cur[6], cur[4]);
      };
      u4 fa[7], fb[7];
      u32 st = rot, xa = xs0 + st * 8192, a = bA, cc = bC;
      BAR();
      STAMP(1);
      fa[5] = lds128(xa); fa[6] = lds128(xa + 2048); fa[0] = ldstr(a); fa[1] = ldstr(a + 32); fa[2] = ldstr(cc);
      fa[3] = ldstr(a + 5120); fa[4] = ldstr(a + 5152);
      u32 lkv[6];
#pragma unroll
      for (int q = 0; q < 2; q++) {
        const u32 dq = 4 * q + g - mt, e0 = lr - 4 * mt, e1 = e0 + 16, sb = L_RING + mt * 5120;
        lkv[3 * q] = opaque(sb + ((dq < 5 && e0 < 20) ? e0 * 40 + dq * 8 : 1024));
        lkv[3 * q + 1] = opaque(sb + ((dq < 5 && e1 < 20) ? e1 * 40 + dq * 8 : 1024));
        lkv[3 * q + 2] = opaque(sb + ((dq < 5 && lr == 0) ? 800 + dq * 8 : (dq == 0 && lr == 1) ? 840 : 1024));
      }
#pragma nounroll
      for (u32 i = 1; i < 10; i++) {
        waitl<0>();
        half(fa, fb, xa ^ 64, a, cc, IC<1>{}, IC<1>{});
        st = st + 1 >= 10 ? 0 : st + 1;
        xa = opaque(xs0 + st * 8192); a = opaque(bA + (i & 3) * 20480); cc = opaque(bC + (i & 3) * 20480);
        BAR();
        half(fb, fa, xa, a, cc, IC<0>{}, IC<1>{});
      }
      if ((lane & 7) == 0) { sts32(L_RS + mt * 64 + (lane >> 3) * 4, rsd[0]); sts32(L_RS + mt * 64 + 32 + (lane >> 3) * 4, rsd[1]); }
      waitl<0>();
      half(fa, fb, xa ^ 64, a, cc, IC<1>{}, IC<1>{});
      BAR();
      const f4 r0 = __builtin_bit_cast(f4, lds128(L_RS + mh * 128 + g * 16)), r1 = __builtin_bit_cast(f4, lds128(L_RS + mh * 128 + 64 + g * 16));
      qrs = *(LAS float*)(L_RS + mt * 64 + lr * 4);
      half(fb, fa, 0, 0, 0, IC<0>{}, IC<0>{});
      MM_DONE();
      STAMP(2);
      const u32 EX = L_RING + 20480;
      const u32 ew = EX + mh * 10240 + nq * 3072 + l16;
      auto put = [&](auto N, auto PHI, u32 a) {
        constexpr int n = N;
        pin(acc[0][n]); pin(acc[1][n]);
        sts128(a, u4{phi2<PHI>(acc[0][n][0], acc[0][n][1], r0[0], r0[1]), phi2<PHI>(acc[0][n][2], acc[0][n][3], r0[2], r0[3]),
                     phi2<PHI>(acc[1][n][0], acc[1][n][1], r1[0], r1[1]), phi2<PHI>(acc[1][n][2], acc[1][n][3], r1[2], r1[3])});
      };
      put(IC<0>{}, IC<1>{}, ew); put(IC<1>{}, IC<1>{}, ew + 1024); put(IC<3>{}, IC<0>{}, ew + 5120); put(IC<4>{}, IC<0>{}, ew + 6144);
      if (nq == 0) put(IC<2>{}, IC<1>{}, ew + 2048); else put(IC<2>{}, IC<0>{}, ew + 4096);
      BAR();
      const u32 ex = EX + mt * 1024 + l16;
      u4 kf[2][2], vf[2][2];
#pragma unroll
      for (int m = 0; m < 2; m++) {
        kf[m][0] = lds128(ex + m * 10240); vf[m][0] = lds128(ex + m * 10240 + 5120);
        vf[m][1] = lds128(ex + m * 10240 + 6144); kf[m][1] = lds128(ex + m * 10240 + 1024);
      }
      const u32 o0 = lr == 0 ? ONE2 : 0u;
      const u4 ONE0 = {o0, o0, o0, o0};
#pragma unroll
      for (int q = 0; q < 2; q++) {
        f4 z4 = {0, 0, 0, 0};
        f4 kv0 = mfma(kf[1][q], vf[1][0], mfma(kf[0][q], vf[0][0], z4)), kv1 = mfma(kf[1][q], vf[1][1], mfma(kf[0][q], vf[0][1], z4));
        f4 zz = mfma(kf[1][q], ONE0, mfma(kf[0][q], ONE0, z4));
        sts64(lkv[3 * q], u2{pk2(kv0[0], kv0[1]), pk2(kv0[2], kv0[3])});
        sts64(lkv[3 * q + 1], u2{pk2(kv1[0], kv1[1]), pk2(kv1[2], kv1[3])});
        sts64(lkv[3 * q + 2], u2{pk2(zz[0], zz[1]), pk2(zz[2], zz[3])});
      }
      asm volatile("" ::: "memory");
      st128(lds128(L_RING + mt * 5120 + l16), rW, lane < 56 ? l16 : OOB, WS_KVP + ((j * 32 + p) * 4 + mt) * 896);
    }
    BAR();
    waitv<0>();
    setflag(ws, WS_FKV + ((j * 4 + mt) * 32 + p) * 8, tag, lane);
    STAMP(3);
    u2 xres[5];
    sfor<0, 5>([&](auto N) { ldp2<N * 32>(xres[N], rX, lr * 1280 + g * 8, (p * 64 + mt * 16) * 1280 + j * 160); });
    u32 pq[5][2];
    {
      f4 qa[5];
#pragma unroll
      for (int n = 0; n < 5; n++) zero(qa[n]);
      u32 qs = rotq, xb = xa0 + mt * 2048 + qs * 16384, a0 = tr0 + 2 * 20480;
      u4 fa[6], fb[6];
      fa[5] = lds128(xb); fa[0] = ldstr(a0); fa[1] = ldstr(a0 + 32);
      asm volatile("" ::: "memory");
      fa[2] = ldstr(a0 + 64); fa[3] = ldstr(a0 + 96); fa[4] = ldstr(a0 + 128);
      auto step = [&](u4* cur, u4* nxt, u32 xa, u32 a) {
        waitl<6>();
        mm(qa[0], cur[0], cur[5]);
        nxt[5] = lds128(xa); nxt[0] = ldstr(a);
        mm(qa[1], cur[1], cur[5]);
        nxt[1] = ldstr(a + 32);
        waitl<5>();
        mm(qa[2], cur[2], cur[5]);
        nxt[2] = ldstr(a + 64);
        mm(qa[3], cur[3], cur[5]);
        nxt[3] = ldstr(a + 96);
        mm(qa[4], cur[4], cur[5]);
        nxt[4] = ldstr(a + 128);
      };
#pragma nounroll
      for (u32 qi = 0; qi < 5; qi++) {
        const u32 xc = opaque(xb ^ 64);
        step(fa, fb, xc, a0 + 5120);
        step(fb, fa, xb + 8192, a0 + 10240);
        step(fa, fb, xc + 8192, a0 + 15360);
        qs = qs + 1 >= 5 ? 0 : qs + 1;
        xb = opaque(xa0 + mt * 2048 + qs * 16384); a0 = opaque(tr0 + ((0x02013u >> (4 * qi)) & 3) * 20480);
        if (qi < 2) BAR();
        step(fb, fa, xb, a0);
      }
      MM_DONE();
      STAMP(4);
#pragma unroll
      for (int n = 0; n < 5; n++) {
        pin(qa[n]);
        pq[n][0] = opaque(phi2<1>(qa[n][0], qa[n][1], qrs, qrs)); pq[n][1] = opaque(phi2<1>(qa[n][2], qa[n][3], qrs, qrs));
      }
    }
    STAMP(5);
    BAR();
    {
      const u32 o = mt * 12288, o2 = 49152 + (mt & 1) * 1024, sw = WS_WOT + j * 102400;
      dma4_sc1(rW, l16, sw + o, L_WO + o); dma4_sc1(rW, l16, sw + o + 4096, L_WO + o + 4096); dma4_sc1(rW, l16, sw + o + 8192, L_WO + o + 8192);
      dma1_sc1(rW, l16, sw + o2, L_WO + o2);
    }
    __builtin_amdgcn_s_setprio(2);
    STAMP(23);
    BAR();
    STAMP(6);
    {
      float o[20];
#pragma unroll
      for (int h = 0; h < 4; h++) {
        u4 bq = {pq[h][0], pq[h][1], pq[h + 1][0], pq[h + 1][1]};
        f4 z4 = {0, 0, 0, 0};
        f4 d0 = mfma(lds128(L_IMG + h * 2048 + l16), bq, z4);
        f4 d1 = mfma(lds128(L_IMG + h * 2048 + 1024 + l16), bq, z4);
        float rd = __builtin_amdgcn_rcpf(d1[1] + 1e-6f);
#pragma unroll
        for (int i = 0; i < 4; i++) o[5 * h + i] = d0[i] * rd;
        o[5 * h + 4] = d1[0] * rd;
      }
      u32 ov[10];
#pragma unroll
      for (int i = 0; i < 10; i++) ov[i] = pk2(o[2 * i], o[2 * i + 1]);
      publish20(ov, rW, l16, WS_OB + ((p * 4 + mt) * 20 + 5 * (j >> 1)) * 1024, j);
    }
    waitv<0>();
    setflag(ws, WS_FO + ((p * 4 + mt) * 8 + j) * 8, tag, lane);
    if (b == 0 && tid == 0) __hip_atomic_store((GAS u64*)ws, tag, __ATOMIC_RELAXED, __HIP_MEMORY_SCOPE_AGENT);
    BAR();
    STAMP(7);
    {
      u4 of[2][10], wf[5];
      u2 oh[2][2][2];
      const u32 kh = mt & 1;
      const u32 so[2] = {WS_OB + (p * 4 + mt) * 20480 + kh * 5120, WS_OB + (p * 4 + (mt ^ 1)) * 20480 + kh * 5120};
      auto ldo = [&](auto S) {
        constexpr int s = S;
        sfor<0, 2>([&](auto M) {
          if constexpr (s % 5 == 2) { ldv2<2048>(oh[M][s / 5][0], rW, l8, so[M] + (s / 5) * 10240); ldv2<2560>(oh[M][s / 5][1], rW, l8, so[M] + (s / 5) * 10240); }
          else ldv<(s % 5 < 4 ? (s % 5) * 1024 : 0)>(of[M][s], rW, l16, so[M] + (s / 5) * 10240 + (s % 5 < 4 ? 0 : 4096));
        });
      };
      const u32 a0 = opaque(L_WO + kh * 5120 + l16), a1 = opaque(L_WO + 51200 + kh * 5120 + l16);
#pragma unroll
      for (int n = 0; n < 5; n++) wf[n] = lds128(a0 + n * 10240);
      poll(ws, WS_FO + ((p * 4 + (mt & 2) + ((lane >> 2) & 1)) * 8 + 2 * kh + (lane & 1) + 2 * (lane & 2)) * 8, tag);
      STAMP(8);
      sfor<0, 6>(ldo);
      f4 ha[2][5];
      sfor<0, 10>([&](auto S) {
        constexpr int s = S, e = s + 6 < 10 ? s + 6 : 10;
        constexpr int yo = 2 * ((e - s - 1) + ((e + 2) / 5 - (s + 3) / 5));
        if constexpr (s == 4) { lset(1, mt); lwait(2); }
        waitvl<yo, 2>();
        sfor<0, 5>([&](auto N) {
          constexpr int n = N, t = s + 1;
          if constexpr (n == 3) waitl<(s < 9 ? 3 : 0)>();
          sfor<0, 2>([&](auto M) {
            if constexpr (s == 0) mm0(ha[M][n], wf[n], of[M][s]);
            else if constexpr (s % 5 == 2) mmh(ha[M][n], wf[n], oh[M][s / 5][0], oh[M][s / 5][1]);
            else mm(ha[M][n], wf[n], of[M][s]);
          });
          if constexpr (s < 9) wf[n] = lds128((t < 5 ? a0 : a1) + n * 10240 + (t % 5) * 1024);
          if constexpr (n == 1 && s + 6 < 10) ldo(IC<s + 6>{});
        });
      });
      MM_DONE();
      {
        lwait(1);
        const u32 xw = (mt < 3 ? 35840 + mt * 5120 : 102400) + l16, xr = ((mt ^ 1) < 3 ? 35840 + (mt ^ 1) * 5120 : 102400) + l16;
#pragma unroll
        for (int n = 0; n < 5; n++) { pin(ha[1][n]); sts128(xw + n * 1024, __builtin_bit_cast(u4, ha[1][n])); }
        waitl<0>();
        *(volatile LAS u32*)(L_CB + 1536 + mt * 4) = 1u;
        while (*(volatile LAS u32*)(L_CB + 1536 + (mt ^ 1) * 4) == 0u) {}
        asm volatile("" ::: "memory");
#pragma unroll
        for (int n = 0; n < 5; n++) { pin(ha[0][n]); ha[0][n] += __builtin_bit_cast(f4, lds128(xr + n * 1024)); }
      }
      STAMP(9);
      u32 hres[10];
      float sum = 0.f, sq = 0.f;
#pragma unroll
      for (int n = 0; n < 5; n++) {
        hres[2 * n] = pk2(unp(xres[n].x) + f2{ha[0][n][0], ha[0][n][1]});
        hres[2 * n + 1] = pk2(unp(xres[n].y) + f2{ha[0][n][2], ha[0][n][3]});
      }
      publish20(hres, rW, l16, WS_HB + ((p * 4 + mt) * 20 + 5 * (j >> 1)) * 1024, j);
#pragma unroll
      for (int i = 0; i < 10; i++) { sum = dot2c(hres[i], ONE2, sum); sq = dot2c(hres[i], hres[i], sq); }
      st64(u2{asu(xsum16_32(sum)), asu(xsum16_32(sq))}, rW, g == 0 ? l8 : OOB, WS_ST + ((p * 4 + mt) * 8 + j) * 128);
#pragma unroll
      for (int n = 0; n < 5; n++) sts64(L_HR + (mt * 5 + n) * 512 + l8, u2{hres[2 * n], hres[2 * n + 1]});
      waitv<0>();
      setflag(ws, WS_FH + ((p * 4 + mt) * 8 + j) * 8, tag, lane);
      lset(3, mt);
      STAMP(10);
      lwait(4);
    }
    {
      constexpr int HD = HDV;
      const u32 pc = 2 * P + (mt >> 1), mc = 2 * (mt & 1);
      f4 ca[2][7];
      u4 hf[2][20], wf[7];
      u2 hh[2][4][2];
      const u32 so = WS_HB + (pc * 4 + mc) * 20480;
      auto ldh = [&](auto S) {
        constexpr int s = S;
        sfor<0, 2>([&](auto M) {
          if constexpr (s % 5 == 2) {
            ldv2<(s & 3) * 1024>(hh[M][s / 5][0], rW, l8, so + M * 20480 + (s & ~3) * 1024);
            ldv2<(s & 3) * 1024 + 512>(hh[M][s / 5][1], rW, l8, so + M * 20480 + (s & ~3) * 1024);
          } else ldv<(s & 3) * 1024>(hf[M][s], rW, l16, so + M * 20480 + (s & ~3) * 1024);
        });
      };
      const u32 a0 = opaque(L_W1A + l16), a1 = opaque(L_W1B + l16), a2 = opaque(L_W1B + 35840 + l16), a3 = opaque(L_W1B + 71680 + l16);
#pragma unroll
      for (int n = 0; n < 7; n++) wf[n] = lds128(a0 + n * 5120);
      poll(ws, WS_FH + ((pc * 4 + mc) * 8 + lr) * 8, tag);
      STAMP(11);
      sfor<0, HD>(ldh);
      sfor<0, 20>([&](auto S) {
        constexpr int s = S, e = s + HD < 20 ? s + HD : 20;
        constexpr int yo = 2 * ((e - s - 1) + ((e + 2) / 5 - (s + 3) / 5));
        if constexpr (s == 9) lwait(5);
        waitvl<yo, 3>();
        sfor<0, 7>([&](auto N) {
          constexpr int n = N, t = s + 1;
          if constexpr (n == 4) waitl<(s < 19 ? 4 : 0)>();
          sfor<0, 2>([&](auto M) {
            if constexpr (s == 0) mm0(ca[M][n], wf[n], hf[M][s]);
            else if constexpr (s % 5 == 2) mmh(ca[M][n], wf[n], hh[M][s / 5][0], hh[M][s / 5][1]);
            else mm(ca[M][n], wf[n], hf[M][s]);
          });
          if constexpr (s < 19) wf[n] = lds128((t < 5 ? a0 : t < 10 ? a1 : t < 15 ? a2 : a3) + n * 5120 + (t % 5) * 1024);
          if constexpr (n == 2 && s + HD < 20) ldh(IC<s + HD>{});
        });
      });
      MM_DONE();
      lset(7, mt);
      lwait(6);
      STAMP(12);
      const u32 sh = WS_HID + (pc * 4 + mc) * 54 * 1024, fh = WS_FHID + (((pc * 16 + c) * 2 + (mt & 1)) * 2) * 8;
      const u2 s0v = lds64(L_CB + 2048 + mt * 256 + lr * 8), s1v = lds64(L_CB + 2048 + mt * 256 + 128 + lr * 8);
      const float sc[2] = {asf(s0v.x), asf(s1v.x)}, sm[2] = {asf(s0v.y), asf(s1v.y)};
      u32 hp[2][2][2];
      sfor<0, 7>([&](auto N) {
        constexpr int n = N;
        const f4 c4 = __builtin_bit_cast(f4, lds128(L_CB + n * 64 + g * 16)), b4 = __builtin_bit_cast(f4, lds128(L_CB + 512 + n * 64 + g * 16));
        if constexpr (n == 6) { waitv<2>(); setflag(ws, fh, tag, lane); STAMP(15); }
        sfor<0, 2>([&](auto M) {
          pin(ca[M][n]);
#pragma unroll
          for (int i = 0; i < 2; i++) {
            const f2 v = f2{ca[M][n][2 * i], ca[M][n][2 * i + 1]} * sc[M] + (f2{b4[2 * i], b4[2 * i + 1]} - sm[M] * f2{c4[2 * i], c4[2 * i + 1]});
            const f2 u = v * (-2.302208198f - 0.1029432397f * v * v);
            const f2 d = f2{__builtin_amdgcn_exp2f(u[0]), __builtin_amdgcn_exp2f(u[1])} + 1.f;
            hp[M][n & 1][i] = pk2(v * f2{__builtin_amdgcn_rcpf(d[0]), __builtin_amdgcn_rcpf(d[1])});
          }
          if constexpr (n & 1) st128(u4{hp[M][0][0], hp[M][0][1], hp[M][1][0], hp[M][1][1]}, rW, l16, sh + M * 54 * 1024 + ((t0 >> 1) + n / 2) * 1024);
          if constexpr (n == 6) st64(u2{hp[M][0][0], hp[M][0][1]}, rW, rho < 6 ? l8 + odd * 512 : OOB, sh + M * 54 * 1024 + (tl >> 1) * 1024);
        });
      });
      STAMP(29);
      waitv<0>();
      setflag(ws, fh + 8, tag, lane);
      __builtin_amdgcn_s_setprio(0);
      STAMP(13);
      lwait(8);
      const u32 wa = opaque(mt * 35840 + l16);
#pragma unroll
      for (int i = 0; i < 35; i++) w2f[i] = lds128(wa + i * 1024);
      lset(9, mt);
    }
  } else {
    STAMP(20);
    LD_EPOCH();
    if (mt < 3) {
      const rsrc_t rQ = __builtin_amdgcn_make_buffer_rsrc((void*)((const char*)wqkv - 4096), 0, 640 * 3840 + 4096, 0x00020000);
      u32 voffW[7];
#pragma unroll
      for (int i = 0; i < 7; i++) {
        const u32 pi = i < 5 ? i : (i == 5 ? mt : (mt < 2 ? mt + 3 : 4u));
        const u32 f = 64 * pi + lane, sg = (f * 6554) >> 16, ch = f - sg * 10;
        voffW[i] = (((sg & 12) << 1) + ((sg >> 4) << 2) + (sg & 3)) * 3840 + ch * 16 + 4096 - (i < 4 ? i * 1024 : 0);
      }
      const u32 soW = (mt >> 1) * 32 * 3840 + ((mt & 1) + 1) * 1280 + j * 160, soW3 = 32 * 3840 + 2560 + j * 160;
      const u32 soQ = mt * 32 * 3840 + j * 160, soQ3 = 96 * 3840 + j * 160;
      const u32 l5 = 15360 + mt * 1024, l6 = 15360 + (mt < 2 ? mt + 3 : 4u) * 1024;
      u32 st = rot, qs = rotq;
      auto issueW = [&](u32 so, u32 so3, u32 slot) {
        const u32 ld = L_RING + slot * 20480;
        dma<0>(rQ, voffW[0], so, ld + mt * 5120); dma_c<1024>(rQ, voffW[1], so); dma_c<2048>(rQ, voffW[2], so); dma_c<3072>(rQ, voffW[3], so);
        dma<0>(rQ, voffW[4], so, ld + mt * 5120 + 4096); dma<0>(rQ, voffW[5], so3, ld + l5);
        if (mt < 2) dma<0>(rQ, voffW[6], so3, ld + l6);
      };
      auto issueKV = [&](u32 slot) { issueW(soW + st * 245760, soW3 + st * 245760, slot); st = st + 1 >= 10 ? 0 : st + 1; };
      auto issueQ = [&](u32 slot) { issueW(soQ + qs * 491520, soQ3 + qs * 491520, slot); qs = qs + 1 >= 5 ? 0 : qs + 1; };
#pragma nounroll
      for (u32 i = 0; i < 3; i++) issueKV(i);
      STAMP(21);
      waitv<12>();
      BAR();
      issueKV(3);
#pragma nounroll
      for (u32 i = 0; i < 6; i++) { waitv<12>(); asm volatile("s_barrier" ::: "memory"); issueKV(i & 3); }
      waitv<12>(); BAR();
      waitv<6>(); BAR();
      waitv<0>(); BAR();
      issueQ(2);
      BAR();
      issueQ(3);
      BAR();
      waitv<0>();
      BAR();
      issueQ(1); issueQ(0);
      BAR();
      issueQ(2);
      GET_TAG();
    } else {
      const bool m1 = b < 108, mo = b >= 216;
      const u32 v = m1 ? b : (mo ? b - 216 : b - 108), vq = m1 ? (v * 2428) >> 16 : (v * 6554) >> 16;
      const u32 sA = uni((m1 || mo) ? 5 * vq : v >> 1), cA = uni(m1 ? v - 27 * vq : (mo ? v - 10 * vq : 5 * (v & 1))), sQ = uni((m1 || mo) ? 1u : 0u);
      const u32 pitch = uni(m1 ? 3456 : 1280);
      const u64 src = m1 ? (u64)w1 : (mo ? (u64)wo : (u64)w2);
      const rsrc_t rS = __builtin_amdgcn_make_buffer_rsrc((void*)src, 0, mo ? 640 * 1280 : 640 * 3456, 0x00020000);
      const u32 vo = (lane >> 3) * pitch + (((lane & 7) ^ (((lane >> 4) & 3) << 1)) << 4);
      const u32 step = uni(sQ ? 32 * pitch : 128u);
      u32 so = 32 * sA * pitch + cA * 128;
#pragma nounroll
      for (u32 q = 0; q < 5; q++, so += step) {
#pragma unroll
        for (u32 i = 0; i < 4; i++) dma<0>(rS, vo, so + i * 8 * pitch, L_RING + 61440 + q * 4096 + i * 1024);
      }
      u4 ONES = {0x3F803F80u, 0x3F803F80u, 0x3F803F80u, 0x3F803F80u};
      landed(ONES);
      u32 ta[5][2];
#pragma unroll
      for (int q = 0; q < 5; q++)
#pragma unroll
        for (int t = 0; t < 2; t++) {
          const u32 i40 = 8 * q + 4 * t + (lr >> 2), hf = i40 >= 20 ? 1u : 0u, id = i40 - 20 * hf;
          const u32 rp = opaque(80 * hf + 15 * ((id * 13) >> 6) + 5 * g + id), row = mo ? rp : 32 * q + 16 * t + 4 * g + (lr >> 2);
          ta[q][t] = opaque(L_RING + 61440 + row * 128 + (lr & 1) * 8 + ((((lr >> 1) & 1) ^ (((row >> 1) & 3) << 1)) << 4));
        }
      const u32 dT = uni(m1 ? 20480u : (mo ? 10240u : 55296u)), dQ = uni(sQ ? 1024u : 4 * 55296u);
      const u32 dA = uni((m1 ? WS_W1T : (mo ? WS_WOT + (sA >= 10 ? 40960u : 0u) : WS_W2T)) + 4 * cA * dT + sA * 1024);
      u32 dJ[4];
#pragma unroll
      for (int n = 0; n < 4; n++) dJ[n] = uni(dA + n * dT + (mo ? (((4 * cA + n) * 52) >> 8) * 51200u : 0u));
      waitv<0>();
      GET_TAG();
      u4 f[5][4];
      f4 cs[4];
#pragma unroll
      for (int q = 0; q < 5; q++)
#pragma unroll
        for (int n = 0; n < 4; n++) f[q][n] = ldstr(ta[q][0] ^ (n << 5), ta[q][1] ^ (n << 5));
      STAMP(21);
      BAR();
#pragma unroll
      for (int q = 0; q < 5; q++) {
#pragma unroll
        for (int n = 0; n < 4; n++) {
          cs[n] = mfma(f[q][n], ONES, q == 0 ? f4{0, 0, 0, 0} : cs[n]);
          st128(f[q][n], rW, l16, dJ[n] + q * dQ);
        }
        BAR();
      }
#pragma unroll
      for (int n = 0; n < 4; n++) st128(__builtin_bit_cast(u4, cs[n]), rW, (m1 && lr == 0) ? g * 16 : OOB, WS_CS1 + vq * 6912 + (4 * cA + n) * 64);
      BAR();
      waitv<0>();
      setflag(ws, WS_FRT + b * 8, tag, lane);
      STAMP(22);
#pragma nounroll
      for (u32 i = 0; i < 7; i++) BAR();
    }
    STAMP(24);
    u4 fr[2];
    ldv<0>(fr[0], rW, lane * 32, WS_FRT); ldv<16>(fr[1], rW, lane * 32, WS_FRT);
    u4 sel[2];
#pragma unroll
    for (int q = 0; q < 2; q++) {
      const u32 k = lr + 16 * q - 8 * g, one = (k & 1) ? 0x3F800000u : 0x00003F80u;
      sel[q] = u4{k < 2 ? one : 0u, (k >> 1) == 1 ? one : 0u, (k >> 1) == 2 ? one : 0u, (k >> 1) == 3 ? one : 0u};
      landed(sel[q]);
    }
    u32 dst[2];
#pragma unroll
    for (int h = 0; h < 2; h++) {
      const u32 qd = 2 * (lr + 16 * (2 * h + (g >> 1))) + (g & 1), e = (qd * 205) >> 10, dq = qd - e * 5;
      const u32 gs = dq + mt, grp = (e * 13) >> 6, ii = e - grp * 5;
      const u32 base = L_IMG + mt * 2048 + (gs & 3) * 256 + (gs >> 2) * 8;
      dst[h] = opaque(e < 20 ? base + (ii < 4 ? (4 * grp + ii) * 16 : 1024 + 4 * grp * 16) : (e == 20 ? base + 1024 + 16 : L_CB + 1792));
    }
    const bool zrow = (dst[1] & 0x430) == 0x410 && dst[1] < L_CB;
    waitv<0>();
    BAR();
    {
      landed(fr[0]); landed(fr[1]);
      const u32 tl32 = (u32)tag, th32 = (u32)(tag >> 32);
      const u32 bad = ((fr[0].x ^ tl32) | (fr[0].z ^ tl32) | (fr[1].x ^ tl32) | (fr[1].z ^ tl32)) | ((fr[0].y ^ th32) | (fr[0].w ^ th32) | (fr[1].y ^ th32) | (fr[1].w ^ th32));
      if (__any(bad != 0)) {
#pragma nounroll
        for (u32 q = 0; q < 4; q++) poll(ws, WS_FRT + (64 * q + lane) * 8, tag);
      }
    }
    sts128(L_IMG + mt * 2048 + l16, u4{0, 0, 0, 0}); sts128(L_IMG + mt * 2048 + 1024 + l16, u4{0, 0, 0, 0});
    if (lane < 12) sts128(L_CB + 1536 + l16, u4{0, 0, 0, 0});
    STAMP(4);
    poll(ws, WS_FKV + ((j * 4 + mt) * 32 + (lane & 31)) * 8, tag);
    STAMP(5);
    {
      const u32 so = WS_KVP + (j * 32 * 4 + mt) * 896;
      const u32 v0 = lane < 53 ? l16 : OOB;
      u4 a[32];
      sfor<0, 32>([&](auto Q) { ldv<(Q & 1) * 3584>(a[Q], rW, v0, so + (Q & ~1) * 3584); });
      BAR();
      f4 s0, s1, s2, s3;
      sfor<0, 16>([&](auto Q) {
        if constexpr ((Q & 1) == 0) waitv<28 - 2 * Q>();
        if constexpr (Q == 0) { mm0(s0, sel[0], a[0]); mm0(s1, sel[1], a[0]); mm0(s2, sel[0], a[1]); mm0(s3, sel[1], a[1]); }
        else { mm(s0, sel[0], a[2 * Q]); mm(s1, sel[1], a[2 * Q]); mm(s2, sel[0], a[2 * Q + 1]); mm(s3, sel[1], a[2 * Q + 1]); }
      });
      MM_DONE();
      pin(s0); pin(s1); pin(s2); pin(s3);
      s0 += s2; s1 += s3;
      const u2 v1 = {pk2(s1[0], s1[1]), pk2(s1[2], s1[3])};
      sts64(dst[0], u2{pk2(s0[0], s0[1]), pk2(s0[2], s0[3])});
      sts64(dst[1], v1);
      if (zrow) { sts64(dst[1] + 64, v1); sts64(dst[1] + 128, v1); sts64(dst[1] + 192, v1); }
      STAMP(6);
      BAR();
    }
    auto issueW1 = [&](u32 it, u32 n, u32 ld) {
      const u32 so = WS_W1T + (n < 6 ? t0 + n : tl) * 20480 + it * 5120;
      dma4_sc1(rW, l16, so, ld + n * 5120); dma1_sc1(rW, l16, so + 4096, ld + n * 5120 + 4096);
    };
    const u32 o = 51200 + mt * 12288, o2 = 51200 + 49152 + (mt & 1) * 1024, sw = WS_WOT + j * 102400;
    {
      f4 csum;
      u32 ci = lane + 64 * (mt & 1); ci = ci > 111 ? 111 : ci;
      const u32 n = ci >> 4, col = (n < 6 ? t0 + n : tl) * 16 + (ci & 15);
#pragma unroll
      for (int q = 0; q < 4; q++) csum[q] = asf(__builtin_amdgcn_raw_buffer_load_b32(rW, col * 4, WS_CS1 + q * 6912, SC1));
      const u32 bv = b1[col], b2v = ((const u32*)b2)[40 * j + (lane < 40 ? lane : 0)];
      sts32(L_CB + ci * 4, (csum[0] + csum[1]) + (csum[2] + csum[3]));
      sts32(L_CB + 512 + ci * 4, blo(bv));
      if (lane < 40) *(LAS u32*)(L_CB + 1024 + lane * 4) = b2v;
    }
    BAR();
    STAMP(9);
    auto w1t = [&](u32 it, u32 n, u32 ld) { waitv<TH>(); issueW1(it, n, ld); };
    auto w2t = [&](auto I) { waitv<TH + 3>(); ldw2(I); };
    dma4_sc1(rW, l16, sw + o, L_WO + o); waitv<TH>(); dma4_sc1(rW, l16, sw + o + 4096, L_WO + o + 4096);
    waitv<TH>(); dma4_sc1(rW, l16, sw + o + 8192, L_WO + o + 8192); dma1_sc1(rW, l16, sw + o2, L_WO + o2);
    w1t(0, mt, L_W1A);
    waitv<5>(); lset(2, mt);
    STAMP(10);
    w1t(0, mt < 3 ? mt + 4 : 6, L_W1A);
    sfor<0, HA>(w2t);
    lwait(1);
    STAMP(7);
    w1t(1, mt, L_W1B); w1t(1, mt < 3 ? mt + 4 : 6, L_W1B);
    sfor<HA, HA + 4>(w2t);
    waitv<4>(); lset(4, mt);
    STAMP(25);
    sfor<HA + 4, HB2>(w2t);
    lwait(3);
    STAMP(8);
#pragma nounroll
    for (u32 u = mt; u < 14; u += 4) { const u32 it = u >= 7 ? 1u : 0u; w1t(it + 2, u - 7 * it, L_W1B + 35840 + it * 35840); }
    waitv<0>(); lset(5, mt);
    STAMP(28);
    {
      const u32 pc = 2 * P + (mt >> 1), mc = 2 * (mt & 1);
      poll(ws, WS_FH + ((pc * 4 + mc) * 8 + lr) * 8, tag);
#pragma unroll
      for (int m = 0; m < 2; m++) {
        const u2 v0 = __builtin_amdgcn_raw_buffer_load_b64(rW, g * 256 + lr * 8, WS_ST + (pc * 4 + mc + m) * 1024, SC1);
        const u2 v1 = __builtin_amdgcn_raw_buffer_load_b64(rW, g * 256 + lr * 8 + 128, WS_ST + (pc * 4 + mc + m) * 1024, SC1);
        const float mean = xsum16_32(asf(v0.x) + asf(v1.x)) * (1.f / 640.f);
        const float var = fmaxf(xsum16_32(asf(v0.y) + asf(v1.y)) * (1.f / 640.f) - mean * mean, 0.f);
        const float m1 = rbf(mean), sd1 = rbf(__builtin_amdgcn_sqrtf(rbf(rbf(var) + 1e-5f))), r1 = __builtin_amdgcn_rcpf(sd1);
        const float m2 = rbf((mean - m1) * r1), sd2 = rbf(__builtin_amdgcn_sqrtf(rbf(rbf(var * r1 * r1) + 1e-5f)));
        const float sc = r1 * __builtin_amdgcn_rcpf(sd2);
        sts64(L_CB + 2048 + mt * 256 + m * 128 + lr * 8, u2{asu(sc), asu(sc * (m1 + m2 * sd1))});
      }
    }
    lset(6, mt);
    STAMP(27);
    sfor<HB2, 35>(w2t);
    STAMP(26);
    lwait(7);
    {
      const u32 so = WS_W2T + (j * 5 * 54 + 7 * mt) * 1024, ld = mt * 35840;
#pragma nounroll
      for (u32 n = 0; n < 5; n++) {
        waitv<TH2>();
        sfor<0, 7>([&](auto S) {
          constexpr int k = S < 2 ? S : (S < 4 ? S + 2 : (S < 6 ? S - 2 : 6));
          dma1_sc1(rW, l16, so + (n * 54 + k) * 1024, ld + (S * 5 + n) * 1024);
        });
      }
    }
    waitv<0>();
    lset(8, mt);
    STAMP(30);
  }
  {
    f4 da[4][5];
    u4 xf[7][4];
    u2 xh[4][2];
    const u32 so = WS_HID + (p * 4 * 54 + s0) * 1024;
    const u32 vh0 = w < 6 ? l8 : l16 + 2048, vh1 = w < 6 ? l8 + 512 : l16 + 2056;
    auto ldx = [&](auto U) {
      constexpr int s = U / 4, a = U % 4;
      if constexpr (s < 2) ldv<s * 1024>(xf[s][a], rW, l16, so + a * 54 * 1024);
      else if constexpr (s < 4) ldv<(s - 2) * 1024>(xf[s][a], rW, vmid, so + (a * 54 + 3) * 1024);
      else if constexpr (s == 4) ldv<2048>(xf[s][a], rW, l16, so + a * 54 * 1024);
      else if constexpr (s == 5) { ldv2<0>(xh[a][0], rW, vh0, so + (a * 54 + 3) * 1024); ldv2<0>(xh[a][1], rW, vh1, so + (a * 54 + 3) * 1024); }
      else ldv<0>(xf[s][a], rW, vlast, so + (a * 54 + 3) * 1024);
    };
    const u32 fo = WS_FHID + ((p * 16 + 2 * w) * 2 + (lane & 3)) * 16;
    sfor<0, 2>([&](auto H) {
      constexpr int u0 = H ? 16 : 0, u1 = H ? 28 : 16, XD = H ? 12 : 7;
      poll(ws, fo + H * 8, tag);
      STAMP(14 + 2 * H);
      sfor<u0, u0 + XD>(ldx);
      sfor<u0, u1>([&](auto U) {
        constexpr int u = U, s = u / 4, a = u % 4;
        constexpr int e0 = u + XD + (u >= 4 ? 4 : 0), e = e0 < u1 ? e0 : u1;
        constexpr int f0 = u + 1 + XD + (u + 1 >= 4 ? 4 : 0), f = f0 < u1 ? f0 : u1;
        constexpr int lo5 = u + 1 > 20 ? u + 1 : 20, hi5 = e < 24 ? e : 24;
        waitv<(e - u - 1) + (hi5 > lo5 ? hi5 - lo5 : 0)>();
        sfor<0, 5>([&](auto N) {
          if constexpr (s == 0) mm0(da[a][N], w2f[N], xf[s][a]);
          else if constexpr (s == 5) mmh(da[a][N], w2f[5 * s + N], xh[a][0], xh[a][1]);
          else mm(da[a][N], w2f[5 * s + N], xf[s][a]);
        });
        sfor<e, f>(ldx);
      });
    });
    MM_DONE();
    STAMP(17);
    lwait(9);
#pragma unroll
    for (int a = 0; a < 4; a++)
#pragma unroll
      for (int n = 0; n < 5; n++) {
        pin(da[a][n]);
        sts64(L_RED + ((a * 5 + n) * 8 + w) * 512 + l8, u2{pk2(da[a][n][0], da[a][n][1]), pk2(da[a][n][2], da[a][n][3])});
      }
    BAR();
    STAMP(18);
    const rsrc_t rOut = __builtin_amdgcn_make_buffer_rsrc((void*)out, 0, 2048 * 1280, 0x00020000);
#pragma nounroll
    for (u32 n = 3 * nh; n < 3 + 2 * nh; n++) {
      const u32 ra = L_RED + (mt * 5 + n) * 4096 + l8;
      f2 lo = {0, 0}, hi = {0, 0};
#pragma unroll
      for (int q = 0; q < 8; q++) { u2 v = lds64(ra + q * 512); lo += unp(v.x); hi += unp(v.y); }
      const u2 bb = lds64(L_CB + 1024 + n * 32 + g * 8), hr = lds64(L_HR + (mt * 5 + n) * 512 + l8);
      const u32 o0 = pk2(unp(hr.x) + unp(pk2(unp(pk2(lo)) + unp(bb.x)))), o1 = pk2(unp(hr.y) + unp(pk2(unp(pk2(hi)) + unp(bb.y))));
      __builtin_amdgcn_raw_buffer_store_b64(u2{o0, o1}, rOut, lr * 1280 + g * 8, (p * 64 + mt * 16) * 1280 + j * 160 + n * 32, 0);
    }
  }
}

extern "C" void kernel_launch(void* const* d_in, const int* in_sizes, int n_in, void* d_out, int out_size, void* d_ws, size_t ws_size,
                              hipStream_t stream) {
  fused_block<<<256, 512, 0, stream>>>((const u16*)d_in[0], (const u16*)d_in[1], (const u16*)d_in[2], (const u16*)d_in[4], (const u16*)d_in[5],
                                       (const u16*)d_in[6], (const u16*)d_in[7], (u16*)d_out, (char*)d_ws);
}
```

```cpp
#include <hip/hip_runtime.h>
#include <stdint.h>
#define PROF
#define PROBE 47
#define PW 0
#pragma clang diagnostic ignored "-Wint-to-pointer-cast"
#pragma clang diagnostic ignored "-Warray-bounds"

typedef unsigned short u16;
typedef unsigned u32;
typedef unsigned long long u64;
typedef float f2 __attribute__((ext_vector_type(2)));
typedef float f4 __attribute__((ext_vector_type(4)));
typedef unsigned u2 __attribute__((ext_vector_type(2)));
typedef unsigned u4 __attribute__((ext_vector_type(4)));
typedef short s4 __attribute__((ext_vector_type(4)));
typedef __bf16 bf2 __attribute__((ext_vector_type(2)));
typedef __bf16 bf8 __attribute__((ext_vector_type(8)));
typedef __amdgpu_buffer_rsrc_t rsrc_t;
#define LAS __attribute__((address_space(3)))
#define GAS __attribute__((address_space(1)))
#define DI __device__ __forceinline__

template <int I> struct IC { static constexpr int v = I; constexpr operator int() const { return I; } };
template <int B, int E, class F> DI void sfor(F&& f) {
  if constexpr (B < E) { f(IC<B>{}); sfor<B + 1, E>(f); }
}

constexpr u32 WS_FRT = 4096, WS_FKV = 8192, WS_FO = 16384, WS_FH = 24576, WS_FHID = 32768;
constexpr u32 WS_KVP = 65536;
constexpr u32 WS_OB = 1u << 20;
constexpr u32 WS_HB = WS_OB + 32 * 81920;
constexpr u32 WS_ST = WS_HB + 32 * 81920;
constexpr u32 WS_CS1 = WS_ST + 2048 * 64;
constexpr u32 WS_WOT = WS_CS1 + 32768;
constexpr u32 WS_W1T = WS_WOT + 40 * 20480;
constexpr u32 WS_W2T = WS_W1T + 108 * 20480;
constexpr u32 WS_HID = WS_W2T + 8 * 54 * 5120;
constexpr u32 WS_END = WS_HID + 32 * 4 * 54 * 1024;
constexpr u32 WS_PROF = WS_END;
constexpr u32 OOB = 0x7FFFFF00u;
constexpr int SC1 = 16;
#define MAP 1
#define ROT 2
#define WQ 6
#define E2 0
#define PR 0
#define PS 4
#define RTE 5
#define HDV 8
#define TH2 4
#define TH 8
#define HA 4
#define HB2 20
#define CW 5
#define CG 32

constexpr u32 L_X = 0, L_RING = 81920;
constexpr u32 L_WO = 0, L_IMG = 147456, L_W1A = 110592, L_HR = 147456, L_CB = 160768, L_W1B = 0, L_RED = 0;

DI f4 mfma(u4 a, u4 b, f4 c) {
  return __builtin_amdgcn_mfma_f32_16x16x32_bf16(__builtin_bit_cast(bf8, a), __builtin_bit_cast(bf8, b), c, 0, 0, 0);
}
DI void mm(f4& c, u4 a, u4 b) { asm volatile("v_mfma_f32_16x16x32_bf16 %0, %1, %2, %0" : "+v"(c) : "v"(a), "v"(b) : "memory"); }
DI void mm0(f4& c, u4 a, u4 b) { asm volatile("v_mfma_f32_16x16x32_bf16 %0, %1, %2, 0" : "=&v"(c) : "v"(a), "v"(b) : "memory"); }
#define MM_DONE() asm volatile("s_nop 7\n\ts_nop 1" ::: "memory")
DI void pin(f4& c) { asm volatile("" : "+v"(c)); }
DI void zero(f4& c) { c = f4{0, 0, 0, 0}; asm volatile("" : "+v"(c)); }
DI void undef(u4& d) { asm volatile("" : "=v"(d)); }
DI u32 uni(u32 v) { v = __builtin_amdgcn_readfirstlane(v); asm volatile("" : "+s"(v)); return v; }
DI u32 opaque(u32 v) { asm volatile("" : "+v"(v)); return v; }

DI u32 pk2(float a, float b) { f2 v = {a, b}; return __builtin_bit_cast(u32, __builtin_convertvector(v, bf2)); }
DI u32 pk2(f2 v) { return __builtin_bit_cast(u32, __builtin_convertvector(v, bf2)); }
DI float rbf(float a) { return __builtin_bit_cast(float, pk2(a, a) << 16); }
DI float blo(u32 v) { return __builtin_bit_cast(float, v << 16); }
DI float bhi(u32 v) { return __builtin_bit_cast(float, v & 0xFFFF0000u); }
DI f2 unp(u32 v) { return f2{blo(v), bhi(v)}; }
DI float asf(u32 v) { return __builtin_bit_cast(float, v); }
DI u32 asu(float v) { return __builtin_bit_cast(u32, v); }

template <int OFF> DI void dma(rsrc_t r, u32 voff, u32 soff, u32 lds) {
  asm volatile("s_mov_b32 m0, %0\n\ts_nop 0\n\tbuffer_load_dwordx4 %1, %2, %3 offen offset:%4 lds" ::"s"(lds), "v"(voff), "s"(r), "s"(soff), "n"(OFF) : "memory");
}
template <int OFF> DI void dma_c(rsrc_t r, u32 voff, u32 soff) {
  asm volatile("buffer_load_dwordx4 %0, %1, %2 offen offset:%3 lds" ::"v"(voff), "s"(r), "s"(soff), "n"(OFF) : "memory");
}
DI void dma4_sc1(rsrc_t r, u32 voff, u32 soff, u32 lds) {
  asm volatile("s_mov_b32 m0, %0\n\ts_nop 0\n\tbuffer_load_dwordx4 %1, %2, %3 offen sc1 lds\n\tbuffer_load_dwordx4 %1, %2, %3 offen offset:1024 sc1 lds\n\t"
               "buffer_load_dwordx4 %1, %2, %3 offen offset:2048 sc1 lds\n\tbuffer_load_dwordx4 %1, %2, %3 offen offset:3072 sc1 lds" ::"s"(lds), "v"(voff), "s"(r), "s"(soff) : "memory");
}
DI void dma2_sc1(rsrc_t r, u32 voff, u32 soff, u32 lds) {
  asm volatile("s_mov_b32 m0, %0\n\ts_nop 0\n\tbuffer_load_dwordx4 %1, %2, %3 offen sc1 lds\n\tbuffer_load_dwordx4 %1, %2, %3 offen offset:1024 sc1 lds" ::"s"(lds), "v"(voff), "s"(r), "s"(soff) : "memory");
}
DI void dma1_sc1(rsrc_t r, u32 voff, u32 soff, u32 lds) {
  asm volatile("s_mov_b32 m0, %0\n\ts_nop 0\n\tbuffer_load_dwordx4 %1, %2, %3 offen sc1 lds" ::"s"(lds), "v"(voff), "s"(r), "s"(soff) : "memory");
}
template <int OFF> DI void ldv(u4& d, rsrc_t r, u32 voff, u32 soff) {
  asm volatile("buffer_load_dwordx4 %0, %1, %2, %3 offen offset:%4 sc1" : "=v"(d) : "v"(voff), "s"(r), "s"(soff), "n"(OFF) : "memory");
}
template <int OFF> DI void ldv_rw(u4& d, rsrc_t r, u32 voff, u32 soff) {
  asm volatile("buffer_load_dwordx4 %0, %1, %2, %3 offen offset:%4 sc1" : "+v"(d) : "v"(voff), "s"(r), "s"(soff), "n"(OFF) : "memory");
}
template <int OFF> DI void ldp(u4& d, rsrc_t r, u32 voff, u32 soff) {
  asm volatile("buffer_load_dwordx4 %0, %1, %2, %3 offen offset:%4" : "=v"(d) : "v"(voff), "s"(r), "s"(soff), "n"(OFF) : "memory");
}
template <int OFF> DI void ldp2(u2& d, rsrc_t r, u32 voff, u32 soff) {
  asm volatile("buffer_load_dwordx2 %0, %1, %2, %3 offen offset:%4" : "=v"(d) : "v"(voff), "s"(r), "s"(soff), "n"(OFF) : "memory");
}
template <int N> DI void waitv() { asm volatile("s_waitcnt vmcnt(%0)" ::"n"(N) : "memory"); }
template <class T> DI void landed(T& d) { asm volatile("" : "+v"(d)); }
#define BAR() do { asm volatile("" ::: "memory"); __builtin_amdgcn_s_waitcnt(0xC07F); asm volatile("s_barrier" ::: "memory"); } while (0)
DI u4 lds128(u32 a) { return *(LAS u4*)(a); }
DI u2 lds64(u32 a) { return *(LAS u2*)(a); }
DI void sts128(u32 a, u4 v) { *(LAS u4*)(a) = v; }
DI void sts64(u32 a, u2 v) { *(LAS u2*)(a) = v; }
DI void sts32(u32 a, float v) { *(LAS float*)(a) = v; }
DI u4 ldstr(u32 a0, u32 a1) {
  u2 x = __builtin_bit_cast(u2, __builtin_amdgcn_ds_read_tr16_b64_v4i16((LAS s4*)(a0)));
  u2 y = __builtin_bit_cast(u2, __builtin_amdgcn_ds_read_tr16_b64_v4i16((LAS s4*)(a1)));
  return u4{x.x, x.y, y.x, y.y};
}
DI u4 ldstr(u32 a) { return ldstr(a, a + 2560); }
DI float xsum16_32(float v) {
  auto r = __builtin_amdgcn_permlane16_swap(asu(v), asu(v), false, false);
  v = asf((u32)r[0]) + asf((u32)r[1]);
  auto q = __builtin_amdgcn_permlane32_swap(asu(v), asu(v), false, false);
  return asf((u32)q[0]) + asf((u32)q[1]);
}
DI float xsum8(float v) {
  v += asf(__builtin_amdgcn_update_dpp(0, asu(v), 0xB1, 0xf, 0xf, true));
  v += asf(__builtin_amdgcn_update_dpp(0, asu(v), 0x4E, 0xf, 0xf, true));
  return v + asf(__builtin_amdgcn_update_dpp(0, asu(v), 0x141, 0xf, 0xf, true));
}
DI void poll(const char* ws, u32 off, u64 tag) {
  const GAS u64* f = (const GAS u64*)(ws + off);
#pragma nounroll
  for (u32 it = 0; it < (1u << 18); ++it) {
    u64 v = __hip_atomic_load(f, __ATOMIC_RELAXED, __HIP_MEMORY_SCOPE_AGENT);
    if (__all(v == tag)) break;
    __builtin_amdgcn_s_sleep(2);
  }
  __builtin_amdgcn_fence(__ATOMIC_ACQUIRE, "wavefront");
  asm volatile("" ::: "memory");
}
DI void lset_(u32 k, u32 mt, u32 tg) { __builtin_amdgcn_s_waitcnt(0xC07F); *(volatile LAS u32*)(L_CB + 1536 + k * 16 + mt * 4) = tg; asm volatile("" ::: "memory"); }
DI void lwait_(u32 k, u32 tg) {
  asm volatile("" ::: "memory");
  for (;;) {
    const u4 v = *(volatile LAS u4*)(L_CB + 1536 + k * 16);
    if (__builtin_amdgcn_readfirstlane((v.x ^ tg) | (v.y ^ tg) | (v.z ^ tg) | (v.w ^ tg)) == 0) break;
    __builtin_amdgcn_s_sleep(1);
  }
  asm volatile("" ::: "memory");
}
#define lset(k, mt) lset_(k, mt, (u32)tag)
#define lwait(k) lwait_(k, (u32)tag)
struct PP { u2 a, b; };
DI void ldf(u2& d, rsrc_t r, u32 voff) { asm volatile("buffer_load_dwordx2 %0, %1, %2, 0 offen sc1" : "+v"(d) : "v"(voff), "s"(r) : "memory"); }
DI void poll2(PP& pp, rsrc_t r, u32 voff, u64 tag) {
  const u32 tl = (u32)tag, th = (u32)(tag >> 32);
  asm volatile("" : "=v"(pp.a), "=v"(pp.b));
  ldf(pp.a, r, voff);
  __builtin_amdgcn_s_sleep(PS);
#pragma nounroll
  for (u32 it = 0; it < (1u << 18); ++it) {
    ldf(pp.b, r, voff);
    waitv<1>(); landed(pp.a);
    if (!__any(((pp.a.x ^ tl) | (pp.a.y ^ th)) != 0)) break;
    ldf(pp.a, r, voff);
    waitv<1>(); landed(pp.b);
    if (!__any(((pp.b.x ^ tl) | (pp.b.y ^ th)) != 0)) break;
  }
  asm volatile("" ::: "memory");
}
DI void pollend(PP& pp) { asm volatile("" ::"v"(pp.a), "v"(pp.b)); }
DI void setflag(char* ws, u32 off, u64 tag, u32 lane) {
  if (lane == 0) __hip_atomic_store((GAS u64*)(ws + off), tag, __ATOMIC_RELAXED, __HIP_MEMORY_SCOPE_AGENT);
  asm volatile("" ::: "memory");
}
template <bool PHI> DI u32 phi2(float a0, float a1, float r0, float r1) {
  const f2 a = {a0, a1}, r = {r0, r1};
  if constexpr (!PHI) return pk2(a * r);
  const f2 t = a * r + 1e-6f, u = a * (r * 1.4426950408889634f);
  return pk2(fmaxf(t[0], 1e-6f) + __builtin_amdgcn_fmed3f(__builtin_amdgcn_exp2f(u[0]), 0.f, 1.f),
             fmaxf(t[1], 1e-6f) + __builtin_amdgcn_fmed3f(__builtin_amdgcn_exp2f(u[1]), 0.f, 1.f));
}
DI float dot2c(u32 a, u32 b, float c) { return __builtin_amdgcn_fdot2_f32_bf16(__builtin_bit_cast(bf2, a), __builtin_bit_cast(bf2, b), c, false); }
DI u4 center8(u4 x, u32 lo, u32 hi, float nm) {
  u4 r; float t0, t1, t2, t3, t4, t5, t6, t7;
  asm("v_dot2_f32_bf16 %4, %12, %16, %18\n\tv_dot2_f32_bf16 %5, %12, %17, %18\n\tv_dot2_f32_bf16 %6, %13, %16, %18\n\tv_dot2_f32_bf16 %7, %13, %17, %18\n\t"
      "v_dot2_f32_bf16 %8, %14, %16, %18\n\tv_dot2_f32_bf16 %9, %14, %17, %18\n\tv_dot2_f32_bf16 %10, %15, %16, %18\n\tv_dot2_f32_bf16 %11, %15, %17, %18\n\t"
      "v_cvt_pk_bf16_f32 %0, %4, %5\n\tv_cvt_pk_bf16_f32 %1, %6, %7\n\tv_cvt_pk_bf16_f32 %2, %8, %9\n\tv_cvt_pk_bf16_f32 %3, %10, %11"
      : "=v"(r.x), "=v"(r.y), "=v"(r.z), "=v"(r.w), "=&v"(t0), "=&v"(t1), "=&v"(t2), "=&v"(t3), "=&v"(t4), "=&v"(t5), "=&v"(t6), "=&v"(t7)
      : "v"(x.x), "v"(x.y), "v"(x.z), "v"(x.w), "v"(lo), "v"(hi), "v"(nm));
  return r;
}
template <int N> DI void waitl() { asm volatile("" ::: "memory"); __builtin_amdgcn_s_waitcnt(0xC07F | (N << 8)); asm volatile("" ::: "memory"); }
DI void st128(u4 v, rsrc_t r, u32 voff, u32 soff) {
  __builtin_amdgcn_raw_buffer_store_b128(v, r, voff, soff, SC1);
  asm volatile("s_nop 1" ::"v"(v) : "memory");
}
DI void st64(u2 v, rsrc_t r, u32 voff, u32 soff) { __builtin_amdgcn_raw_buffer_store_b64(v, r, voff, soff, SC1); }
DI void publish20(const u32* v, rsrc_t r, u32 l16, u32 so, u32 j) {
  if ((j & 1) == 0) {
    st128(u4{v[0], v[1], v[2], v[3]}, r, l16, so);
    st128(u4{v[4], v[5], v[6], v[7]}, r, l16, so + 1024);
    st64(u2{v[8], v[9]}, r, l16 >> 1, so + 2048);
  } else {
    st64(u2{v[0], v[1]}, r, l16 >> 1, so + 2560);
    st128(u4{v[2], v[3], v[4], v[5]}, r, l16, so + 3072);
    st128(u4{v[6], v[7], v[8], v[9]}, r, l16, so + 4096);
  }
}
template <int OFF> DI void ldv2(u2& d, rsrc_t r, u32 voff, u32 soff) {
  asm volatile("buffer_load_dwordx2 %0, %1, %2, %3 offen offset:%4 sc1" : "=v"(d) : "v"(voff), "s"(r), "s"(soff), "n"(OFF) : "memory");
}
DI void mmh(f4& c, u4 a, u2 b0, u2 b1) { mm(c, a, u4{b0.x, b0.y, b1.x, b1.y}); }
template <int V, int L> DI void waitvl() {
  asm volatile("" ::: "memory"); __builtin_amdgcn_s_waitcnt(0x0070 | (V & 15) | ((V >> 4) << 14) | (L << 8)); asm volatile("" ::: "memory");
}

#define PROFW 3
#define TIMEOP "s_memrealtime"
#define STAMP(i) do { u64 t_; asm volatile(TIMEOP " %0\n\ts_waitcnt lgkmcnt(0)" : "=s"(t_) :: "memory"); asm volatile("s_nop 0\n\tv_writelane_b32 %0, %1, %2" : "+v"(tsv) : "s"((u32)t_), "n"(i)); } while (0)

__global__ __launch_bounds__(512, 2) void fused_block(const u16* __restrict__ x, const u16* __restrict__ wqkv, const u16* __restrict__ wo,
                                                      const u16* __restrict__ w1, const u16* __restrict__ b1, const u16* __restrict__ w2,
                                                      const u16* __restrict__ b2, u16* __restrict__ out, char* __restrict__ ws) {
  __shared__ __attribute__((aligned(1024))) char smem[163840];
  asm volatile("" ::"v"((u32)(uintptr_t)(LAS char*)smem));
  u32 tsv = 0;
  STAMP(47);
  const u32 tid = threadIdx.x, lane = tid & 63, lr = lane & 15, g = lane >> 4;
  const u32 w = __builtin_amdgcn_readfirstlane(tid >> 6), mt = w & 3, nh = w >> 2;
  const bool helper = nh != 0;
  const u32 b = blockIdx.x, xg = b & 7, rr = b >> 3, p = 8 * (xg >> 1) + (rr >> 2), j = 4 * (xg & 1) + (rr & 3);
  asm volatile("" ::"s"(x), "s"(wqkv), "s"(wo), "s"(w1), "s"(b1), "s"(w2), "s"(b2), "s"(out), "s"(ws));
  STAMP(0);
  const rsrc_t rW = __builtin_amdgcn_make_buffer_rsrc((void*)ws, 0, WS_END, 0x00020000);
  const u32 l16 = lane * 16, l8 = lane * 8;
  u2 ep;
  u64 tag;
#define LD_EPOCH() asm volatile("buffer_load_dwordx2 %0, off, %1, 0 sc1" : "=v"(ep) : "s"(rW) : "memory")
#define GET_TAG() do { u32 lo_, hi_; asm volatile("v_readfirstlane_b32 %0, %2\n\tv_readfirstlane_b32 %1, %3\n\ts_nop 3" : "=s"(lo_), "=s"(hi_) : "v"(ep.x), "v"(ep.y) : "memory"); \
    tag = (((u64)hi_ << 32) | lo_) + 1; } while (0)
  u32 rot = xg + 3 * (p & 3), rotq = xg + (p & 3);
  rot = rot >= 10 ? rot - 10 : rot; rotq = rotq >= 10 ? rotq - 10 : (rotq >= 5 ? rotq - 5 : rotq);
  const u32 P = p >> 1, c = ((p & 1) << 3) | j, rho = c >> 1, odd = c & 1;
  const u32 t0 = rho < 6 ? 14 * rho + 8 * odd : 12 * rho + 12 + 6 * odd, tl = rho < 6 ? 14 * rho + 6 + odd : t0;
  const u32 s0 = w < 6 ? 7 * w : 6 * w + 6;
  const u32 vlast = w >= 6 ? OOB : l16 + 3072, vmid = w >= 6 ? l16 : l16 + 1024, vsp = w >= 6 ? l16 + 2048 : l16, soW2 = WS_W2T + (j * 5 * 54 + s0) * 1024;
  u4 w2f[35];
  auto ldw2 = [&](auto I) {
    constexpr int s = I / 5, n = I % 5;
    if constexpr (s < 2) ldv<s * 1024>(w2f[I], rW, l16, soW2 + n * 54 * 1024);
    else if constexpr (s < 4) ldv<(s - 2) * 1024>(w2f[I], rW, vmid, soW2 + (n * 54 + 3) * 1024);
    else if constexpr (s == 4) ldv<2048>(w2f[I], rW, l16, soW2 + n * 54 * 1024);
    else ldv<0>(w2f[I], rW, s == 5 ? vsp : vlast, soW2 + (n * 54 + 3) * 1024);
  };
#define RT_CHECK() do { waitv<0>(); landed(fr[0]); landed(fr[1]); const u32 tl32 = (u32)tag, th32 = (u32)(tag >> 32); \
    const u32 bad = ((fr[0].x ^ tl32) | (fr[0].z ^ tl32) | (fr[1].x ^ tl32) | (fr[1].z ^ tl32)) | ((fr[0].y ^ th32) | (fr[0].w ^ th32) | (fr[1].y ^ th32) | (fr[1].w ^ th32)); \
    if (__any(bad != 0)) { _Pragma("nounroll") for (u32 q = 0; q < 4; q++) poll(ws, WS_FRT + (64 * q + lane) * 8, tag); } } while (0)
  constexpr u32 L_RS = L_RING + 4096;

  if (!helper) {
    const rsrc_t rX = __builtin_amdgcn_make_buffer_rsrc((void*)x, 0, 2048 * 1280, 0x00020000);
    float rsd[2];
    const u32 ONE2 = opaque(0x3F803F80u);
    {
      u4 xr[2][10];
      const u32 vx = (lane >> 3) * 1280 + (lane & 7) * 16, sx = (p * 64 + 16 * mt) * 1280;
      sfor<0, 10>([&](auto I) { ldp<I * 128>(xr[0][I], rX, vx, sx); });
      sfor<0, 10>([&](auto I) { ldp<I * 128>(xr[1][I], rX, vx, sx + 8 * 1280); });
      LD_EPOCH();
      const u32 wa = L_X + mt * 2048 + (lane >> 3) * 128 + (((lane & 7) ^ (lane >> 3)) << 4);
      const u32 lo = opaque(0x00003F80u), hi = opaque(0x3F800000u);
      float dm[2];
      sfor<0, 2>([&](auto G) {
        waitv<(G == 0 ? 11 : 1)>();
        STAMP(20 + G);
        float s0 = 0.f, s1 = 0.f;
#pragma unroll
        for (int i = 0; i < 10; i++) {
          landed(xr[G][i]);
          s0 = dot2c(xr[G][i].x, ONE2, s0); s1 = dot2c(xr[G][i].y, ONE2, s1); s0 = dot2c(xr[G][i].z, ONE2, s0); s1 = dot2c(xr[G][i].w, ONE2, s1);
        }
        const float m = xsum8(s0 + s1) * (1.f / 640.f), nm = -rbf(m);
        dm[G] = m + nm;
#pragma unroll
        for (int i = 0; i < 10; i++) { xr[G][i] = center8(xr[G][i], lo, hi, nm); sts128(wa + G * 1024 + i * 8192, xr[G][i]); }
      });
      sfor<0, 2>([&](auto G) {
        float s0 = 0.f, s1 = 0.f;
#pragma unroll
        for (int i = 0; i < 10; i++) {
          const u4 v = xr[G][i];
          s0 = dot2c(v.x, v.x, s0); s1 = dot2c(v.y, v.y, s1); s0 = dot2c(v.z, v.z, s0); s1 = dot2c(v.w, v.w, s1);
        }
        const float var = fmaxf(xsum8(s0 + s1) * (1.f / 640.f) - dm[G] * dm[G], 0.f);
        rsd[G] = __builtin_amdgcn_rcpf(rbf(__builtin_amdgcn_sqrtf(rbf(rbf(var) + 1e-5f))));
        landed(rsd[G]);
      });
    }
    STAMP(22);
    waitv<0>();
    GET_TAG();
    const u32 xa0 = L_X + (lr >> 3) * 1024 + (lr & 7) * 128 + ((g ^ (lr & 7)) << 4);
    const u32 tr0 = L_RING + (4 * g + (lr >> 2)) * 160 + (lr & 3) * 8;
    const u32 mh = mt & 1, nq = mt >> 1;
    float qrs;
    u4 xq[20];
    {
      f4 acc[2][5];
#pragma unroll
      for (int i = 0; i < 5; i++) { zero(acc[0][i]); zero(acc[1][i]); }
      const u32 bA = tr0 + nq * 96, bC = tr0 + 64 + nq * 5120;
      const u32 xs0 = xa0 + mh * 4096;
      auto half = [&](u4* cur, u4* nxt, u32 xa, u32 a, u32 c, auto KS, auto LD) {
        constexpr int o = KS * 10240;
        mm(acc[0][0], cur[5], cur[0]);
        if constexpr (LD) nxt[5] = lds128(xa);
        mm(acc[1][0], cur[6], cur[0]);
        if constexpr (LD) nxt[6] = lds128(xa + 2048);
        mm(acc[0][1], cur[5], cur[1]);
        if constexpr (LD) nxt[0] = ldstr(a + o);
        mm(acc[1][1], cur[6], cur[1]);
        if constexpr (LD) nxt[1] = ldstr(a + o + 32);
        mm(acc[0][2], cur[5], cur[2]);
        if constexpr (LD) nxt[2] = ldstr(c + o);
        mm(acc[1][2], cur[6], cur[2]);
        if constexpr (LD) nxt[3] = ldstr(a + o + 5120);
        mm(acc[0][3], cur[5], cur[3]);
        if constexpr (LD) nxt[4] = ldstr(a + o + 5152);
        mm(acc[1][3], cur[6], cur[3]);
        mm(acc[0][4], cur[5], cur[4]); mm(acc[1][4], cur[6], cur[4]);
      };
      u4 fa[7], fb[7];
      u32 st = rot, xa = xs0 + st * 8192, a = bA, cc = bC;
      BAR();
      STAMP(1);
      fa[5] = lds128(xa); fa[6] = lds128(xa + 2048); fa[0] = ldstr(a); fa[1] = ldstr(a + 32); fa[2] = ldstr(cc);
      fa[3] = ldstr(a + 5120); fa[4] = ldstr(a + 5152);
      u32 lkv[6];
#pragma unroll
      for (int q = 0; q < 2; q++) {
        const u32 dq = 4 * q + g - mt, e0 = lr - 4 * mt, e1 = e0 + 16, sb = L_RING + mt * 5120;
        lkv[3 * q] = opaque(sb + ((dq < 5 && e0 < 20) ? e0 * 40 + dq * 8 : 896));
        lkv[3 * q + 1] = opaque(sb + ((dq < 5 && e1 < 20) ? e1 * 40 + dq * 8 : 896));
        lkv[3 * q + 2] = opaque(sb + ((dq < 5 && lr == 0) ? 800 + dq * 8 : (dq == 0 && lr == 1) ? 840 : 896));
      }
#pragma nounroll
      for (u32 i = 1; i < 10; i++) {
        waitl<0>();
        half(fa, fb, xa ^ 64, a, cc, IC<1>{}, IC<1>{});
        st = st + 1 >= 10 ? 0 : st + 1;
        xa = opaque(xs0 + st * 8192); a = opaque(bA + (i & 3) * 20480); cc = opaque(bC + (i & 3) * 20480);
        BAR();
        half(fb, fa, xa, a, cc, IC<0>{}, IC<1>{});
      }
      if ((lane & 7) == 0) { sts32(L_RS + mt * 64 + (lane >> 3) * 4, rsd[0]); sts32(L_RS + mt * 64 + 32 + (lane >> 3) * 4, rsd[1]); }
      waitl<0>();
      half(fa, fb, xa ^ 64, a, cc, IC<1>{}, IC<1>{});
      {
        u32 qs = rotq;
#pragma unroll
        for (int q = 0; q < 5; q++) {
          const u32 xb = xa0 + mt * 2048 + qs * 16384, xc = xb ^ 64;
          xq[4 * q] = lds128(xb); xq[4 * q + 1] = lds128(xc); xq[4 * q + 2] = lds128(xb + 8192); xq[4 * q + 3] = lds128(xc + 8192);
          qs = qs + 1 >= 5 ? 0 : qs + 1;
        }
      }
      BAR();
      const f4 r0 = __builtin_bit_cast(f4, lds128(L_RS + mh * 128 + g * 16)), r1 = __builtin_bit_cast(f4, lds128(L_RS + mh * 128 + 64 + g * 16));
      qrs = *(LAS float*)(L_RS + mt * 64 + lr * 4);
      half(fb, fa, 0, 0, 0, IC<0>{}, IC<0>{});
      MM_DONE();
      __builtin_amdgcn_s_setprio(PR);
      STAMP(2);
      const u32 EX = L_RING + 20480;
      const u32 ew = EX + mh * 10240 + nq * 3072 + l16;
      auto put = [&](auto N, auto PHI, u32 a) {
        constexpr int n = N;
        pin(acc[0][n]); pin(acc[1][n]);
        sts128(a, u4{phi2<PHI>(acc[0][n][0], acc[0][n][1], r0[0], r0[1]), phi2<PHI>(acc[0][n][2], acc[0][n][3], r0[2], r0[3]),
                     phi2<PHI>(acc[1][n][0], acc[1][n][1], r1[0], r1[1]), phi2<PHI>(acc[1][n][2], acc[1][n][3], r1[2], r1[3])});
      };
      put(IC<0>{}, IC<1>{}, ew); put(IC<1>{}, IC<1>{}, ew + 1024); put(IC<3>{}, IC<0>{}, ew + 5120); put(IC<4>{}, IC<0>{}, ew + 6144);
      if (nq == 0) put(IC<2>{}, IC<1>{}, ew + 2048); else put(IC<2>{}, IC<0>{}, ew + 4096);
      STAMP(41);
      BAR();
      STAMP(42);
      const u32 ex = EX + mt * 1024 + l16;
      u4 kf[2][2], vf[2][2];
#pragma unroll
      for (int m = 0; m < 2; m++) {
        kf[m][0] = lds128(ex + m * 10240); vf[m][0] = lds128(ex + m * 10240 + 5120);
        vf[m][1] = lds128(ex + m * 10240 + 6144); kf[m][1] = lds128(ex + m * 10240 + 1024);
      }
      const u32 o0 = lr == 0 ? ONE2 : 0u;
      const u4 ONE0 = {o0, o0, o0, o0};
#pragma unroll
      for (int q = 0; q < 2; q++) {
        f4 z4 = {0, 0, 0, 0};
        f4 kv0 = mfma(kf[1][q], vf[1][0], mfma(kf[0][q], vf[0][0], z4)), kv1 = mfma(kf[1][q], vf[1][1], mfma(kf[0][q], vf[0][1], z4));
        f4 zz = mfma(kf[1][q], ONE0, mfma(kf[0][q], ONE0, z4));
        sts64(lkv[3 * q], u2{pk2(kv0[0], kv0[1]), pk2(kv0[2], kv0[3])});
        sts64(lkv[3 * q + 1], u2{pk2(kv1[0], kv1[1]), pk2(kv1[2], kv1[3])});
        sts64(lkv[3 * q + 2], u2{pk2(zz[0], zz[1]), pk2(zz[2], zz[3])});
      }
      asm volatile("" ::: "memory");
      st128(lds128(L_RING + mt * 5120 + l16), rW, lane < 56 ? l16 : OOB, WS_KVP + ((j * 32 + p) * 4 + mt) * 896);
    }
    u32 pq[5][2];
    u2 xres[5];
    {
      const u32 qa0 = opaque(tr0 + 2 * 20480), qa1 = opaque(tr0 - L_RING);
      u4 wf[2][5];
#pragma unroll
      for (int n = 0; n < 5; n++) wf[0][n] = ldstr(qa0 + n * 32);
      waitv<0>();
      setflag(ws, WS_FKV + ((j * 4 + mt) * 32 + p) * 8, tag, lane);
      STAMP(3);
      sfor<0, 5>([&](auto N) { ldp2<N * 32>(xres[N], rX, lr * 1280 + g * 8, (p * 64 + mt * 16) * 1280 + j * 160); });
      f4 qa[5];
      sfor<0, 20>([&](auto S) {
        constexpr int s = S, t = s + 1;
        if constexpr (s == 7) BAR();
        waitl<6>();
        sfor<0, 5>([&](auto N) {
          constexpr int n = N;
          if constexpr (n == 2) waitl<(s < 19 ? 4 : 0)>();
          if constexpr (s == 0) mm0(qa[n], wf[0][n], xq[0]); else mm(qa[n], wf[s & 1][n], xq[s]);
          if constexpr (s < 19) wf[t & 1][n] = ldstr((t < 8 ? qa0 + t * 5120 : qa1 + (t - 8) * 5120) + n * 32);
        });
      });
      MM_DONE();
      STAMP(4);
#pragma unroll
      for (int n = 0; n < 5; n++) {
        pin(qa[n]);
        pq[n][0] = opaque(phi2<1>(qa[n][0], qa[n][1], qrs, qrs)); pq[n][1] = opaque(phi2<1>(qa[n][2], qa[n][3], qrs, qrs));
      }
    }
    STAMP(5);
    __builtin_amdgcn_s_setprio(2);
    STAMP(23);
    BAR();
    STAMP(6);
    {
      float o[20];
#pragma unroll
      for (int h = 0; h < 4; h++) {
        u4 bq = {pq[h][0], pq[h][1], pq[h + 1][0], pq[h + 1][1]};
        f4 z4 = {0, 0, 0, 0};
        f4 d0 = mfma(lds128(L_IMG + h * 2048 + l16), bq, z4);
        f4 d1 = mfma(lds128(L_IMG + h * 2048 + 1024 + l16), bq, z4);
        float rd = __builtin_amdgcn_rcpf(d1[1] + 1e-6f);
#pragma unroll
        for (int i = 0; i < 4; i++) o[5 * h + i] = d0[i] * rd;
        o[5 * h + 4] = d1[0] * rd;
      }
      u32 ov[10];
#pragma unroll
      for (int i = 0; i < 10; i++) ov[i] = pk2(o[2 * i], o[2 * i + 1]);
      publish20(ov, rW, l16, WS_OB + ((p * 4 + mt) * 20 + 5 * (j >> 1)) * 1024, j);
    }
    waitv<0>();
    setflag(ws, WS_FO + ((p * 4 + mt) * 8 + j) * 8, tag, lane);
    BAR();
    STAMP(7);
    {
      u4 of[2][10], wf[5];
      u2 oh[2][2][2];
      const u32 kh = mt & 1;
      const u32 so[2] = {WS_OB + (p * 4 + mt) * 20480 + kh * 5120, WS_OB + (p * 4 + (mt ^ 1)) * 20480 + kh * 5120};
      auto ldo = [&](auto S) {
        constexpr int s = S;
        sfor<0, 2>([&](auto M) {
          if constexpr (s % 5 == 2) { ldv2<2048>(oh[M][s / 5][0], rW, l8, so[M] + (s / 5) * 10240); ldv2<2560>(oh[M][s / 5][1], rW, l8, so[M] + (s / 5) * 10240); }
          else ldv<(s % 5 < 4 ? (s % 5) * 1024 : 0)>(of[M][s], rW, l16, so[M] + (s / 5) * 10240 + (s % 5 < 4 ? 0 : 4096));
        });
      };
      const u32 a0 = opaque(L_WO + kh * 5120 + l16), a1 = opaque(L_WO + 51200 + kh * 5120 + l16);
#pragma unroll
      for (int n = 0; n < 5; n++) wf[n] = lds128(a0 + n * 10240);
      PP pp;
      poll2(pp, rW, WS_FO + ((p * 4 + (mt & 2) + ((lane >> 2) & 1)) * 8 + 2 * kh + (lane & 1) + 2 * (lane & 2)) * 8, tag);
      STAMP(8);
      sfor<0, 3>(ldo);
      f4 ha[2][5];
      sfor<0, 10>([&](auto S) {
        constexpr int s = S, e = 3 + 2 * s < 10 ? 3 + 2 * s : 10, f = 5 + 2 * s < 10 ? 5 + 2 * s : 10;
        constexpr int yo = 2 * ((e - s - 1) + ((e + 2) / 5 - (s + 3) / 5));
        if constexpr (s == 4) { STAMP(38); lset(1, mt); lwait(2); STAMP(39); }
        waitvl<yo, 2>();
        if constexpr (s == 0) { pollend(pp); STAMP(37); }
        if constexpr (s == 8) lwait(1);
        sfor<0, 5>([&](auto N) {
          constexpr int n = N, t = s + 1;
          if constexpr (n == 3) waitl<(s < 9 ? 3 : 0)>();
          sfor<0, 2>([&](auto M) {
            if constexpr (s == 0) mm0(ha[M][n], wf[n], of[M][s]);
            else if constexpr (s % 5 == 2) mmh(ha[M][n], wf[n], oh[M][s / 5][0], oh[M][s / 5][1]);
            else mm(ha[M][n], wf[n], of[M][s]);
          });
          if constexpr (s < 9) wf[n] = lds128((t < 5 ? a0 : a1) + n * 10240 + (t % 5) * 1024);
          if constexpr (n == 1 && e < f) ldo(IC<e>{});
          if constexpr (n == 3 && e + 1 < f) ldo(IC<e + 1>{});
        });
      });
      MM_DONE();
      STAMP(40);
      {
        const u32 xw = (mt < 3 ? 35840 + mt * 5120 : 102400) + l16, xr = ((mt ^ 1) < 3 ? 35840 + (mt ^ 1) * 5120 : 102400) + l16;
#pragma unroll
        for (int n = 0; n < 5; n++) { pin(ha[1][n]); sts128(xw + n * 1024, __builtin_bit_cast(u4, ha[1][n])); }
        waitl<0>();
        *(volatile LAS u32*)(L_CB + 1536 + mt * 4) = (u32)tag;
        while (*(volatile LAS u32*)(L_CB + 1536 + (mt ^ 1) * 4) != (u32)tag) {}
        asm volatile("" ::: "memory");
#pragma unroll
        for (int n = 0; n < 5; n++) { pin(ha[0][n]); ha[0][n] += __builtin_bit_cast(f4, lds128(xr + n * 1024)); }
      }
      STAMP(9);
      u32 hres[10];
      float sum = 0.f, sq = 0.f;
#pragma unroll
      for (int n = 0; n < 5; n++) {
        hres[2 * n] = pk2(unp(xres[n].x) + f2{ha[0][n][0], ha[0][n][1]});
        hres[2 * n + 1] = pk2(unp(xres[n].y) + f2{ha[0][n][2], ha[0][n][3]});
      }
      publish20(hres, rW, l16, WS_HB + ((p * 4 + mt) * 20 + 5 * (j >> 1)) * 1024, j);
#pragma unroll
      for (int i = 0; i < 10; i++) { sum = dot2c(hres[i], ONE2, sum); sq = dot2c(hres[i], hres[i], sq); }
      st64(u2{asu(xsum16_32(sum)), asu(xsum16_32(sq))}, rW, g == 0 ? l8 : OOB, WS_ST + ((p * 4 + mt) * 8 + j) * 128);
#pragma unroll
      for (int n = 0; n < 5; n++) sts64(L_HR + (mt * 5 + n) * 512 + l8, u2{hres[2 * n], hres[2 * n + 1]});
      waitv<0>();
      setflag(ws, WS_FH + ((p * 4 + mt) * 8 + j) * 8, tag, lane);
      lset(3, mt);
      STAMP(10);
      lwait(4);
    }
    {
      constexpr int HD = HDV;
      const u32 pc = 2 * P + (mt >> 1), mc = 2 * (mt & 1);
      f4 ca[2][7];
      u4 hf[2][20], wf[7];
      u2 hh[2][4][2];
      const u32 so = WS_HB + (pc * 4 + mc) * 20480;
      auto ldh = [&](auto S) {
        constexpr int s = S;
        sfor<0, 2>([&](auto M) {
          if constexpr (s % 5 == 2) {
            ldv2<(s & 3) * 1024>(hh[M][s / 5][0], rW, l8, so + M * 20480 + (s & ~3) * 1024);
            ldv2<(s & 3) * 1024 + 512>(hh[M][s / 5][1], rW, l8, so + M * 20480 + (s & ~3) * 1024);
          } else ldv<(s & 3) * 1024>(hf[M][s], rW, l16, so + M * 20480 + (s & ~3) * 1024);
        });
      };
      const u32 a0 = opaque(L_W1A + l16), a1 = opaque(L_W1B + l16), a2 = opaque(L_W1B + 35840 + l16), a3 = opaque(L_W1B + 71680 + l16);
#pragma unroll
      for (int n = 0; n < 7; n++) wf[n] = lds128(a0 + n * 5120);
      PP pp;
      poll2(pp, rW, WS_FH + ((pc * 4 + mc) * 8 + lr) * 8, tag);
      STAMP(11);
      sfor<0, 3>(ldh);
      sfor<0, 20>([&](auto S) {
        constexpr int s = S, e1 = 3 + 2 * s < s + HD ? 3 + 2 * s : s + HD, e = e1 < 20 ? e1 : 20;
        constexpr int f1 = 5 + 2 * s < s + 1 + HD ? 5 + 2 * s : s + 1 + HD, f = f1 < 20 ? f1 : 20;
        constexpr int yo = 2 * ((e - s - 1) + ((e + 2) / 5 - (s + 3) / 5));
        if constexpr (s == 9) { STAMP(33); lwait(5); STAMP(34); }
        waitvl<yo, 3>();
        if constexpr (s == 0) { pollend(pp); STAMP(31); }
        if constexpr (s == 5) STAMP(32);
        if constexpr (s == 14) STAMP(35);
        sfor<0, 7>([&](auto N) {
          constexpr int n = N, t = s + 1;
          if constexpr (n == 4) waitl<(s < 19 ? 4 : 0)>();
          sfor<0, 2>([&](auto M) {
            if constexpr (s == 0) mm0(ca[M][n], wf[n], hf[M][s]);
            else if constexpr (s % 5 == 2) mmh(ca[M][n], wf[n], hh[M][s / 5][0], hh[M][s / 5][1]);
            else mm(ca[M][n], wf[n], hf[M][s]);
          });
          if constexpr (s < 19) wf[n] = lds128((t < 5 ? a0 : t < 10 ? a1 : t < 15 ? a2 : a3) + n * 5120 + (t % 5) * 1024);
          if constexpr (n == 1 && e < f) ldh(IC<e>{});
          if constexpr (n == 4 && e + 1 < f) ldh(IC<e + 1>{});
        });
      });
      MM_DONE();
      STAMP(36);
      lset(7, mt);
      lwait(6);
      STAMP(12);
      const u32 sh = WS_HID + (pc * 4 + mc) * 54 * 1024, fh = WS_FHID + (((pc * 16 + c) * 2 + (mt & 1)) * 2) * 8;
      const u2 s0v = lds64(L_CB + 2048 + mt * 256 + lr * 8), s1v = lds64(L_CB + 2048 + mt * 256 + 128 + lr * 8);
      const float sc[2] = {asf(s0v.x), asf(s1v.x)}, sm[2] = {asf(s0v.y), asf(s1v.y)};
      u32 hp[2][2][2];
      sfor<0, 7>([&](auto N) {
        constexpr int n = N;
        const f4 c4 = __builtin_bit_cast(f4, lds128(L_CB + n * 64 + g * 16)), b4 = __builtin_bit_cast(f4, lds128(L_CB + 512 + n * 64 + g * 16));
        if constexpr (n == 6) { waitv<2>(); setflag(ws, fh, tag, lane); STAMP(15); }
        sfor<0, 2>([&](auto M) {
          pin(ca[M][n]);
#pragma unroll
          for (int i = 0; i < 2; i++) {
            const f2 v = f2{ca[M][n][2 * i], ca[M][n][2 * i + 1]} * sc[M] + (f2{b4[2 * i], b4[2 * i + 1]} - sm[M] * f2{c4[2 * i], c4[2 * i + 1]});
            const f2 u = v * (-2.302208198f - 0.1029432397f * v * v);
            const f2 d = f2{__builtin_amdgcn_exp2f(u[0]), __builtin_amdgcn_exp2f(u[1])} + 1.f;
            hp[M][n & 1][i] = pk2(v * f2{__builtin_amdgcn_rcpf(d[0]), __builtin_amdgcn_rcpf(d[1])});
          }
          if constexpr (n & 1) st128(u4{hp[M][0][0], hp[M][0][1], hp[M][1][0], hp[M][1][1]}, rW, l16, sh + M * 54 * 1024 + ((t0 >> 1) + n / 2) * 1024);
          if constexpr (n == 6) st64(u2{hp[M][0][0], hp[M][0][1]}, rW, rho < 6 ? l8 + odd * 512 : OOB, sh + M * 54 * 1024 + (tl >> 1) * 1024);
        });
      });
      STAMP(29);
      waitv<0>();
      setflag(ws, fh + 8, tag, lane);
      __builtin_amdgcn_s_setprio(0);
      STAMP(13);
      lwait(8);
      const u32 wa = opaque(mt * 35840 + l16);
#pragma unroll
      for (int i = 0; i < 35; i++) w2f[i] = lds128(wa + i * 1024);
      lset(9, mt);
    }
  } else {
    STAMP(20);
    LD_EPOCH();
    u4 sel[2];
    u32 dst[2];
    auto setup = [&]() {
#pragma unroll
    for (int q = 0; q < 2; q++) {
      const u32 k = lr + 16 * q - 8 * g, one = (k & 1) ? 0x3F800000u : 0x00003F80u;
      sel[q] = u4{k < 2 ? one : 0u, (k >> 1) == 1 ? one : 0u, (k >> 1) == 2 ? one : 0u, (k >> 1) == 3 ? one : 0u};
      landed(sel[q]);
    }
#pragma unroll
    for (int h = 0; h < 2; h++) {
      const u32 qd = 2 * (lr + 16 * (2 * h + (g >> 1))) + (g & 1), e = (qd * 205) >> 10, dq = qd - e * 5;
      const u32 gs = dq + mt, grp = (e * 13) >> 6, ii = e - grp * 5;
      const u32 base = L_IMG + mt * 2048 + (gs & 3) * 256 + (gs >> 2) * 8;
      dst[h] = opaque(e < 20 ? base + (ii < 4 ? (4 * grp + ii) * 16 : 1024 + 4 * grp * 16) : (e == 20 ? base + 1024 + 16 : L_CB + 1792));
    }
    };
    if (mt < 3) {
      const rsrc_t rQ = __builtin_amdgcn_make_buffer_rsrc((void*)((const char*)wqkv - 4096), 0, 640 * 3840 + 4096, 0x00020000);
      u32 voffW[7];
#pragma unroll
      for (int i = 0; i < 7; i++) {
        const u32 pi = i < 5 ? i : (i == 5 ? mt : (mt < 2 ? mt + 3 : 4u));
        const u32 f = 64 * pi + lane, sg = (f * 6554) >> 16, ch = f - sg * 10;
        voffW[i] = (((sg & 12) << 1) + ((sg >> 4) << 2) + (sg & 3)) * 3840 + ch * 16 + 4096 - (i < 4 ? i * 1024 : 0);
      }
      const u32 soW = (mt >> 1) * 32 * 3840 + ((mt & 1) + 1) * 1280 + j * 160, soW3 = 32 * 3840 + 2560 + j * 160;
      const u32 soQ = mt * 32 * 3840 + j * 160, soQ3 = 96 * 3840 + j * 160;
      const u32 l5 = 15360 + mt * 1024, l6 = 15360 + (mt < 2 ? mt + 3 : 4u) * 1024;
      u32 st = rot, qs = rotq;
      auto issueW = [&](u32 so, u32 so3, u32 ld) {
        dma<0>(rQ, voffW[0], so, ld + mt * 5120); dma_c<1024>(rQ, voffW[1], so); dma_c<2048>(rQ, voffW[2], so); dma_c<3072>(rQ, voffW[3], so);
        dma<0>(rQ, voffW[4], so, ld + mt * 5120 + 4096); dma<0>(rQ, voffW[5], so3, ld + l5);
        if (mt < 2) dma<0>(rQ, voffW[6], so3, ld + l6);
      };
      auto issueKV = [&](u32 slot) { issueW(soW + st * 245760, soW3 + st * 245760, L_RING + slot * 20480); st = st + 1 >= 10 ? 0 : st + 1; };
      auto issueQ = [&](u32 ld) { issueW(soQ + qs * 491520, soQ3 + qs * 491520, ld); qs = qs + 1 >= 5 ? 0 : qs + 1; };
#pragma nounroll
      for (u32 i = 0; i < 4; i++) issueKV(i);
      STAMP(21);
      waitv<18>();
      BAR();
#pragma nounroll
      for (u32 i = 0; i < 6; i++) { waitv<12>(); asm volatile("s_barrier" ::: "memory"); issueKV(i & 3); }
      auto issueQa = [&](u32 slot) {
        const u32 so = soQ + qs * 491520;
        dma<0>(rQ, voffW[0], so, L_RING + slot * 20480 + mt * 5120); dma_c<1024>(rQ, voffW[1], so); dma_c<2048>(rQ, voffW[2], so);
      };
      auto issueQb = [&](u32 slot) {
        const u32 so = soQ + qs * 491520, so3 = soQ3 + qs * 491520, ld = L_RING + slot * 20480;
        dma<3072>(rQ, voffW[3], so, ld + mt * 5120); dma<0>(rQ, voffW[4], so, ld + mt * 5120 + 4096); dma<0>(rQ, voffW[5], so3, ld + l5);
        if (mt < 2) dma<0>(rQ, voffW[6], so3, ld + l6);
        qs = qs + 1 >= 5 ? 0 : qs + 1;
      };
      waitv<12>(); BAR(); issueQa(2);
      waitv<9>(); BAR(); issueQb(2);
      waitv<6>(); BAR(); issueQa(3);
      BAR();
      issueQb(3);
      issueQ(0);
      waitv<6>();
      BAR();
      GET_TAG();
      setup();
      issueQ(20480); issueQ(40960);
    } else {
      const bool m1 = b < 108, mo = b >= 216;
      const u32 v = m1 ? b : (mo ? b - 216 : b - 108), vq = m1 ? (v * 2428) >> 16 : (v * 6554) >> 16;
      const u32 sA = uni((m1 || mo) ? 5 * vq : v >> 1), cA = uni(m1 ? v - 27 * vq : (mo ? v - 10 * vq : 5 * (v & 1))), sQ = uni((m1 || mo) ? 1u : 0u);
      const u32 pitch = uni(m1 ? 3456 : 1280);
      const u64 src = m1 ? (u64)w1 : (mo ? (u64)wo : (u64)w2);
      const rsrc_t rS = __builtin_amdgcn_make_buffer_rsrc((void*)src, 0, mo ? 640 * 1280 : 640 * 3456, 0x00020000);
      u32 vrt[20];
#pragma unroll
      for (int I = 0; I < 20; I++) {
        const u32 q = I >> 2, i = I & 3, ro = 8 * i + (lane >> 3);
        const u32 i40 = 8 * q + 4 * (i >> 1) + (ro & 3), hf = i40 >= 20 ? 1u : 0u, id = i40 - 20 * hf;
        const u32 rp = opaque(80 * hf + 15 * ((id * 13) >> 6) + 5 * ((ro >> 2) & 3) + id);
        vrt[I] = opaque((mo ? rp : 32 * sQ * q + ro) * pitch + (lane & 7) * 16 + (q - sQ * q) * 128);
      }
      u4 ONES = {0x3F803F80u, 0x3F803F80u, 0x3F803F80u, 0x3F803F80u};
      landed(ONES);
      const u32 sc = L_RING + 16384;
      const u32 wa = sc + (lane >> 3) * 128 + (((lane & 7) ^ (((lane >> 4) & 3) << 1)) << 4);
      const u32 r0 = 4 * g + (lr >> 2), ta = opaque(sc + r0 * 128 + (lr & 1) * 8 + ((((lr >> 1) & 1) ^ (((r0 >> 1) & 3) << 1)) << 4));
      const u32 dT = uni(m1 ? 20480u : (mo ? 10240u : 55296u)), dQ = uni(sQ ? 1024u : 4 * 55296u);
      const u32 dA = uni((m1 ? WS_W1T : (mo ? WS_WOT + (sA >= 10 ? 40960u : 0u) : WS_W2T)) + 4 * cA * dT + sA * 1024);
      u32 dJ[4];
#pragma unroll
      for (int n = 0; n < 4; n++) dJ[n] = uni(dA + n * dT + (mo ? (((4 * cA + n) * 52) >> 8) * 51200u : 0u));
      waitv<0>();
      GET_TAG();
      setup();
      STAMP(21);
#pragma nounroll
      for (u32 i = 0; i < 8; i++) BAR();
      u4 f[5][4];
      sfor<0, 7>([&](auto I) { ldp<0>(f[I >> 2][I & 3], rS, vrt[I], 32 * sA * pitch + cA * 128); });
      BAR();
      sfor<7, 14>([&](auto I) { ldp<0>(f[I >> 2][I & 3], rS, vrt[I], 32 * sA * pitch + cA * 128); });
      BAR();
      sfor<14, 20>([&](auto I) { ldp<0>(f[I >> 2][I & 3], rS, vrt[I], 32 * sA * pitch + cA * 128); });
      BAR();
      f4 cs[4];
      sfor<0, 5>([&](auto Q) {
        constexpr int q = Q;
        waitv<16 - 4 * q>();
#pragma unroll
        for (int i = 0; i < 4; i++) { landed(f[q][i]); sts128(wa + i * 1024, f[q][i]); }
        asm volatile("" ::: "memory");
#pragma unroll
        for (int n = 0; n < 4; n++) f[q][n] = ldstr(ta ^ (n << 5), (ta ^ (n << 5)) + 2048);
        asm volatile("" ::: "memory");
#pragma unroll
        for (int n = 0; n < 4; n++) cs[n] = mfma(f[q][n], ONES, q == 0 ? f4{0, 0, 0, 0} : cs[n]);
      });
      STAMP(22);
#pragma unroll
      for (int q = 0; q < 5; q++) {
        if (q == RTE) { BAR(); if (E2) BAR(); }
#pragma unroll
        for (int n = 0; n < 4; n++) st128(f[q][n], rW, l16, dJ[n] + q * dQ);
      }
#pragma unroll
      for (int n = 0; n < 4; n++) st128(__builtin_bit_cast(u4, cs[n]), rW, (m1 && lr == 0) ? g * 16 : OOB, WS_CS1 + vq * 6912 + (4 * cA + n) * 64);
      if (RTE == 5) { BAR(); if (E2) BAR(); }
    }
    STAMP(24);
    if (mt < 3) waitv<0>();
    BAR();
    if (mt == 3) { waitv<0>(); setflag(ws, WS_FRT + b * 8, tag, lane); }
    STAMP(4);
    PP pp;
    poll2(pp, rW, WS_FKV + ((j * 4 + mt) * 32 + (lane & 31)) * 8, tag);
    STAMP(5);
    {
      const u32 so = WS_KVP + (j * 32 * 4 + mt) * 896;
      const u32 v0 = lane < 53 ? l16 : OOB;
      u4 a[32], fr[2];
      auto lda = [&](auto Q) { ldv<(Q & 1) * 3584>(a[Q], rW, v0, so + (Q & ~1) * 3584); };
      sfor<0, 14>(lda);
      sts128(L_IMG + mt * 2048 + l16, u4{0, 0, 0, 0}); sts128(L_IMG + mt * 2048 + 1024 + l16, u4{0, 0, 0, 0});
      const bool zrow = (dst[1] & 0x430) == 0x410 && dst[1] < L_CB;
      f4 s0, s1, s2, s3;
      sfor<0, 16>([&](auto Q) {
        waitv<(Q < 9 ? 12 : 32 - 2 * Q)>();
        if constexpr (Q == 0) pollend(pp);
        if constexpr (Q == 0) { mm0(s0, sel[0], a[0]); mm0(s1, sel[1], a[0]); mm0(s2, sel[0], a[1]); mm0(s3, sel[1], a[1]); }
        else { mm(s0, sel[0], a[2 * Q]); mm(s1, sel[1], a[2 * Q]); mm(s2, sel[0], a[2 * Q + 1]); mm(s3, sel[1], a[2 * Q + 1]); }
        if constexpr (Q < 9) { lda(IC<14 + 2 * Q>{}); lda(IC<15 + 2 * Q>{}); }
        if constexpr (Q == 8) { ldv<0>(fr[0], rW, lane * 32, WS_FRT); ldv<16>(fr[1], rW, lane * 32, WS_FRT); }
      });
      MM_DONE();
      pin(s0); pin(s1); pin(s2); pin(s3);
      s0 += s2; s1 += s3;
      const u2 v1 = {pk2(s1[0], s1[1]), pk2(s1[2], s1[3])};
      sts64(dst[0], u2{pk2(s0[0], s0[1]), pk2(s0[2], s0[3])});
      sts64(dst[1], v1);
      if (zrow) { sts64(dst[1] + 64, v1); sts64(dst[1] + 128, v1); sts64(dst[1] + 192, v1); }
      STAMP(6);
      BAR();
      RT_CHECK();
    }
    auto issueW1 = [&](u32 it, u32 n, u32 ld) {
      const u32 so = WS_W1T + (n < 6 ? t0 + n : tl) * 20480 + it * 5120;
      dma4_sc1(rW, l16, so, ld + n * 5120); dma1_sc1(rW, l16, so + 4096, ld + n * 5120 + 4096);
    };
    const u32 o = 51200 + mt * 12288, o2 = 51200 + 49152 + (mt & 1) * 1024, sw = WS_WOT + j * 102400;
    {
      const u32 p0 = mt * 12288, p2 = 49152 + (mt & 1) * 1024;
      dma4_sc1(rW, l16, sw + p0, L_WO + p0); dma4_sc1(rW, l16, sw + p0 + 4096, L_WO + p0 + 4096); dma4_sc1(rW, l16, sw + p0 + 8192, L_WO + p0 + 8192);
      dma1_sc1(rW, l16, sw + p2, L_WO + p2);
    }
    if (b == 0 && tid == 256) __hip_atomic_store((GAS u64*)ws, tag, __ATOMIC_RELAXED, __HIP_MEMORY_SCOPE_AGENT);
    {
      f4 csum;
      u32 ci = lane + 64 * (mt & 1); ci = ci > 111 ? 111 : ci;
      const u32 n = ci >> 4, col = (n < 6 ? t0 + n : tl) * 16 + (ci & 15);
#pragma unroll
      for (int q = 0; q < 4; q++) csum[q] = asf(__builtin_amdgcn_raw_buffer_load_b32(rW, col * 4, WS_CS1 + q * 6912, SC1));
      const u32 bv = b1[col], b2v = ((const u32*)b2)[40 * j + (lane < 40 ? lane : 0)];
      sts32(L_CB + ci * 4, (csum[0] + csum[1]) + (csum[2] + csum[3]));
      sts32(L_CB + 512 + ci * 4, blo(bv));
      if (lane < 40) *(LAS u32*)(L_CB + 1024 + lane * 4) = b2v;
    }
    waitv<0>();
    BAR();
    STAMP(9);
    auto w1t = [&](u32 it, u32 n, u32 ld) { waitv<TH>(); issueW1(it, n, ld); };
    auto w2t = [&](auto I) { waitv<TH + 3>(); ldw2(I); };
    dma4_sc1(rW, l16, sw + o, L_WO + o); waitv<TH>(); dma4_sc1(rW, l16, sw + o + 4096, L_WO + o + 4096);
    waitv<TH>(); dma4_sc1(rW, l16, sw + o + 8192, L_WO + o + 8192); dma1_sc1(rW, l16, sw + o2, L_WO + o2);
    w1t(0, mt, L_W1A);
    waitv<5>(); lset(2, mt);
    STAMP(10);
    w1t(0, mt < 3 ? mt + 4 : 6, L_W1A);
    sfor<0, HA>(w2t);
    lwait(1);
    STAMP(7);
    w1t(1, mt, L_W1B); w1t(1, mt < 3 ? mt + 4 : 6, L_W1B);
    sfor<HA, HA + 4>(w2t);
    waitv<4>(); lset(4, mt);
    STAMP(25);
    sfor<HA + 4, HB2>(w2t);
    lwait(3);
    STAMP(8);
#pragma nounroll
    for (u32 u = mt; u < 14; u += 4) { const u32 it = u >= 7 ? 1u : 0u; w1t(it + 2, u - 7 * it, L_W1B + 35840 + it * 35840); }
    waitv<0>(); lset(5, mt);
    STAMP(28);
    {
      const u32 pc = 2 * P + (mt >> 1), mc = 2 * (mt & 1);
      poll(ws, WS_FH + ((pc * 4 + mc) * 8 + lr) * 8, tag);
#pragma unroll
      for (int m = 0; m < 2; m++) {
        const u2 v0 = __builtin_amdgcn_raw_buffer_load_b64(rW, g * 256 + lr * 8, WS_ST + (pc * 4 + mc + m) * 1024, SC1);
        const u2 v1 = __builtin_amdgcn_raw_buffer_load_b64(rW, g * 256 + lr * 8 + 128, WS_ST + (pc * 4 + mc + m) * 1024, SC1);
        const float mean = xsum16_32(asf(v0.x) + asf(v1.x)) * (1.f / 640.f);
        const float var = fmaxf(xsum16_32(asf(v0.y) + asf(v1.y)) * (1.f / 640.f) - mean * mean, 0.f);
        const float m1 = rbf(mean), sd1 = rbf(__builtin_amdgcn_sqrtf(rbf(rbf(var) + 1e-5f))), r1 = __builtin_amdgcn_rcpf(sd1);
        const float m2 = rbf((mean - m1) * r1), sd2 = rbf(__builtin_amdgcn_sqrtf(rbf(rbf(var * r1 * r1) + 1e-5f)));
        const float sc = r1 * __builtin_amdgcn_rcpf(sd2);
        sts64(L_CB + 2048 + mt * 256 + m * 128 + lr * 8, u2{asu(sc), asu(sc * (m1 + m2 * sd1))});
      }
    }
    lset(6, mt);
    STAMP(27);
    sfor<HB2, 35>(w2t);
    STAMP(26);
    lwait(7);
    {
      const u32 so = WS_W2T + (j * 5 * 54 + 7 * mt) * 1024, ld = mt * 35840;
#pragma nounroll
      for (u32 n = 0; n < 5; n++) {
        waitv<TH2>();
        sfor<0, 7>([&](auto S) {
          constexpr int k = S < 2 ? S : (S < 4 ? S + 2 : (S < 6 ? S - 2 : 6));
          dma1_sc1(rW, l16, so + (n * 54 + k) * 1024, ld + (S * 5 + n) * 1024);
        });
      }
    }
    waitv<0>();
    lset(8, mt);
    STAMP(30);
  }
  {
    f4 da[4][5];
    u4 xf[7][4];
    u2 xh[4][2];
    const u32 so = WS_HID + (p * 4 * 54 + s0) * 1024;
    const u32 vh0 = w < 6 ? l8 : l16 + 2048, vh1 = w < 6 ? l8 + 512 : l16 + 2056;
    auto ldx = [&](auto U) {
      constexpr int s = U / 4, a = U % 4;
      if constexpr (s < 2) ldv<s * 1024>(xf[s][a], rW, l16, so + a * 54 * 1024);
      else if constexpr (s < 4) ldv<(s - 2) * 1024>(xf[s][a], rW, vmid, so + (a * 54 + 3) * 1024);
      else if constexpr (s == 4) ldv<2048>(xf[s][a], rW, l16, so + a * 54 * 1024);
      else if constexpr (s == 5) { ldv2<0>(xh[a][0], rW, vh0, so + (a * 54 + 3) * 1024); ldv2<0>(xh[a][1], rW, vh1, so + (a * 54 + 3) * 1024); }
      else ldv<0>(xf[s][a], rW, vlast, so + (a * 54 + 3) * 1024);
    };
    const u32 fo = WS_FHID + ((p * 16 + 2 * w) * 2 + (lane & 3)) * 16;
    sfor<0, 2>([&](auto H) {
      constexpr int u0 = H ? 16 : 0, u1 = H ? 28 : 16, XD = H ? 12 : 6;
      PP pp;
      poll2(pp, rW, fo + H * 8, tag);
      STAMP(14 + 2 * H);
      sfor<u0, u0 + XD>(ldx);
      sfor<u0, u1>([&](auto U) {
        constexpr int u = U, s = u / 4, a = u % 4;
        constexpr int e0 = u + XD + (u >= 4 ? 4 : 0), e = e0 < u1 ? e0 : u1;
        constexpr int f0 = u + 1 + XD + (u + 1 >= 4 ? 4 : 0), f = f0 < u1 ? f0 : u1;
        constexpr int lo5 = u + 1 > 20 ? u + 1 : 20, hi5 = e < 24 ? e : 24;
        waitv<(e - u - 1) + (hi5 > lo5 ? hi5 - lo5 : 0)>();
        if constexpr (u == u0) pollend(pp);
        sfor<0, 5>([&](auto N) {
          if constexpr (s == 0) mm0(da[a][N], w2f[N], xf[s][a]);
          else if constexpr (s == 5) mmh(da[a][N], w2f[5 * s + N], xh[a][0], xh[a][1]);
          else mm(da[a][N], w2f[5 * s + N], xf[s][a]);
        });
        sfor<e, f>(ldx);
      });
    });
    MM_DONE();
    STAMP(17);
    lwait(9);
#pragma unroll
    for (int a = 0; a < 4; a++)
#pragma unroll
      for (int n = 0; n < 5; n++) {
        pin(da[a][n]);
        sts64(L_RED + ((a * 5 + n) * 4 + (w >> 1)) * 1024 + l16 + (w & 1) * 8, u2{pk2(da[a][n][0], da[a][n][1]), pk2(da[a][n][2], da[a][n][3])});
      }
    const rsrc_t rOut = __builtin_amdgcn_make_buffer_rsrc((void*)out, 0, 2048 * 1280, 0x00020000);
    const u32 LO = opaque(0x00003F80u), HI = opaque(0x3F800000u), n0 = 3 * nh;
    const u32 ra = opaque(L_RED + (mt * 5 + n0) * 4096 + l16);
    u2 bb[3], hr[3];
#pragma unroll
    for (int i = 0; i < 3; i++) { bb[i] = lds64(L_CB + 1024 + (n0 + i) * 32 + g * 8); hr[i] = lds64(L_HR + (mt * 5 + n0 + i) * 512 + l8); }
    BAR();
    STAMP(18);
    u4 pv[3][4];
#pragma unroll
    for (int i = 0; i < 3; i++)
#pragma unroll
      for (int q = 0; q < 4; q++) pv[i][q] = lds128(ra + i * 4096 + q * 1024);
#pragma unroll
    for (int i = 0; i < 3; i++) {
      float s0 = 0.f, s1 = 0.f, s2 = 0.f, s3 = 0.f;
#pragma unroll
      for (int q = 0; q < 4; q++) {
        s0 = dot2c(pv[i][q].x, LO, s0); s1 = dot2c(pv[i][q].x, HI, s1); s2 = dot2c(pv[i][q].y, LO, s2); s3 = dot2c(pv[i][q].y, HI, s3);
        s0 = dot2c(pv[i][q].z, LO, s0); s1 = dot2c(pv[i][q].z, HI, s1); s2 = dot2c(pv[i][q].w, LO, s2); s3 = dot2c(pv[i][q].w, HI, s3);
      }
      const u32 o0 = pk2(unp(hr[i].x) + unp(pk2(f2{s0, s1} + unp(bb[i].x)))), o1 = pk2(unp(hr[i].y) + unp(pk2(f2{s2, s3} + unp(bb[i].y))));
      __builtin_amdgcn_raw_buffer_store_b64(u2{o0, o1}, rOut, (i == 2 && helper) ? OOB : lr * 1280 + g * 8 + i * 32, (p * 64 + mt * 16) * 1280 + j * 160 + n0 * 32, 0);
    }
  }
  waitv<0>();
  STAMP(19);
  if (b == 77 && w == PW) {
    u32 d = __builtin_amdgcn_readlane(tsv, PROBE) - __builtin_amdgcn_readlane(tsv, 47);
    u64 t0 = __builtin_amdgcn_s_memrealtime();
    while (__builtin_amdgcn_s_memrealtime() - t0 < 20ull * d) __builtin_amdgcn_s_sleep(2);
  }
}

extern "C" void kernel_launch(void* const* d_in, const int* in_sizes, int n_in, void* d_out, int out_size, void* d_ws, size_t ws_size,
                              hipStream_t stream) {
  fused_block<<<256, 512, 0, stream>>>((const u16*)d_in[0], (const u16*)d_in[1], (const u16*)d_in[2], (const u16*)d_in[4], (const u16*)d_in[5],
                                       (const u16*)d_in[6], (const u16*)d_in[7], (u16*)d_out, (char*)d_ws);
}
```

```cpp
#include <hip/hip_runtime.h>
#include <stdint.h>
#pragma clang diagnostic ignored "-Wint-to-pointer-cast"
#pragma clang diagnostic ignored "-Warray-bounds"

typedef unsigned short u16;
typedef unsigned u32;
typedef unsigned long long u64;
typedef float f2 __attribute__((ext_vector_type(2)));
typedef float f4 __attribute__((ext_vector_type(4)));
typedef unsigned u2 __attribute__((ext_vector_type(2)));
typedef unsigned u4 __attribute__((ext_vector_type(4)));
typedef short s4 __attribute__((ext_vector_type(4)));
typedef __bf16 bf2 __attribute__((ext_vector_type(2)));
typedef __bf16 bf8 __attribute__((ext_vector_type(8)));
typedef __amdgpu_buffer_rsrc_t rsrc_t;
#define LAS __attribute__((address_space(3)))
#define GAS __attribute__((address_space(1)))
#define DI __device__ __forceinline__

template <int I> struct IC { static constexpr int v = I; constexpr operator int() const { return I; } };
template <int B, int E, class F> DI void sfor(F&& f) {
  if constexpr (B < E) { f(IC<B>{}); sfor<B + 1, E>(f); }
}

constexpr u32 WS_FRT = 4096, WS_FKV = 8192, WS_FO = 16384, WS_FH = 24576, WS_FHID = 32768;
constexpr u32 WS_KVP = 65536;
constexpr u32 WS_OB = 1u << 20;
constexpr u32 WS_HB = WS_OB + 32 * 81920;
constexpr u32 WS_ST = WS_HB + 32 * 81920;
constexpr u32 WS_CS1 = WS_ST + 2048 * 64;
constexpr u32 WS_WOT = WS_CS1 + 32768;
constexpr u32 WS_W1T = WS_WOT + 40 * 20480;
constexpr u32 WS_W2T = WS_W1T + 108 * 20480;
constexpr u32 WS_HID = WS_W2T + 8 * 54 * 5120;
constexpr u32 WS_END = WS_HID + 32 * 4 * 54 * 1024;
constexpr u32 WS_PROF = WS_END;
constexpr u32 OOB = 0x7FFFFF00u;
constexpr int SC1 = 16;
#define MAP 1
#define ROT 2
#define WQ 6
#define E2 0
#define PR 0
#define CPF 0
#define CODE_BYTES 50176
#define PS 4
#define RTE 5
#define HDV 8
#define TH2 4
#define TH 8
#define HA 4
#define HB2 20
#define CW 5
#define CG 32

constexpr u32 L_X = 0, L_RING = 81920;
constexpr u32 L_WO = 0, L_IMG = 147456, L_W1A = 110592, L_HR = 147456, L_CB = 160768, L_W1B = 0, L_RED = 0;

DI f4 mfma(u4 a, u4 b, f4 c) {
  return __builtin_amdgcn_mfma_f32_16x16x32_bf16(__builtin_bit_cast(bf8, a), __builtin_bit_cast(bf8, b), c, 0, 0, 0);
}
DI void mm(f4& c, u4 a, u4 b) { asm volatile("v_mfma_f32_16x16x32_bf16 %0, %1, %2, %0" : "+v"(c) : "v"(a), "v"(b) : "memory"); }
DI void mm0(f4& c, u4 a, u4 b) { asm volatile("v_mfma_f32_16x16x32_bf16 %0, %1, %2, 0" : "=&v"(c) : "v"(a), "v"(b) : "memory"); }
#define MM_DONE() asm volatile("s_nop 7\n\ts_nop 1" ::: "memory")
DI void pin(f4& c) { asm volatile("" : "+v"(c)); }
DI void zero(f4& c) { c = f4{0, 0, 0, 0}; asm volatile("" : "+v"(c)); }
DI void undef(u4& d) { asm volatile("" : "=v"(d)); }
DI u32 uni(u32 v) { v = __builtin_amdgcn_readfirstlane(v); asm volatile("" : "+s"(v)); return v; }
DI u32 opaque(u32 v) { asm volatile("" : "+v"(v)); return v; }

DI u32 pk2(float a, float b) { f2 v = {a, b}; return __builtin_bit_cast(u32, __builtin_convertvector(v, bf2)); }
DI u32 pk2(f2 v) { return __builtin_bit_cast(u32, __builtin_convertvector(v, bf2)); }
DI float rbf(float a) { return __builtin_bit_cast(float, pk2(a, a) << 16); }
DI float blo(u32 v) { return __builtin_bit_cast(float, v << 16); }
DI float bhi(u32 v) { return __builtin_bit_cast(float, v & 0xFFFF0000u); }
DI f2 unp(u32 v) { return f2{blo(v), bhi(v)}; }
DI float asf(u32 v) { return __builtin_bit_cast(float, v); }
DI u32 asu(float v) { return __builtin_bit_cast(u32, v); }

template <int OFF> DI void dma(rsrc_t r, u32 voff, u32 soff, u32 lds) {
  asm volatile("s_mov_b32 m0, %0\n\ts_nop 0\n\tbuffer_load_dwordx4 %1, %2, %3 offen offset:%4 lds" ::"s"(lds), "v"(voff), "s"(r), "s"(soff), "n"(OFF) : "memory");
}
template <int OFF> DI void dma_c(rsrc_t r, u32 voff, u32 soff) {
  asm volatile("buffer_load_dwordx4 %0, %1, %2 offen offset:%3 lds" ::"v"(voff), "s"(r), "s"(soff), "n"(OFF) : "memory");
}
DI void dma4_sc1(rsrc_t r, u32 voff, u32 soff, u32 lds) {
  asm volatile("s_mov_b32 m0, %0\n\ts_nop 0\n\tbuffer_load_dwordx4 %1, %2, %3 offen sc1 lds\n\tbuffer_load_dwordx4 %1, %2, %3 offen offset:1024 sc1 lds\n\t"
               "buffer_load_dwordx4 %1, %2, %3 offen offset:2048 sc1 lds\n\tbuffer_load_dwordx4 %1, %2, %3 offen offset:3072 sc1 lds" ::"s"(lds), "v"(voff), "s"(r), "s"(soff) : "memory");
}
DI void dma2_sc1(rsrc_t r, u32 voff, u32 soff, u32 lds) {
  asm volatile("s_mov_b32 m0, %0\n\ts_nop 0\n\tbuffer_load_dwordx4 %1, %2, %3 offen sc1 lds\n\tbuffer_load_dwordx4 %1, %2, %3 offen offset:1024 sc1 lds" ::"s"(lds), "v"(voff), "s"(r), "s"(soff) : "memory");
}
DI void dma1_sc1(rsrc_t r, u32 voff, u32 soff, u32 lds) {
  asm volatile("s_mov_b32 m0, %0\n\ts_nop 0\n\tbuffer_load_dwordx4 %1, %2, %3 offen sc1 lds" ::"s"(lds), "v"(voff), "s"(r), "s"(soff) : "memory");
}
template <int OFF> DI void ldv(u4& d, rsrc_t r, u32 voff, u32 soff) {
  asm volatile("buffer_load_dwordx4 %0, %1, %2, %3 offen offset:%4 sc1" : "=v"(d) : "v"(voff), "s"(r), "s"(soff), "n"(OFF) : "memory");
}
template <int OFF> DI void ldv_rw(u4& d, rsrc_t r, u32 voff, u32 soff) {
  asm volatile("buffer_load_dwordx4 %0, %1, %2, %3 offen offset:%4 sc1" : "+v"(d) : "v"(voff), "s"(r), "s"(soff), "n"(OFF) : "memory");
}
template <int OFF> DI void ldp(u4& d, rsrc_t r, u32 voff, u32 soff) {
  asm volatile("buffer_load_dwordx4 %0, %1, %2, %3 offen offset:%4" : "=v"(d) : "v"(voff), "s"(r), "s"(soff), "n"(OFF) : "memory");
}
template <int OFF> DI void ldp2(u2& d, rsrc_t r, u32 voff, u32 soff) {
  asm volatile("buffer_load_dwordx2 %0, %1, %2, %3 offen offset:%4" : "=v"(d) : "v"(voff), "s"(r), "s"(soff), "n"(OFF) : "memory");
}
template <int N> DI void waitv() { asm volatile("s_waitcnt vmcnt(%0)" ::"n"(N) : "memory"); }
template <class T> DI void landed(T& d) { asm volatile("" : "+v"(d)); }
#define BAR() do { asm volatile("" ::: "memory"); __builtin_amdgcn_s_waitcnt(0xC07F); asm volatile("s_barrier" ::: "memory"); } while (0)
DI u4 lds128(u32 a) { return *(LAS u4*)(a); }
DI u2 lds64(u32 a) { return *(LAS u2*)(a); }
DI void sts128(u32 a, u4 v) { *(LAS u4*)(a) = v; }
DI void sts64(u32 a, u2 v) { *(LAS u2*)(a) = v; }
DI void sts32(u32 a, float v) { *(LAS float*)(a) = v; }
DI u4 ldstr(u32 a0, u32 a1) {
  u2 x = __builtin_bit_cast(u2, __builtin_amdgcn_ds_read_tr16_b64_v4i16((LAS s4*)(a0)));
  u2 y = __builtin_bit_cast(u2, __builtin_amdgcn_ds_read_tr16_b64_v4i16((LAS s4*)(a1)));
  return u4{x.x, x.y, y.x, y.y};
}
DI u4 ldstr(u32 a) { return ldstr(a, a + 2560); }
DI float xsum16_32(float v) {
  auto r = __builtin_amdgcn_permlane16_swap(asu(v), asu(v), false, false);
  v = asf((u32)r[0]) + asf((u32)r[1]);
  auto q = __builtin_amdgcn_permlane32_swap(asu(v), asu(v), false, false);
  return asf((u32)q[0]) + asf((u32)q[1]);
}
DI float xsum8(float v) {
  v += asf(__builtin_amdgcn_update_dpp(0, asu(v), 0xB1, 0xf, 0xf, true));
  v += asf(__builtin_amdgcn_update_dpp(0, asu(v), 0x4E, 0xf, 0xf, true));
  return v + asf(__builtin_amdgcn_update_dpp(0, asu(v), 0x141, 0xf, 0xf, true));
}
DI void poll(const char* ws, u32 off, u64 tag) {
  const GAS u64* f = (const GAS u64*)(ws + off);
#pragma nounroll
  for (u32 it = 0; it < (1u << 18); ++it) {
    u64 v = __hip_atomic_load(f, __ATOMIC_RELAXED, __HIP_MEMORY_SCOPE_AGENT);
    if (__all(v == tag)) break;
    __builtin_amdgcn_s_sleep(2);
  }
  __builtin_amdgcn_fence(__ATOMIC_ACQUIRE, "wavefront");
  asm volatile("" ::: "memory");
}
DI void lset_(u32 k, u32 mt, u32 tg) { __builtin_amdgcn_s_waitcnt(0xC07F); *(volatile LAS u32*)(L_CB + 1536 + k * 16 + mt * 4) = tg; asm volatile("" ::: "memory"); }
DI void lwait_(u32 k, u32 tg) {
  asm volatile("" ::: "memory");
  for (;;) {
    const u4 v = *(volatile LAS u4*)(L_CB + 1536 + k * 16);
    if (__builtin_amdgcn_readfirstlane((v.x ^ tg) | (v.y ^ tg) | (v.z ^ tg) | (v.w ^ tg)) == 0) break;
    __builtin_amdgcn_s_sleep(1);
  }
  asm volatile("" ::: "memory");
}
#define lset(k, mt) lset_(k, mt, (u32)tag)
#define lwait(k) lwait_(k, (u32)tag)
struct PP { u2 a, b; };
DI void ldf(u2& d, rsrc_t r, u32 voff) { asm volatile("buffer_load_dwordx2 %0, %1, %2, 0 offen sc1" : "+v"(d) : "v"(voff), "s"(r) : "memory"); }
DI void poll2(PP& pp, rsrc_t r, u32 voff, u64 tag) {
  const u32 tl = (u32)tag, th = (u32)(tag >> 32);
  asm volatile("" : "=v"(pp.a), "=v"(pp.b));
  ldf(pp.a, r, voff);
  __builtin_amdgcn_s_sleep(PS);
#pragma nounroll
  for (u32 it = 0; it < (1u << 18); ++it) {
    ldf(pp.b, r, voff);
    waitv<1>(); landed(pp.a);
    if (!__any(((pp.a.x ^ tl) | (pp.a.y ^ th)) != 0)) break;
    ldf(pp.a, r, voff);
    waitv<1>(); landed(pp.b);
    if (!__any(((pp.b.x ^ tl) | (pp.b.y ^ th)) != 0)) break;
  }
  asm volatile("" ::: "memory");
}
DI void pollend(PP& pp) { asm volatile("" ::"v"(pp.a), "v"(pp.b)); }
DI void setflag(char* ws, u32 off, u64 tag, u32 lane) {
  if (lane == 0) __hip_atomic_store((GAS u64*)(ws + off), tag, __ATOMIC_RELAXED, __HIP_MEMORY_SCOPE_AGENT);
  asm volatile("" ::: "memory");
}
template <bool PHI> DI u32 phi2(float a0, float a1, float r0, float r1) {
  const f2 a = {a0, a1}, r = {r0, r1};
  if constexpr (!PHI) return pk2(a * r);
  const f2 t = a * r + 1e-6f, u = a * (r * 1.4426950408889634f);
  return pk2(fmaxf(t[0], 1e-6f) + __builtin_amdgcn_fmed3f(__builtin_amdgcn_exp2f(u[0]), 0.f, 1.f),
             fmaxf(t[1], 1e-6f) + __builtin_amdgcn_fmed3f(__builtin_amdgcn_exp2f(u[1]), 0.f, 1.f));
}
DI float dot2c(u32 a, u32 b, float c) { return __builtin_amdgcn_fdot2_f32_bf16(__builtin_bit_cast(bf2, a), __builtin_bit_cast(bf2, b), c, false); }
DI u4 center8(u4 x, u32 lo, u32 hi, float nm) {
  u4 r; float t0, t1, t2, t3, t4, t5, t6, t7;
  asm("v_dot2_f32_bf16 %4, %12, %16, %18\n\tv_dot2_f32_bf16 %5, %12, %17, %18\n\tv_dot2_f32_bf16 %6, %13, %16, %18\n\tv_dot2_f32_bf16 %7, %13, %17, %18\n\t"
      "v_dot2_f32_bf16 %8, %14, %16, %18\n\tv_dot2_f32_bf16 %9, %14, %17, %18\n\tv_dot2_f32_bf16 %10, %15, %16, %18\n\tv_dot2_f32_bf16 %11, %15, %17, %18\n\t"
      "v_cvt_pk_bf16_f32 %0, %4, %5\n\tv_cvt_pk_bf16_f32 %1, %6, %7\n\tv_cvt_pk_bf16_f32 %2, %8, %9\n\tv_cvt_pk_bf16_f32 %3, %10, %11"
      : "=v"(r.x), "=v"(r.y), "=v"(r.z), "=v"(r.w), "=&v"(t0), "=&v"(t1), "=&v"(t2), "=&v"(t3), "=&v"(t4), "=&v"(t5), "=&v"(t6), "=&v"(t7)
      : "v"(x.x), "v"(x.y), "v"(x.z), "v"(x.w), "v"(lo), "v"(hi), "v"(nm));
  return r;
}
template <int N> DI void waitl() { asm volatile("" ::: "memory"); __builtin_amdgcn_s_waitcnt(0xC07F | (N << 8)); asm volatile("" ::: "memory"); }
DI void st128(u4 v, rsrc_t r, u32 voff, u32 soff) {
  __builtin_amdgcn_raw_buffer_store_b128(v, r, voff, soff, SC1);
  asm volatile("s_nop 1" ::"v"(v) : "memory");
}
DI void st64(u2 v, rsrc_t r, u32 voff, u32 soff) { __builtin_amdgcn_raw_buffer_store_b64(v, r, voff, soff, SC1); }
DI void publish20(const u32* v, rsrc_t r, u32 l16, u32 so, u32 j) {
  if ((j & 1) == 0) {
    st128(u4{v[0], v[1], v[2], v[3]}, r, l16, so);
    st128(u4{v[4], v[5], v[6], v[7]}, r, l16, so + 1024);
    st64(u2{v[8], v[9]}, r, l16 >> 1, so + 2048);
  } else {
    st64(u2{v[0], v[1]}, r, l16 >> 1, so + 2560);
    st128(u4{v[2], v[3], v[4], v[5]}, r, l16, so + 3072);
    st128(u4{v[6], v[7], v[8], v[9]}, r, l16, so + 4096);
  }
}
template <int OFF> DI void ldv2(u2& d, rsrc_t r, u32 voff, u32 soff) {
  asm volatile("buffer_load_dwordx2 %0, %1, %2, %3 offen offset:%4 sc1" : "=v"(d) : "v"(voff), "s"(r), "s"(soff), "n"(OFF) : "memory");
}
DI void mmh(f4& c, u4 a, u2 b0, u2 b1) { mm(c, a, u4{b0.x, b0.y, b1.x, b1.y}); }
template <int V, int L> DI void waitvl() {
  asm volatile("" ::: "memory"); __builtin_amdgcn_s_waitcnt(0x0070 | (V & 15) | ((V >> 4) << 14) | (L << 8)); asm volatile("" ::: "memory");
}

#define STAMP(i)

__global__ __launch_bounds__(512, 2) void fused_block(const u16* __restrict__ x, const u16* __restrict__ wqkv, const u16* __restrict__ wo,
                                                      const u16* __restrict__ w1, const u16* __restrict__ b1, const u16* __restrict__ w2,
                                                      const u16* __restrict__ b2, u16* __restrict__ out, char* __restrict__ ws) {
  __shared__ __attribute__((aligned(1024))) char smem[163840];
  asm volatile("" ::"v"((u32)(uintptr_t)(LAS char*)smem));
  const u32 tid = threadIdx.x, lane = tid & 63, lr = lane & 15, g = lane >> 4;
  const u32 w = __builtin_amdgcn_readfirstlane(tid >> 6), mt = w & 3, nh = w >> 2;
  const bool helper = nh != 0;
  const u32 b = blockIdx.x, xg = b & 7, rr = b >> 3, p = 8 * (xg >> 1) + (rr >> 2), j = 4 * (xg & 1) + (rr & 3);
  asm volatile("" ::"s"(x), "s"(wqkv), "s"(wo), "s"(w1), "s"(b1), "s"(w2), "s"(b2), "s"(out), "s"(ws));
  STAMP(0);
  const rsrc_t rW = __builtin_amdgcn_make_buffer_rsrc((void*)ws, 0, WS_END, 0x00020000);
  const u32 l16 = lane * 16, l8 = lane * 8;
  u2 ep;
  u64 tag;
#define LD_EPOCH() asm volatile("buffer_load_dwordx2 %0, off, %1, 0 sc1" : "=v"(ep) : "s"(rW) : "memory")
#define GET_TAG() do { u32 lo_, hi_; asm volatile("v_readfirstlane_b32 %0, %2\n\tv_readfirstlane_b32 %1, %3\n\ts_nop 3" : "=s"(lo_), "=s"(hi_) : "v"(ep.x), "v"(ep.y) : "memory"); \
    tag = (((u64)hi_ << 32) | lo_) + 1; } while (0)
  u32 rot = xg + 3 * (p & 3), rotq = xg + (p & 3);
  rot = rot >= 10 ? rot - 10 : rot; rotq = rotq >= 10 ? rotq - 10 : (rotq >= 5 ? rotq - 5 : rotq);
  const u32 P = p >> 1, c = ((p & 1) << 3) | j, rho = c >> 1, odd = c & 1;
  const u32 t0 = rho < 6 ? 14 * rho + 8 * odd : 12 * rho + 12 + 6 * odd, tl = rho < 6 ? 14 * rho + 6 + odd : t0;
  const u32 s0 = w < 6 ? 7 * w : 6 * w + 6;
  const u32 vlast = w >= 6 ? OOB : l16 + 3072, vmid = w >= 6 ? l16 : l16 + 1024, vsp = w >= 6 ? l16 + 2048 : l16, soW2 = WS_W2T + (j * 5 * 54 + s0) * 1024;
  u4 w2f[35];
  auto ldw2 = [&](auto I) {
    constexpr int s = I / 5, n = I % 5;
    if constexpr (s < 2) ldv<s * 1024>(w2f[I], rW, l16, soW2 + n * 54 * 1024);
    else if constexpr (s < 4) ldv<(s - 2) * 1024>(w2f[I], rW, vmid, soW2 + (n * 54 + 3) * 1024);
    else if constexpr (s == 4) ldv<2048>(w2f[I], rW, l16, soW2 + n * 54 * 1024);
    else ldv<0>(w2f[I], rW, s == 5 ? vsp : vlast, soW2 + (n * 54 + 3) * 1024);
  };
#define RT_CHECK() do { waitv<0>(); landed(fr[0]); landed(fr[1]); const u32 tl32 = (u32)tag, th32 = (u32)(tag >> 32); \
    const u32 bad = ((fr[0].x ^ tl32) | (fr[0].z ^ tl32) | (fr[1].x ^ tl32) | (fr[1].z ^ tl32)) | ((fr[0].y ^ th32) | (fr[0].w ^ th32) | (fr[1].y ^ th32) | (fr[1].w ^ th32)); \
    if (__any(bad != 0)) { _Pragma("nounroll") for (u32 q = 0; q < 4; q++) poll(ws, WS_FRT + (64 * q + lane) * 8, tag); } } while (0)
  constexpr u32 L_RS = L_RING + 4096;

  if (!helper) {
    const rsrc_t rX = __builtin_amdgcn_make_buffer_rsrc((void*)x, 0, 2048 * 1280, 0x00020000);
    float rsd[2];
    const u32 ONE2 = opaque(0x3F803F80u);
    {
      u4 xr[2][10];
      const u32 vx = (lane >> 3) * 1280 + (lane & 7) * 16, sx = (p * 64 + 16 * mt) * 1280;
      sfor<0, 10>([&](auto I) { ldp<I * 128>(xr[0][I], rX, vx, sx); });
      sfor<0, 10>([&](auto I) { ldp<I * 128>(xr[1][I], rX, vx, sx + 8 * 1280); });
      LD_EPOCH();
      const u32 wa = L_X + mt * 2048 + (lane >> 3) * 128 + (((lane & 7) ^ (lane >> 3)) << 4);
      const u32 lo = opaque(0x00003F80u), hi = opaque(0x3F800000u);
      float dm[2];
      sfor<0, 2>([&](auto G) {
        waitv<(G == 0 ? 11 : 1)>();
        STAMP(20 + G);
        float s0 = 0.f, s1 = 0.f;
#pragma unroll
        for (int i = 0; i < 10; i++) {
          landed(xr[G][i]);
          s0 = dot2c(xr[G][i].x, ONE2, s0); s1 = dot2c(xr[G][i].y, ONE2, s1); s0 = dot2c(xr[G][i].z, ONE2, s0); s1 = dot2c(xr[G][i].w, ONE2, s1);
        }
        const float m = xsum8(s0 + s1) * (1.f / 640.f), nm = -rbf(m);
        dm[G] = m + nm;
#pragma unroll
        for (int i = 0; i < 10; i++) { xr[G][i] = center8(xr[G][i], lo, hi, nm); sts128(wa + G * 1024 + i * 8192, xr[G][i]); }
      });
      sfor<0, 2>([&](auto G) {
        float s0 = 0.f, s1 = 0.f;
#pragma unroll
        for (int i = 0; i < 10; i++) {
          const u4 v = xr[G][i];
          s0 = dot2c(v.x, v.x, s0); s1 = dot2c(v.y, v.y, s1); s0 = dot2c(v.z, v.z, s0); s1 = dot2c(v.w, v.w, s1);
        }
        const float var = fmaxf(xsum8(s0 + s1) * (1.f / 640.f) - dm[G] * dm[G], 0.f);
        rsd[G] = __builtin_amdgcn_rcpf(rbf(__builtin_amdgcn_sqrtf(rbf(rbf(var) + 1e-5f))));
        landed(rsd[G]);
      });
    }
    STAMP(22);
    waitv<0>();
    GET_TAG();
    const u32 xa0 = L_X + (lr >> 3) * 1024 + (lr & 7) * 128 + ((g ^ (lr & 7)) << 4);
    const u32 tr0 = L_RING + (4 * g + (lr >> 2)) * 160 + (lr & 3) * 8;
    const u32 mh = mt & 1, nq = mt >> 1;
    float qrs;
    u4 xq[20];
    {
      f4 acc[2][5];
#pragma unroll
      for (int i = 0; i < 5; i++) { zero(acc[0][i]); zero(acc[1][i]); }
      const u32 bA = tr0 + nq * 96, bC = tr0 + 64 + nq * 5120;
      const u32 xs0 = xa0 + mh * 4096;
      auto half = [&](u4* cur, u4* nxt, u32 xa, u32 a, u32 c, auto KS, auto LD) {
        constexpr int o = KS * 10240;
        mm(acc[0][0], cur[5], cur[0]);
        if constexpr (LD) nxt[5] = lds128(xa);
        mm(acc[1][0], cur[6], cur[0]);
        if constexpr (LD) nxt[6] = lds128(xa + 2048);
        mm(acc[0][1], cur[5], cur[1]);
        if constexpr (LD) nxt[0] = ldstr(a + o);
        mm(acc[1][1], cur[6], cur[1]);
        if constexpr (LD) nxt[1] = ldstr(a + o + 32);
        mm(acc[0][2], cur[5], cur[2]);
        if constexpr (LD) nxt[2] = ldstr(c + o);
        mm(acc[1][2], cur[6], cur[2]);
        if constexpr (LD) nxt[3] = ldstr(a + o + 5120);
        mm(acc[0][3], cur[5], cur[3]);
        if constexpr (LD) nxt[4] = ldstr(a + o + 5152);
        mm(acc[1][3], cur[6], cur[3]);
        mm(acc[0][4], cur[5], cur[4]); mm(acc[1][4], cur[6], cur[4]);
      };
      u4 fa[7], fb[7];
      u32 st = rot, xa = xs0 + st * 8192, a = bA, cc = bC;
      BAR();
      STAMP(1);
      fa[5] = lds128(xa); fa[6] = lds128(xa + 2048); fa[0] = ldstr(a); fa[1] = ldstr(a + 32); fa[2] = ldstr(cc);
      fa[3] = ldstr(a + 5120); fa[4] = ldstr(a + 5152);
      u32 lkv[6];
#pragma unroll
      for (int q = 0; q < 2; q++) {
        const u32 dq = 4 * q + g - mt, e0 = lr - 4 * mt, e1 = e0 + 16, sb = L_RING + mt * 5120;
        lkv[3 * q] = opaque(sb + ((dq < 5 && e0 < 20) ? e0 * 40 + dq * 8 : 896));
        lkv[3 * q + 1] = opaque(sb + ((dq < 5 && e1 < 20) ? e1 * 40 + dq * 8 : 896));
        lkv[3 * q + 2] = opaque(sb + ((dq < 5 && lr == 0) ? 800 + dq * 8 : (dq == 0 && lr == 1) ? 840 : 896));
      }
#pragma nounroll
      for (u32 i = 1; i < 10; i++) {
        waitl<0>();
        half(fa, fb, xa ^ 64, a, cc, IC<1>{}, IC<1>{});
        st = st + 1 >= 10 ? 0 : st + 1;
        xa = opaque(xs0 + st * 8192); a = opaque(bA + (i & 3) * 20480); cc = opaque(bC + (i & 3) * 20480);
        BAR();
        half(fb, fa, xa, a, cc, IC<0>{}, IC<1>{});
      }
      if ((lane & 7) == 0) { sts32(L_RS + mt * 64 + (lane >> 3) * 4, rsd[0]); sts32(L_RS + mt * 64 + 32 + (lane >> 3) * 4, rsd[1]); }
      waitl<0>();
      half(fa, fb, xa ^ 64, a, cc, IC<1>{}, IC<1>{});
      {
        u32 qs = rotq;
#pragma unroll
        for (int q = 0; q < 5; q++) {
          const u32 xb = xa0 + mt * 2048 + qs * 16384, xc = xb ^ 64;
          xq[4 * q] = lds128(xb); xq[4 * q + 1] = lds128(xc); xq[4 * q + 2] = lds128(xb + 8192); xq[4 * q + 3] = lds128(xc + 8192);
          qs = qs + 1 >= 5 ? 0 : qs + 1;
        }
      }
      BAR();
      const f4 r0 = __builtin_bit_cast(f4, lds128(L_RS + mh * 128 + g * 16)), r1 = __builtin_bit_cast(f4, lds128(L_RS + mh * 128 + 64 + g * 16));
      qrs = *(LAS float*)(L_RS + mt * 64 + lr * 4);
      half(fb, fa, 0, 0, 0, IC<0>{}, IC<0>{});
      MM_DONE();
      __builtin_amdgcn_s_setprio(PR);
      STAMP(2);
      const u32 EX = L_RING + 20480;
      const u32 ew = EX + mh * 10240 + nq * 3072 + l16;
      auto put = [&](auto N, auto PHI, u32 a) {
        constexpr int n = N;
        pin(acc[0][n]); pin(acc[1][n]);
        sts128(a, u4{phi2<PHI>(acc[0][n][0], acc[0][n][1], r0[0], r0[1]), phi2<PHI>(acc[0][n][2], acc[0][n][3], r0[2], r0[3]),
                     phi2<PHI>(acc[1][n][0], acc[1][n][1], r1[0], r1[1]), phi2<PHI>(acc[1][n][2], acc[1][n][3], r1[2], r1[3])});
      };
      put(IC<0>{}, IC<1>{}, ew); put(IC<1>{}, IC<1>{}, ew + 1024); put(IC<3>{}, IC<0>{}, ew + 5120); put(IC<4>{}, IC<0>{}, ew + 6144);
      if (nq == 0) put(IC<2>{}, IC<1>{}, ew + 2048); else put(IC<2>{}, IC<0>{}, ew + 4096);
      STAMP(41);
      BAR();
      STAMP(42);
      const u32 ex = EX + mt * 1024 + l16;
      u4 kf[2][2], vf[2][2];
#pragma unroll
      for (int m = 0; m < 2; m++) {
        kf[m][0] = lds128(ex + m * 10240); vf[m][0] = lds128(ex + m * 10240 + 5120);
        vf[m][1] = lds128(ex + m * 10240 + 6144); kf[m][1] = lds128(ex + m * 10240 + 1024);
      }
      const u32 o0 = lr == 0 ? ONE2 : 0u;
      const u4 ONE0 = {o0, o0, o0, o0};
#pragma unroll
      for (int q = 0; q < 2; q++) {
        f4 z4 = {0, 0, 0, 0};
        f4 kv0 = mfma(kf[1][q], vf[1][0], mfma(kf[0][q], vf[0][0], z4)), kv1 = mfma(kf[1][q], vf[1][1], mfma(kf[0][q], vf[0][1], z4));
        f4 zz = mfma(kf[1][q], ONE0, mfma(kf[0][q], ONE0, z4));
        sts64(lkv[3 * q], u2{pk2(kv0[0], kv0[1]), pk2(kv0[2], kv0[3])});
        sts64(lkv[3 * q + 1], u2{pk2(kv1[0], kv1[1]), pk2(kv1[2], kv1[3])});
        sts64(lkv[3 * q + 2], u2{pk2(zz[0], zz[1]), pk2(zz[2], zz[3])});
      }
      asm volatile("" ::: "memory");
      st128(lds128(L_RING + mt * 5120 + l16), rW, lane < 56 ? l16 : OOB, WS_KVP + ((j * 32 + p) * 4 + mt) * 896);
    }
    u32 pq[5][2];
    u2 xres[5];
    {
      const u32 qa0 = opaque(tr0 + 2 * 20480), qa1 = opaque(tr0 - L_RING);
      u4 wf[2][5];
#pragma unroll
      for (int n = 0; n < 5; n++) wf[0][n] = ldstr(qa0 + n * 32);
      waitv<0>();
      setflag(ws, WS_FKV + ((j * 4 + mt) * 32 + p) * 8, tag, lane);
      STAMP(3);
      sfor<0, 5>([&](auto N) { ldp2<N * 32>(xres[N], rX, lr * 1280 + g * 8, (p * 64 + mt * 16) * 1280 + j * 160); });
      f4 qa[5];
      sfor<0, 20>([&](auto S) {
        constexpr int s = S, t = s + 1;
        if constexpr (s == 7) BAR();
        waitl<6>();
        sfor<0, 5>([&](auto N) {
          constexpr int n = N;
          if constexpr (n == 2) waitl<(s < 19 ? 4 : 0)>();
          if constexpr (s == 0) mm0(qa[n], wf[0][n], xq[0]); else mm(qa[n], wf[s & 1][n], xq[s]);
          if constexpr (s < 19) wf[t & 1][n] = ldstr((t < 8 ? qa0 + t * 5120 : qa1 + (t - 8) * 5120) + n * 32);
        });
      });
      MM_DONE();
      STAMP(4);
#pragma unroll
      for (int n = 0; n < 5; n++) {
        pin(qa[n]);
        pq[n][0] = opaque(phi2<1>(qa[n][0], qa[n][1], qrs, qrs)); pq[n][1] = opaque(phi2<1>(qa[n][2], qa[n][3], qrs, qrs));
      }
    }
    STAMP(5);
    __builtin_amdgcn_s_setprio(2);
    STAMP(23);
    BAR();
    STAMP(6);
    {
      float o[20];
#pragma unroll
      for (int h = 0; h < 4; h++) {
        u4 bq = {pq[h][0], pq[h][1], pq[h + 1][0], pq[h + 1][1]};
        f4 z4 = {0, 0, 0, 0};
        f4 d0 = mfma(lds128(L_IMG + h * 2048 + l16), bq, z4);
        f4 d1 = mfma(lds128(L_IMG + h * 2048 + 1024 + l16), bq, z4);
        float rd = __builtin_amdgcn_rcpf(d1[1] + 1e-6f);
#pragma unroll
        for (int i = 0; i < 4; i++) o[5 * h + i] = d0[i] * rd;
        o[5 * h + 4] = d1[0] * rd;
      }
      u32 ov[10];
#pragma unroll
      for (int i = 0; i < 10; i++) ov[i] = pk2(o[2 * i], o[2 * i + 1]);
      publish20(ov, rW, l16, WS_OB + ((p * 4 + mt) * 20 + 5 * (j >> 1)) * 1024, j);
    }
    waitv<0>();
    setflag(ws, WS_FO + ((p * 4 + mt) * 8 + j) * 8, tag, lane);
    BAR();
    STAMP(7);
    {
      u4 of[2][10], wf[5];
      u2 oh[2][2][2];
      const u32 kh = mt & 1;
      const u32 so[2] = {WS_OB + (p * 4 + mt) * 20480 + kh * 5120, WS_OB + (p * 4 + (mt ^ 1)) * 20480 + kh * 5120};
      auto ldo = [&](auto S) {
        constexpr int s = S;
        sfor<0, 2>([&](auto M) {
          if constexpr (s % 5 == 2) { ldv2<2048>(oh[M][s / 5][0], rW, l8, so[M] + (s / 5) * 10240); ldv2<2560>(oh[M][s / 5][1], rW, l8, so[M] + (s / 5) * 10240); }
          else ldv<(s % 5 < 4 ? (s % 5) * 1024 : 0)>(of[M][s], rW, l16, so[M] + (s / 5) * 10240 + (s % 5 < 4 ? 0 : 4096));
        });
      };
      const u32 a0 = opaque(L_WO + kh * 5120 + l16), a1 = opaque(L_WO + 51200 + kh * 5120 + l16);
#pragma unroll
      for (int n = 0; n < 5; n++) wf[n] = lds128(a0 + n * 10240);
      PP pp;
      poll2(pp, rW, WS_FO + ((p * 4 + (mt & 2) + ((lane >> 2) & 1)) * 8 + 2 * kh + (lane & 1) + 2 * (lane & 2)) * 8, tag);
      STAMP(8);
      sfor<0, 3>(ldo);
      f4 ha[2][5];
      sfor<0, 10>([&](auto S) {
        constexpr int s = S, e = 3 + 2 * s < 10 ? 3 + 2 * s : 10, f = 5 + 2 * s < 10 ? 5 + 2 * s : 10;
        constexpr int yo = 2 * ((e - s - 1) + ((e + 2) / 5 - (s + 3) / 5));
        if constexpr (s == 4) { STAMP(38); lset(1, mt); lwait(2); STAMP(39); }
        waitvl<yo, 2>();
        if constexpr (s == 0) { pollend(pp); STAMP(37); }
        if constexpr (s == 8) lwait(1);
        sfor<0, 5>([&](auto N) {
          constexpr int n = N, t = s + 1;
          if constexpr (n == 3) waitl<(s < 9 ? 3 : 0)>();
          sfor<0, 2>([&](auto M) {
            if constexpr (s == 0) mm0(ha[M][n], wf[n], of[M][s]);
            else if constexpr (s % 5 == 2) mmh(ha[M][n], wf[n], oh[M][s / 5][0], oh[M][s / 5][1]);
            else mm(ha[M][n], wf[n], of[M][s]);
          });
          if constexpr (s < 9) wf[n] = lds128((t < 5 ? a0 : a1) + n * 10240 + (t % 5) * 1024);
          if constexpr (n == 1 && e < f) ldo(IC<e>{});
          if constexpr (n == 3 && e + 1 < f) ldo(IC<e + 1>{});
        });
      });
      MM_DONE();
      STAMP(40);
      {
        const u32 xw = (mt < 3 ? 35840 + mt * 5120 : 102400) + l16, xr = ((mt ^ 1) < 3 ? 35840 + (mt ^ 1) * 5120 : 102400) + l16;
#pragma unroll
        for (int n = 0; n < 5; n++) { pin(ha[1][n]); sts128(xw + n * 1024, __builtin_bit_cast(u4, ha[1][n])); }
        waitl<0>();
        *(volatile LAS u32*)(L_CB + 1536 + mt * 4) = (u32)tag;
        while (*(volatile LAS u32*)(L_CB + 1536 + (mt ^ 1) * 4) != (u32)tag) {}
        asm volatile("" ::: "memory");
#pragma unroll
        for (int n = 0; n < 5; n++) { pin(ha[0][n]); ha[0][n] += __builtin_bit_cast(f4, lds128(xr + n * 1024)); }
      }
      STAMP(9);
      u32 hres[10];
      float sum = 0.f, sq = 0.f;
#pragma unroll
      for (int n = 0; n < 5; n++) {
        hres[2 * n] = pk2(unp(xres[n].x) + f2{ha[0][n][0], ha[0][n][1]});
        hres[2 * n + 1] = pk2(unp(xres[n].y) + f2{ha[0][n][2], ha[0][n][3]});
      }
      publish20(hres, rW, l16, WS_HB + ((p * 4 + mt) * 20 + 5 * (j >> 1)) * 1024, j);
#pragma unroll
      for (int i = 0; i < 10; i++) { sum = dot2c(hres[i], ONE2, sum); sq = dot2c(hres[i], hres[i], sq); }
      st64(u2{asu(xsum16_32(sum)), asu(xsum16_32(sq))}, rW, g == 0 ? l8 : OOB, WS_ST + ((p * 4 + mt) * 8 + j) * 128);
#pragma unroll
      for (int n = 0; n < 5; n++) sts64(L_HR + (mt * 5 + n) * 512 + l8, u2{hres[2 * n], hres[2 * n + 1]});
      waitv<0>();
      setflag(ws, WS_FH + ((p * 4 + mt) * 8 + j) * 8, tag, lane);
      lset(3, mt);
      STAMP(10);
      lwait(4);
    }
    {
      constexpr int HD = HDV;
      const u32 pc = 2 * P + (mt >> 1), mc = 2 * (mt & 1);
      f4 ca[2][7];
      u4 hf[2][20], wf[7];
      u2 hh[2][4][2];
      const u32 so = WS_HB + (pc * 4 + mc) * 20480;
      auto ldh = [&](auto S) {
        constexpr int s = S;
        sfor<0, 2>([&](auto M) {
          if constexpr (s % 5 == 2) {
            ldv2<(s & 3) * 1024>(hh[M][s / 5][0], rW, l8, so + M * 20480 + (s & ~3) * 1024);
            ldv2<(s & 3) * 1024 + 512>(hh[M][s / 5][1], rW, l8, so + M * 20480 + (s & ~3) * 1024);
          } else ldv<(s & 3) * 1024>(hf[M][s], rW, l16, so + M * 20480 + (s & ~3) * 1024);
        });
      };
      const u32 a0 = opaque(L_W1A + l16), a1 = opaque(L_W1B + l16), a2 = opaque(L_W1B + 35840 + l16), a3 = opaque(L_W1B + 71680 + l16);
#pragma unroll
      for (int n = 0; n < 7; n++) wf[n] = lds128(a0 + n * 5120);
      PP pp;
      poll2(pp, rW, WS_FH + ((pc * 4 + mc) * 8 + lr) * 8, tag);
      STAMP(11);
      sfor<0, 3>(ldh);
      sfor<0, 20>([&](auto S) {
        constexpr int s = S, e1 = 3 + 2 * s < s + HD ? 3 + 2 * s : s + HD, e = e1 < 20 ? e1 : 20;
        constexpr int f1 = 5 + 2 * s < s + 1 + HD ? 5 + 2 * s : s + 1 + HD, f = f1 < 20 ? f1 : 20;
        constexpr int yo = 2 * ((e - s - 1) + ((e + 2) / 5 - (s + 3) / 5));
        if constexpr (s == 9) { STAMP(33); lwait(5); STAMP(34); }
        waitvl<yo, 3>();
        if constexpr (s == 0) { pollend(pp); STAMP(31); }
        if constexpr (s == 5) STAMP(32);
        if constexpr (s == 14) STAMP(35);
        sfor<0, 7>([&](auto N) {
          constexpr int n = N, t = s + 1;
          if constexpr (n == 4) waitl<(s < 19 ? 4 : 0)>();
          sfor<0, 2>([&](auto M) {
            if constexpr (s == 0) mm0(ca[M][n], wf[n], hf[M][s]);
            else if constexpr (s % 5 == 2) mmh(ca[M][n], wf[n], hh[M][s / 5][0], hh[M][s / 5][1]);
            else mm(ca[M][n], wf[n], hf[M][s]);
          });
          if constexpr (s < 19) wf[n] = lds128((t < 5 ? a0 : t < 10 ? a1 : t < 15 ? a2 : a3) + n * 5120 + (t % 5) * 1024);
          if constexpr (n == 1 && e < f) ldh(IC<e>{});
          if constexpr (n == 4 && e + 1 < f) ldh(IC<e + 1>{});
        });
      });
      MM_DONE();
      STAMP(36);
      lset(7, mt);
      lwait(6);
      STAMP(12);
      const u32 sh = WS_HID + (pc * 4 + mc) * 54 * 1024, fh = WS_FHID + (((pc * 16 + c) * 2 + (mt & 1)) * 2) * 8;
      const u2 s0v = lds64(L_CB + 2048 + mt * 256 + lr * 8), s1v = lds64(L_CB + 2048 + mt * 256 + 128 + lr * 8);
      const float sc[2] = {asf(s0v.x), asf(s1v.x)}, sm[2] = {asf(s0v.y), asf(s1v.y)};
      u32 hp[2][2][2];
      sfor<0, 7>([&](auto N) {
        constexpr int n = N;
        const f4 c4 = __builtin_bit_cast(f4, lds128(L_CB + n * 64 + g * 16)), b4 = __builtin_bit_cast(f4, lds128(L_CB + 512 + n * 64 + g * 16));
        if constexpr (n == 6) { waitv<2>(); setflag(ws, fh, tag, lane); STAMP(15); }
        sfor<0, 2>([&](auto M) {
          pin(ca[M][n]);
#pragma unroll
          for (int i = 0; i < 2; i++) {
            const f2 v = f2{ca[M][n][2 * i], ca[M][n][2 * i + 1]} * sc[M] + (f2{b4[2 * i], b4[2 * i + 1]} - sm[M] * f2{c4[2 * i], c4[2 * i + 1]});
            const f2 u = v * (-2.302208198f - 0.1029432397f * v * v);
            const f2 d = f2{__builtin_amdgcn_exp2f(u[0]), __builtin_amdgcn_exp2f(u[1])} + 1.f;
            hp[M][n & 1][i] = pk2(v * f2{__builtin_amdgcn_rcpf(d[0]), __builtin_amdgcn_rcpf(d[1])});
          }
          if constexpr (n & 1) st128(u4{hp[M][0][0], hp[M][0][1], hp[M][1][0], hp[M][1][1]}, rW, l16, sh + M * 54 * 1024 + ((t0 >> 1) + n / 2) * 1024);
          if constexpr (n == 6) st64(u2{hp[M][0][0], hp[M][0][1]}, rW, rho < 6 ? l8 + odd * 512 : OOB, sh + M * 54 * 1024 + (tl >> 1) * 1024);
        });
      });
      STAMP(29);
      waitv<0>();
      setflag(ws, fh + 8, tag, lane);
      __builtin_amdgcn_s_setprio(0);
      STAMP(13);
      lwait(8);
      const u32 wa = opaque(mt * 35840 + l16);
#pragma unroll
      for (int i = 0; i < 35; i++) w2f[i] = lds128(wa + i * 1024);
      lset(9, mt);
    }
  } else {
    STAMP(20);
    LD_EPOCH();
    u4 sel[2];
    u32 dst[2];
    auto setup = [&]() {
#pragma unroll
    for (int q = 0; q < 2; q++) {
      const u32 k = lr + 16 * q - 8 * g, one = (k & 1) ? 0x3F800000u : 0x00003F80u;
      sel[q] = u4{k < 2 ? one : 0u, (k >> 1) == 1 ? one : 0u, (k >> 1) == 2 ? one : 0u, (k >> 1) == 3 ? one : 0u};
      landed(sel[q]);
    }
#pragma unroll
    for (int h = 0; h < 2; h++) {
      const u32 qd = 2 * (lr + 16 * (2 * h + (g >> 1))) + (g & 1), e = (qd * 205) >> 10, dq = qd - e * 5;
      const u32 gs = dq + mt, grp = (e * 13) >> 6, ii = e - grp * 5;
      const u32 base = L_IMG + mt * 2048 + (gs & 3) * 256 + (gs >> 2) * 8;
      dst[h] = opaque(e < 20 ? base + (ii < 4 ? (4 * grp + ii) * 16 : 1024 + 4 * grp * 16) : (e == 20 ? base + 1024 + 16 : L_CB + 1792));
    }
    };
    if (mt < 3) {
      const rsrc_t rQ = __builtin_amdgcn_make_buffer_rsrc((void*)((const char*)wqkv - 4096), 0, 640 * 3840 + 4096, 0x00020000);
      u32 voffW[7];
#pragma unroll
      for (int i = 0; i < 7; i++) {
        const u32 pi = i < 5 ? i : (i == 5 ? mt : (mt < 2 ? mt + 3 : 4u));
        const u32 f = 64 * pi + lane, sg = (f * 6554) >> 16, ch = f - sg * 10;
        voffW[i] = (((sg & 12) << 1) + ((sg >> 4) << 2) + (sg & 3)) * 3840 + ch * 16 + 4096 - (i < 4 ? i * 1024 : 0);
      }
      const u32 soW = (mt >> 1) * 32 * 3840 + ((mt & 1) + 1) * 1280 + j * 160, soW3 = 32 * 3840 + 2560 + j * 160;
      const u32 soQ = mt * 32 * 3840 + j * 160, soQ3 = 96 * 3840 + j * 160;
      const u32 l5 = 15360 + mt * 1024, l6 = 15360 + (mt < 2 ? mt + 3 : 4u) * 1024;
      u32 st = rot, qs = rotq;
      auto issueW = [&](u32 so, u32 so3, u32 ld) {
        dma<0>(rQ, voffW[0], so, ld + mt * 5120); dma_c<1024>(rQ, voffW[1], so); dma_c<2048>(rQ, voffW[2], so); dma_c<3072>(rQ, voffW[3], so);
        dma<0>(rQ, voffW[4], so, ld + mt * 5120 + 4096); dma<0>(rQ, voffW[5], so3, ld + l5);
        if (mt < 2) dma<0>(rQ, voffW[6], so3, ld + l6);
      };
      auto issueKV = [&](u32 slot) { issueW(soW + st * 245760, soW3 + st * 245760, L_RING + slot * 20480); st = st + 1 >= 10 ? 0 : st + 1; };
      auto issueQ = [&](u32 ld) { issueW(soQ + qs * 491520, soQ3 + qs * 491520, ld); qs = qs + 1 >= 5 ? 0 : qs + 1; };
#pragma nounroll
      for (u32 i = 0; i < 4; i++) issueKV(i);
      STAMP(21);
      waitv<18>();
      BAR();
#pragma nounroll
      for (u32 i = 0; i < 6; i++) { waitv<12>(); asm volatile("s_barrier" ::: "memory"); issueKV(i & 3); }
      auto issueQa = [&](u32 slot) {
        const u32 so = soQ + qs * 491520;
        dma<0>(rQ, voffW[0], so, L_RING + slot * 20480 + mt * 5120); dma_c<1024>(rQ, voffW[1], so); dma_c<2048>(rQ, voffW[2], so);
      };
      auto issueQb = [&](u32 slot) {
        const u32 so = soQ + qs * 491520, so3 = soQ3 + qs * 491520, ld = L_RING + slot * 20480;
        dma<3072>(rQ, voffW[3], so, ld + mt * 5120); dma<0>(rQ, voffW[4], so, ld + mt * 5120 + 4096); dma<0>(rQ, voffW[5], so3, ld + l5);
        if (mt < 2) dma<0>(rQ, voffW[6], so3, ld + l6);
        qs = qs + 1 >= 5 ? 0 : qs + 1;
      };
      waitv<12>(); BAR(); issueQa(2);
      waitv<9>(); BAR(); issueQb(2);
      waitv<6>(); BAR(); issueQa(3);
      BAR();
      issueQb(3);
      issueQ(0);
      waitv<6>();
      BAR();
      GET_TAG();
      setup();
      issueQ(20480); issueQ(40960);
    } else {
      const bool m1 = b < 108, mo = b >= 216;
      const u32 v = m1 ? b : (mo ? b - 216 : b - 108), vq = m1 ? (v * 2428) >> 16 : (v * 6554) >> 16;
      const u32 sA = uni((m1 || mo) ? 5 * vq : v >> 1), cA = uni(m1 ? v - 27 * vq : (mo ? v - 10 * vq : 5 * (v & 1))), sQ = uni((m1 || mo) ? 1u : 0u);
      const u32 pitch = uni(m1 ? 3456 : 1280);
      const u64 src = m1 ? (u64)w1 : (mo ? (u64)wo : (u64)w2);
      const rsrc_t rS = __builtin_amdgcn_make_buffer_rsrc((void*)src, 0, mo ? 640 * 1280 : 640 * 3456, 0x00020000);
      u32 vrt[20];
#pragma unroll
      for (int I = 0; I < 20; I++) {
        const u32 q = I >> 2, i = I & 3, ro = 8 * i + (lane >> 3);
        const u32 i40 = 8 * q + 4 * (i >> 1) + (ro & 3), hf = i40 >= 20 ? 1u : 0u, id = i40 - 20 * hf;
        const u32 rp = opaque(80 * hf + 15 * ((id * 13) >> 6) + 5 * ((ro >> 2) & 3) + id);
        vrt[I] = opaque((mo ? rp : 32 * sQ * q + ro) * pitch + (lane & 7) * 16 + (q - sQ * q) * 128);
      }
      u4 ONES = {0x3F803F80u, 0x3F803F80u, 0x3F803F80u, 0x3F803F80u};
      landed(ONES);
      const u32 sc = L_RING + 16384;
      const u32 wa = sc + (lane >> 3) * 128 + (((lane & 7) ^ (((lane >> 4) & 3) << 1)) << 4);
      const u32 r0 = 4 * g + (lr >> 2), ta = opaque(sc + r0 * 128 + (lr & 1) * 8 + ((((lr >> 1) & 1) ^ (((r0 >> 1) & 3) << 1)) << 4));
      const u32 dT = uni(m1 ? 20480u : (mo ? 10240u : 55296u)), dQ = uni(sQ ? 1024u : 4 * 55296u);
      const u32 dA = uni((m1 ? WS_W1T : (mo ? WS_WOT + (sA >= 10 ? 40960u : 0u) : WS_W2T)) + 4 * cA * dT + sA * 1024);
      u32 dJ[4];
#pragma unroll
      for (int n = 0; n < 4; n++) dJ[n] = uni(dA + n * dT + (mo ? (((4 * cA + n) * 52) >> 8) * 51200u : 0u));
      waitv<0>();
      GET_TAG();
      setup();
      STAMP(21);
#pragma nounroll
      for (u32 i = 0; i < 8; i++) BAR();
      u4 f[5][4];
      sfor<0, 7>([&](auto I) { ldp<0>(f[I >> 2][I & 3], rS, vrt[I], 32 * sA * pitch + cA * 128); });
      BAR();
      sfor<7, 14>([&](auto I) { ldp<0>(f[I >> 2][I & 3], rS, vrt[I], 32 * sA * pitch + cA * 128); });
      BAR();
      sfor<14, 20>([&](auto I) { ldp<0>(f[I >> 2][I & 3], rS, vrt[I], 32 * sA * pitch + cA * 128); });
      BAR();
      f4 cs[4];
      sfor<0, 5>([&](auto Q) {
        constexpr int q = Q;
        waitv<16 - 4 * q>();
#pragma unroll
        for (int i = 0; i < 4; i++) { landed(f[q][i]); sts128(wa + i * 1024, f[q][i]); }
        asm volatile("" ::: "memory");
#pragma unroll
        for (int n = 0; n < 4; n++) f[q][n] = ldstr(ta ^ (n << 5), (ta ^ (n << 5)) + 2048);
        asm volatile("" ::: "memory");
#pragma unroll
        for (int n = 0; n < 4; n++) cs[n] = mfma(f[q][n], ONES, q == 0 ? f4{0, 0, 0, 0} : cs[n]);
      });
      STAMP(22);
#pragma unroll
      for (int q = 0; q < 5; q++) {
        if (q == RTE) { BAR(); if (E2) BAR(); }
#pragma unroll
        for (int n = 0; n < 4; n++) st128(f[q][n], rW, l16, dJ[n] + q * dQ);
      }
#pragma unroll
      for (int n = 0; n < 4; n++) st128(__builtin_bit_cast(u4, cs[n]), rW, (m1 && lr == 0) ? g * 16 : OOB, WS_CS1 + vq * 6912 + (4 * cA + n) * 64);
      if (RTE == 5) { BAR(); if (E2) BAR(); }
    }
    STAMP(24);
    if (mt < 3) waitv<0>();
    BAR();
    if (mt == 3) { waitv<0>(); setflag(ws, WS_FRT + b * 8, tag, lane); }
    STAMP(4);
    PP pp;
    poll2(pp, rW, WS_FKV + ((j * 4 + mt) * 32 + (lane & 31)) * 8, tag);
    STAMP(5);
    {
      const u32 so = WS_KVP + (j * 32 * 4 + mt) * 896;
      const u32 v0 = lane < 53 ? l16 : OOB;
      u4 a[32], fr[2];
      auto lda = [&](auto Q) { ldv<(Q & 1) * 3584>(a[Q], rW, v0, so + (Q & ~1) * 3584); };
      sfor<0, 14>(lda);
      sts128(L_IMG + mt * 2048 + l16, u4{0, 0, 0, 0}); sts128(L_IMG + mt * 2048 + 1024 + l16, u4{0, 0, 0, 0});
      const bool zrow = (dst[1] & 0x430) == 0x410 && dst[1] < L_CB;
      f4 s0, s1, s2, s3;
      sfor<0, 16>([&](auto Q) {
        waitv<(Q < 9 ? 12 : 32 - 2 * Q)>();
        if constexpr (Q == 0) pollend(pp);
        if constexpr (Q == 0) { mm0(s0, sel[0], a[0]); mm0(s1, sel[1], a[0]); mm0(s2, sel[0], a[1]); mm0(s3, sel[1], a[1]); }
        else { mm(s0, sel[0], a[2 * Q]); mm(s1, sel[1], a[2 * Q]); mm(s2, sel[0], a[2 * Q + 1]); mm(s3, sel[1], a[2 * Q + 1]); }
        if constexpr (Q < 9) { lda(IC<14 + 2 * Q>{}); lda(IC<15 + 2 * Q>{}); }
        if constexpr (Q == 8) { ldv<0>(fr[0], rW, lane * 32, WS_FRT); ldv<16>(fr[1], rW, lane * 32, WS_FRT); }
      });
      MM_DONE();
      pin(s0); pin(s1); pin(s2); pin(s3);
      s0 += s2; s1 += s3;
      const u2 v1 = {pk2(s1[0], s1[1]), pk2(s1[2], s1[3])};
      sts64(dst[0], u2{pk2(s0[0], s0[1]), pk2(s0[2], s0[3])});
      sts64(dst[1], v1);
      if (zrow) { sts64(dst[1] + 64, v1); sts64(dst[1] + 128, v1); sts64(dst[1] + 192, v1); }
      STAMP(6);
      BAR();
      RT_CHECK();
    }
    auto issueW1 = [&](u32 it, u32 n, u32 ld) {
      const u32 so = WS_W1T + (n < 6 ? t0 + n : tl) * 20480 + it * 5120;
      dma4_sc1(rW, l16, so, ld + n * 5120); dma1_sc1(rW, l16, so + 4096, ld + n * 5120 + 4096);
    };
    const u32 o = 51200 + mt * 12288, o2 = 51200 + 49152 + (mt & 1) * 1024, sw = WS_WOT + j * 102400;
    {
      const u32 p0 = mt * 12288, p2 = 49152 + (mt & 1) * 1024;
      dma4_sc1(rW, l16, sw + p0, L_WO + p0); dma4_sc1(rW, l16, sw + p0 + 4096, L_WO + p0 + 4096); dma4_sc1(rW, l16, sw + p0 + 8192, L_WO + p0 + 8192);
      dma1_sc1(rW, l16, sw + p2, L_WO + p2);
    }
    if (b == 0 && tid == 256) __hip_atomic_store((GAS u64*)ws, tag, __ATOMIC_RELAXED, __HIP_MEMORY_SCOPE_AGENT);
    {
      f4 csum;
      u32 ci = lane + 64 * (mt & 1); ci = ci > 111 ? 111 : ci;
      const u32 n = ci >> 4, col = (n < 6 ? t0 + n : tl) * 16 + (ci & 15);
#pragma unroll
      for (int q = 0; q < 4; q++) csum[q] = asf(__builtin_amdgcn_raw_buffer_load_b32(rW, col * 4, WS_CS1 + q * 6912, SC1));
      const u32 bv = b1[col], b2v = ((const u32*)b2)[40 * j + (lane < 40 ? lane : 0)];
      sts32(L_CB + ci * 4, (csum[0] + csum[1]) + (csum[2] + csum[3]));
      sts32(L_CB + 512 + ci * 4, blo(bv));
      if (lane < 40) *(LAS u32*)(L_CB + 1024 + lane * 4) = b2v;
    }
    waitv<0>();
    BAR();
    STAMP(9);
    auto w1t = [&](u32 it, u32 n, u32 ld) { waitv<TH>(); issueW1(it, n, ld); };
    auto w2t = [&](auto I) { waitv<TH + 3>(); ldw2(I); };
    dma4_sc1(rW, l16, sw + o, L_WO + o); waitv<TH>(); dma4_sc1(rW, l16, sw + o + 4096, L_WO + o + 4096);
    waitv<TH>(); dma4_sc1(rW, l16, sw + o + 8192, L_WO + o + 8192); dma1_sc1(rW, l16, sw + o2, L_WO + o2);
    w1t(0, mt, L_W1A);
    waitv<5>(); lset(2, mt);
    STAMP(10);
    w1t(0, mt < 3 ? mt + 4 : 6, L_W1A);
    sfor<0, HA>(w2t);
    lwait(1);
    STAMP(7);
    w1t(1, mt, L_W1B); w1t(1, mt < 3 ? mt + 4 : 6, L_W1B);
    sfor<HA, HA + 4>(w2t);
    waitv<4>(); lset(4, mt);
    STAMP(25);
    sfor<HA + 4, HB2>(w2t);
    lwait(3);
    STAMP(8);
#pragma nounroll
    for (u32 u = mt; u < 14; u += 4) { const u32 it = u >= 7 ? 1u : 0u; w1t(it + 2, u - 7 * it, L_W1B + 35840 + it * 35840); }
    waitv<0>(); lset(5, mt);
    STAMP(28);
    {
      const u32 pc = 2 * P + (mt >> 1), mc = 2 * (mt & 1);
      poll(ws, WS_FH + ((pc * 4 + mc) * 8 + lr) * 8, tag);
#pragma unroll
      for (int m = 0; m < 2; m++) {
        const u2 v0 = __builtin_amdgcn_raw_buffer_load_b64(rW, g * 256 + lr * 8, WS_ST + (pc * 4 + mc + m) * 1024, SC1);
        const u2 v1 = __builtin_amdgcn_raw_buffer_load_b64(rW, g * 256 + lr * 8 + 128, WS_ST + (pc * 4 + mc + m) * 1024, SC1);
        const float mean = xsum16_32(asf(v0.x) + asf(v1.x)) * (1.f / 640.f);
        const float var = fmaxf(xsum16_32(asf(v0.y) + asf(v1.y)) * (1.f / 640.f) - mean * mean, 0.f);
        const float m1 = rbf(mean), sd1 = rbf(__builtin_amdgcn_sqrtf(rbf(rbf(var) + 1e-5f))), r1 = __builtin_amdgcn_rcpf(sd1);
        const float m2 = rbf((mean - m1) * r1), sd2 = rbf(__builtin_amdgcn_sqrtf(rbf(rbf(var * r1 * r1) + 1e-5f)));
        const float sc = r1 * __builtin_amdgcn_rcpf(sd2);
        sts64(L_CB + 2048 + mt * 256 + m * 128 + lr * 8, u2{asu(sc), asu(sc * (m1 + m2 * sd1))});
      }
    }
    lset(6, mt);
    STAMP(27);
    sfor<HB2, 35>(w2t);
    STAMP(26);
    lwait(7);
    {
      const u32 so = WS_W2T + (j * 5 * 54 + 7 * mt) * 1024, ld = mt * 35840;
#pragma nounroll
      for (u32 n = 0; n < 5; n++) {
        waitv<TH2>();
        sfor<0, 7>([&](auto S) {
          constexpr int k = S < 2 ? S : (S < 4 ? S + 2 : (S < 6 ? S - 2 : 6));
          dma1_sc1(rW, l16, so + (n * 54 + k) * 1024, ld + (S * 5 + n) * 1024);
        });
      }
    }
    waitv<0>();
    lset(8, mt);
    STAMP(30);
  }
  {
    f4 da[4][5];
    u4 xf[7][4];
    u2 xh[4][2];
    const u32 so = WS_HID + (p * 4 * 54 + s0) * 1024;
    const u32 vh0 = w < 6 ? l8 : l16 + 2048, vh1 = w < 6 ? l8 + 512 : l16 + 2056;
    auto ldx = [&](auto U) {
      constexpr int s = U / 4, a = U % 4;
      if constexpr (s < 2) ldv<s * 1024>(xf[s][a], rW, l16, so + a * 54 * 1024);
      else if constexpr (s < 4) ldv<(s - 2) * 1024>(xf[s][a], rW, vmid, so + (a * 54 + 3) * 1024);
      else if constexpr (s == 4) ldv<2048>(xf[s][a], rW, l16, so + a * 54 * 1024);
      else if constexpr (s == 5) { ldv2<0>(xh[a][0], rW, vh0, so + (a * 54 + 3) * 1024); ldv2<0>(xh[a][1], rW, vh1, so + (a * 54 + 3) * 1024); }
      else ldv<0>(xf[s][a], rW, vlast, so + (a * 54 + 3) * 1024);
    };
    const u32 fo = WS_FHID + ((p * 16 + 2 * w) * 2 + (lane & 3)) * 16;
    sfor<0, 2>([&](auto H) {
      constexpr int u0 = H ? 16 : 0, u1 = H ? 28 : 16, XD = H ? 12 : 6;
      PP pp;
      poll2(pp, rW, fo + H * 8, tag);
      STAMP(14 + 2 * H);
      sfor<u0, u0 + XD>(ldx);
      sfor<u0, u1>([&](auto U) {
        constexpr int u = U, s = u / 4, a = u % 4;
        constexpr int e0 = u + XD + (u >= 4 ? 4 : 0), e = e0 < u1 ? e0 : u1;
        constexpr int f0 = u + 1 + XD + (u + 1 >= 4 ? 4 : 0), f = f0 < u1 ? f0 : u1;
        constexpr int lo5 = u + 1 > 20 ? u + 1 : 20, hi5 = e < 24 ? e : 24;
        waitv<(e - u - 1) + (hi5 > lo5 ? hi5 - lo5 : 0)>();
        if constexpr (u == u0) pollend(pp);
        sfor<0, 5>([&](auto N) {
          if constexpr (s == 0) mm0(da[a][N], w2f[N], xf[s][a]);
          else if constexpr (s == 5) mmh(da[a][N], w2f[5 * s + N], xh[a][0], xh[a][1]);
          else mm(da[a][N], w2f[5 * s + N], xf[s][a]);
        });
        sfor<e, f>(ldx);
      });
    });
    MM_DONE();
    STAMP(17);
    lwait(9);
#pragma unroll
    for (int a = 0; a < 4; a++)
#pragma unroll
      for (int n = 0; n < 5; n++) {
        pin(da[a][n]);
        sts64(L_RED + ((a * 5 + n) * 4 + (w >> 1)) * 1024 + l16 + (w & 1) * 8, u2{pk2(da[a][n][0], da[a][n][1]), pk2(da[a][n][2], da[a][n][3])});
      }
    const rsrc_t rOut = __builtin_amdgcn_make_buffer_rsrc((void*)out, 0, 2048 * 1280, 0x00020000);
    const u32 LO = opaque(0x00003F80u), HI = opaque(0x3F800000u), n0 = 3 * nh;
    const u32 ra = opaque(L_RED + (mt * 5 + n0) * 4096 + l16);
    u2 bb[3], hr[3];
#pragma unroll
    for (int i = 0; i < 3; i++) { bb[i] = lds64(L_CB + 1024 + (n0 + i) * 32 + g * 8); hr[i] = lds64(L_HR + (mt * 5 + n0 + i) * 512 + l8); }
    BAR();
    STAMP(18);
    u4 pv[3][4];
#pragma unroll
    for (int i = 0; i < 3; i++)
#pragma unroll
      for (int q = 0; q < 4; q++) pv[i][q] = lds128(ra + i * 4096 + q * 1024);
#pragma unroll
    for (int i = 0; i < 3; i++) {
      float s0 = 0.f, s1 = 0.f, s2 = 0.f, s3 = 0.f;
#pragma unroll
      for (int q = 0; q < 4; q++) {
        s0 = dot2c(pv[i][q].x, LO, s0); s1 = dot2c(pv[i][q].x, HI, s1); s2 = dot2c(pv[i][q].y, LO, s2); s3 = dot2c(pv[i][q].y, HI, s3);
        s0 = dot2c(pv[i][q].z, LO, s0); s1 = dot2c(pv[i][q].z, HI, s1); s2 = dot2c(pv[i][q].w, LO, s2); s3 = dot2c(pv[i][q].w, HI, s3);
      }
      const u32 o0 = pk2(unp(hr[i].x) + unp(pk2(f2{s0, s1} + unp(bb[i].x)))), o1 = pk2(unp(hr[i].y) + unp(pk2(f2{s2, s3} + unp(bb[i].y))));
      __builtin_amdgcn_raw_buffer_store_b64(u2{o0, o1}, rOut, (i == 2 && helper) ? OOB : lr * 1280 + g * 8 + i * 32, (p * 64 + mt * 16) * 1280 + j * 160 + n0 * 32, 0);
    }
  }
}

extern "C" void kernel_launch(void* const* d_in, const int* in_sizes, int n_in, void* d_out, int out_size, void* d_ws, size_t ws_size,
                              hipStream_t stream) {
  fused_block<<<256, 512, 0, stream>>>((const u16*)d_in[0], (const u16*)d_in[1], (const u16*)d_in[2], (const u16*)d_in[4], (const u16*)d_in[5],
                                       (const u16*)d_in[6], (const u16*)d_in[7], (u16*)d_out, (char*)d_ws);
}
```

```cpp
#include <hip/hip_runtime.h>
#include <stdint.h>
#pragma clang diagnostic ignored "-Wint-to-pointer-cast"
#pragma clang diagnostic ignored "-Warray-bounds"

typedef unsigned short u16;
typedef unsigned u32;
typedef unsigned long long u64;
typedef float f2 __attribute__((ext_vector_type(2)));
typedef float f4 __attribute__((ext_vector_type(4)));
typedef unsigned u2 __attribute__((ext_vector_type(2)));
typedef unsigned u4 __attribute__((ext_vector_type(4)));
typedef short s4 __attribute__((ext_vector_type(4)));
typedef __bf16 bf2 __attribute__((ext_vector_type(2)));
typedef __bf16 bf8 __attribute__((ext_vector_type(8)));
typedef __amdgpu_buffer_rsrc_t rsrc_t;
#define LAS __attribute__((address_space(3)))
#define GAS __attribute__((address_space(1)))
#define DI __device__ __forceinline__

template <int I> struct IC { static constexpr int v = I; constexpr operator int() const { return I; } };
template <int B, int E, class F> DI void sfor(F&& f) {
  if constexpr (B < E) { f(IC<B>{}); sfor<B + 1, E>(f); }
}

constexpr u32 WS_FRT = 4096, WS_FKV = 8192, WS_FO = 16384, WS_FH = 24576, WS_FHID = 32768;
constexpr u32 WS_KVP = 65536;
constexpr u32 WS_OB = 1u << 20;
constexpr u32 WS_HB = WS_OB + 32 * 81920;
constexpr u32 WS_ST = WS_HB + 32 * 81920;
constexpr u32 WS_CS1 = WS_ST + 2048 * 64;
constexpr u32 WS_WOT = WS_CS1 + 32768;
constexpr u32 WS_W1T = WS_WOT + 40 * 20480;
constexpr u32 WS_W2T = WS_W1T + 108 * 20480;
constexpr u32 WS_HID = WS_W2T + 8 * 54 * 5120;
constexpr u32 WS_END = WS_HID + 32 * 4 * 54 * 1024;
constexpr u32 WS_PROF = WS_END;
constexpr u32 OOB = 0x7FFFFF00u;
constexpr int SC1 = 16;
#define MAP 1
#define ROT 2
#define WQ 6
#define E2 0
#define PR 0
#define DMAP 0
#define OP 6
#define PS 4
#define RTE 5
#define HDV 8
#define TH2 4
#define TH 4
#define HA 8
#define HB2 12
#define CW 5
#define CG 32

constexpr u32 L_X = 0, L_RING = 81920;
constexpr u32 L_WO = 0, L_IMG = 147456, L_W1A = 110592, L_HR = 147456, L_CB = 160768, L_W1B = 0, L_RED = 0;

DI f4 mfma(u4 a, u4 b, f4 c) {
  return __builtin_amdgcn_mfma_f32_16x16x32_bf16(__builtin_bit_cast(bf8, a), __builtin_bit_cast(bf8, b), c, 0, 0, 0);
}
DI void mm(f4& c, u4 a, u4 b) { asm volatile("v_mfma_f32_16x16x32_bf16 %0, %1, %2, %0" : "+v"(c) : "v"(a), "v"(b) : "memory"); }
DI void mm0(f4& c, u4 a, u4 b) { asm volatile("v_mfma_f32_16x16x32_bf16 %0, %1, %2, 0" : "=&v"(c) : "v"(a), "v"(b) : "memory"); }
#define MM_DONE() asm volatile("s_nop 7\n\ts_nop 1" ::: "memory")
DI void pin(f4& c) { asm volatile("" : "+v"(c)); }
DI void zero(f4& c) { c = f4{0, 0, 0, 0}; asm volatile("" : "+v"(c)); }
DI void undef(u4& d) { asm volatile("" : "=v"(d)); }
DI u32 uni(u32 v) { v = __builtin_amdgcn_readfirstlane(v); asm volatile("" : "+s"(v)); return v; }
DI u32 opaque(u32 v) { asm volatile("" : "+v"(v)); return v; }

DI u32 pk2(float a, float b) { f2 v = {a, b}; return __builtin_bit_cast(u32, __builtin_convertvector(v, bf2)); }
DI u32 pk2(f2 v) { return __builtin_bit_cast(u32, __builtin_convertvector(v, bf2)); }
DI float rbf(float a) { return __builtin_bit_cast(float, pk2(a, a) << 16); }
DI float blo(u32 v) { return __builtin_bit_cast(float, v << 16); }
DI float bhi(u32 v) { return __builtin_bit_cast(float, v & 0xFFFF0000u); }
DI f2 unp(u32 v) { return f2{blo(v), bhi(v)}; }
DI float asf(u32 v) { return __builtin_bit_cast(float, v); }
DI u32 asu(float v) { return __builtin_bit_cast(u32, v); }

template <int OFF> DI void dma(rsrc_t r, u32 voff, u32 soff, u32 lds) {
  asm volatile("s_mov_b32 m0, %0\n\ts_nop 0\n\tbuffer_load_dwordx4 %1, %2, %3 offen offset:%4 lds" ::"s"(lds), "v"(voff), "s"(r), "s"(soff), "n"(OFF) : "memory");
}
template <int OFF> DI void dma_c(rsrc_t r, u32 voff, u32 soff) {
  asm volatile("buffer_load_dwordx4 %0, %1, %2 offen offset:%3 lds" ::"v"(voff), "s"(r), "s"(soff), "n"(OFF) : "memory");
}
DI void dma4_sc1(rsrc_t r, u32 voff, u32 soff, u32 lds) {
  asm volatile("s_mov_b32 m0, %0\n\ts_nop 0\n\tbuffer_load_dwordx4 %1, %2, %3 offen sc1 lds\n\tbuffer_load_dwordx4 %1, %2, %3 offen offset:1024 sc1 lds\n\t"
               "buffer_load_dwordx4 %1, %2, %3 offen offset:2048 sc1 lds\n\tbuffer_load_dwordx4 %1, %2, %3 offen offset:3072 sc1 lds" ::"s"(lds), "v"(voff), "s"(r), "s"(soff) : "memory");
}
DI void dma2_sc1(rsrc_t r, u32 voff, u32 soff, u32 lds) {
  asm volatile("s_mov_b32 m0, %0\n\ts_nop 0\n\tbuffer_load_dwordx4 %1, %2, %3 offen sc1 lds\n\tbuffer_load_dwordx4 %1, %2, %3 offen offset:1024 sc1 lds" ::"s"(lds), "v"(voff), "s"(r), "s"(soff) : "memory");
}
DI void dma1_sc1(rsrc_t r, u32 voff, u32 soff, u32 lds) {
  asm volatile("s_mov_b32 m0, %0\n\ts_nop 0\n\tbuffer_load_dwordx4 %1, %2, %3 offen sc1 lds" ::"s"(lds), "v"(voff), "s"(r), "s"(soff) : "memory");
}
template <int OFF> DI void ldv(u4& d, rsrc_t r, u32 voff, u32 soff) {
  asm volatile("buffer_load_dwordx4 %0, %1, %2, %3 offen offset:%4 sc1" : "=v"(d) : "v"(voff), "s"(r), "s"(soff), "n"(OFF) : "memory");
}
template <int OFF> DI void ldv_rw(u4& d, rsrc_t r, u32 voff, u32 soff) {
  asm volatile("buffer_load_dwordx4 %0, %1, %2, %3 offen offset:%4 sc1" : "+v"(d) : "v"(voff), "s"(r), "s"(soff), "n"(OFF) : "memory");
}
template <int OFF> DI void ldp(u4& d, rsrc_t r, u32 voff, u32 soff) {
  asm volatile("buffer_load_dwordx4 %0, %1, %2, %3 offen offset:%4" : "=v"(d) : "v"(voff), "s"(r), "s"(soff), "n"(OFF) : "memory");
}
template <int OFF> DI void ldp2(u2& d, rsrc_t r, u32 voff, u32 soff) {
  asm volatile("buffer_load_dwordx2 %0, %1, %2, %3 offen offset:%4" : "=v"(d) : "v"(voff), "s"(r), "s"(soff), "n"(OFF) : "memory");
}
template <int N> DI void waitv() { asm volatile("s_waitcnt vmcnt(%0)" ::"n"(N) : "memory"); }
template <class T> DI void landed(T& d) { asm volatile("" : "+v"(d)); }
#define BAR() do { asm volatile("" ::: "memory"); __builtin_amdgcn_s_waitcnt(0xC07F); asm volatile("s_barrier" ::: "memory"); } while (0)
DI u4 lds128(u32 a) { return *(LAS u4*)(a); }
DI u2 lds64(u32 a) { return *(LAS u2*)(a); }
DI void sts128(u32 a, u4 v) { *(LAS u4*)(a) = v; }
DI void sts64(u32 a, u2 v) { *(LAS u2*)(a) = v; }
DI void sts32(u32 a, float v) { *(LAS float*)(a) = v; }
DI u4 ldstr(u32 a0, u32 a1) {
  u2 x = __builtin_bit_cast(u2, __builtin_amdgcn_ds_read_tr16_b64_v4i16((LAS s4*)(a0)));
  u2 y = __builtin_bit_cast(u2, __builtin_amdgcn_ds_read_tr16_b64_v4i16((LAS s4*)(a1)));
  return u4{x.x, x.y, y.x, y.y};
}
DI u4 ldstr(u32 a) { return ldstr(a, a + 2560); }
DI float xsum16_32(float v) {
  auto r = __builtin_amdgcn_permlane16_swap(asu(v), asu(v), false, false);
  v = asf((u32)r[0]) + asf((u32)r[1]);
  auto q = __builtin_amdgcn_permlane32_swap(asu(v), asu(v), false, false);
  return asf((u32)q[0]) + asf((u32)q[1]);
}
DI float xsum8(float v) {
  v += asf(__builtin_amdgcn_update_dpp(0, asu(v), 0xB1, 0xf, 0xf, true));
  v += asf(__builtin_amdgcn_update_dpp(0, asu(v), 0x4E, 0xf, 0xf, true));
  return v + asf(__builtin_amdgcn_update_dpp(0, asu(v), 0x141, 0xf, 0xf, true));
}
DI void poll(const char* ws, u32 off, u64 tag) {
  const GAS u64* f = (const GAS u64*)(ws + off);
#pragma nounroll
  for (u32 it = 0; it < (1u << 18); ++it) {
    u64 v = __hip_atomic_load(f, __ATOMIC_RELAXED, __HIP_MEMORY_SCOPE_AGENT);
    if (__all(v == tag)) break;
    __builtin_amdgcn_s_sleep(2);
  }
  __builtin_amdgcn_fence(__ATOMIC_ACQUIRE, "wavefront");
  asm volatile("" ::: "memory");
}
DI void lset_(u32 k, u32 mt, u32 tg) { __builtin_amdgcn_s_waitcnt(0xC07F); *(volatile LAS u32*)(L_CB + 1536 + k * 16 + mt * 4) = tg; asm volatile("" ::: "memory"); }
DI void lwait_(u32 k, u32 tg) {
  asm volatile("" ::: "memory");
  for (;;) {
    const u4 v = *(volatile LAS u4*)(L_CB + 1536 + k * 16);
    if (__builtin_amdgcn_readfirstlane((v.x ^ tg) | (v.y ^ tg) | (v.z ^ tg) | (v.w ^ tg)) == 0) break;
    __builtin_amdgcn_s_sleep(1);
  }
  asm volatile("" ::: "memory");
}
#define lset(k, mt) lset_(k, mt, (u32)tag)
#define lwait(k) lwait_(k, (u32)tag)
struct PP { u2 a, b; };
DI void ldf(u2& d, rsrc_t r, u32 voff) { asm volatile("buffer_load_dwordx2 %0, %1, %2, 0 offen sc1" : "+v"(d) : "v"(voff), "s"(r) : "memory"); }
DI void poll2(PP& pp, rsrc_t r, u32 voff, u64 tag) {
  const u32 tl = (u32)tag, th = (u32)(tag >> 32);
  asm volatile("" : "=v"(pp.a), "=v"(pp.b));
  ldf(pp.a, r, voff);
  __builtin_amdgcn_s_sleep(PS);
#pragma nounroll
  for (u32 it = 0; it < (1u << 18); ++it) {
    ldf(pp.b, r, voff);
    waitv<1>(); landed(pp.a);
    if (!__any(((pp.a.x ^ tl) | (pp.a.y ^ th)) != 0)) break;
    ldf(pp.a, r, voff);
    waitv<1>(); landed(pp.b);
    if (!__any(((pp.b.x ^ tl) | (pp.b.y ^ th)) != 0)) break;
  }
  asm volatile("" ::: "memory");
}
DI void pollend(PP& pp) { asm volatile("" ::"v"(pp.a), "v"(pp.b)); }
DI void setflag(char* ws, u32 off, u64 tag, u32 lane) {
  if (lane == 0) __hip_atomic_store((GAS u64*)(ws + off), tag, __ATOMIC_RELAXED, __HIP_MEMORY_SCOPE_AGENT);
  asm volatile("" ::: "memory");
}
template <bool PHI> DI u32 phi2(float a0, float a1, float r0, float r1) {
  const f2 a = {a0, a1}, r = {r0, r1};
  if constexpr (!PHI) return pk2(a * r);
  const f2 t = a * r + 1e-6f, u = a * (r * 1.4426950408889634f);
  return pk2(fmaxf(t[0], 1e-6f) + __builtin_amdgcn_fmed3f(__builtin_amdgcn_exp2f(u[0]), 0.f, 1.f),
             fmaxf(t[1], 1e-6f) + __builtin_amdgcn_fmed3f(__builtin_amdgcn_exp2f(u[1]), 0.f, 1.f));
}
DI float dot2c(u32 a, u32 b, float c) { return __builtin_amdgcn_fdot2_f32_bf16(__builtin_bit_cast(bf2, a), __builtin_bit_cast(bf2, b), c, false); }
DI u4 center8(u4 x, u32 lo, u32 hi, float nm) {
  u4 r; float t0, t1, t2, t3, t4, t5, t6, t7;
  asm("v_dot2_f32_bf16 %4, %12, %16, %18\n\tv_dot2_f32_bf16 %5, %12, %17, %18\n\tv_dot2_f32_bf16 %6, %13, %16, %18\n\tv_dot2_f32_bf16 %7, %13, %17, %18\n\t"
      "v_dot2_f32_bf16 %8, %14, %16, %18\n\tv_dot2_f32_bf16 %9, %14, %17, %18\n\tv_dot2_f32_bf16 %10, %15, %16, %18\n\tv_dot2_f32_bf16 %11, %15, %17, %18\n\t"
      "v_cvt_pk_bf16_f32 %0, %4, %5\n\tv_cvt_pk_bf16_f32 %1, %6, %7\n\tv_cvt_pk_bf16_f32 %2, %8, %9\n\tv_cvt_pk_bf16_f32 %3, %10, %11"
      : "=v"(r.x), "=v"(r.y), "=v"(r.z), "=v"(r.w), "=&v"(t0), "=&v"(t1), "=&v"(t2), "=&v"(t3), "=&v"(t4), "=&v"(t5), "=&v"(t6), "=&v"(t7)
      : "v"(x.x), "v"(x.y), "v"(x.z), "v"(x.w), "v"(lo), "v"(hi), "v"(nm));
  return r;
}
template <int N> DI void waitl() { asm volatile("" ::: "memory"); __builtin_amdgcn_s_waitcnt(0xC07F | (N << 8)); asm volatile("" ::: "memory"); }
DI void st128(u4 v, rsrc_t r, u32 voff, u32 soff) {
  __builtin_amdgcn_raw_buffer_store_b128(v, r, voff, soff, SC1);
  asm volatile("s_nop 1" ::"v"(v) : "memory");
}
DI void st64(u2 v, rsrc_t r, u32 voff, u32 soff) { __builtin_amdgcn_raw_buffer_store_b64(v, r, voff, soff, SC1); }
DI void publish20(const u32* v, rsrc_t r, u32 l16, u32 so, u32 j) {
  if ((j & 1) == 0) {
    st128(u4{v[0], v[1], v[2], v[3]}, r, l16, so);
    st128(u4{v[4], v[5], v[6], v[7]}, r, l16, so + 1024);
    st64(u2{v[8], v[9]}, r, l16 >> 1, so + 2048);
  } else {
    st64(u2{v[0], v[1]}, r, l16 >> 1, so + 2560);
    st128(u4{v[2], v[3], v[4], v[5]}, r, l16, so + 3072);
    st128(u4{v[6], v[7], v[8], v[9]}, r, l16, so + 4096);
  }
}
template <int OFF> DI void ldv2(u2& d, rsrc_t r, u32 voff, u32 soff) {
  asm volatile("buffer_load_dwordx2 %0, %1, %2, %3 offen offset:%4 sc1" : "=v"(d) : "v"(voff), "s"(r), "s"(soff), "n"(OFF) : "memory");
}
DI void mmh(f4& c, u4 a, u2 b0, u2 b1) { mm(c, a, u4{b0.x, b0.y, b1.x, b1.y}); }
template <int V, int L> DI void waitvl() {
  asm volatile("" ::: "memory"); __builtin_amdgcn_s_waitcnt(0x0070 | (V & 15) | ((V >> 4) << 14) | (L << 8)); asm volatile("" ::: "memory");
}

#define STAMP(i)

__global__ __launch_bounds__(512, 2) void fused_block(const u16* __restrict__ x, const u16* __restrict__ wqkv, const u16* __restrict__ wo,
                                                      const u16* __restrict__ w1, const u16* __restrict__ b1, const u16* __restrict__ w2,
                                                      const u16* __restrict__ b2, u16* __restrict__ out, char* __restrict__ ws) {
  __shared__ __attribute__((aligned(1024))) char smem[163840];
  asm volatile("" ::"v"((u32)(uintptr_t)(LAS char*)smem));
  const u32 tid = threadIdx.x, lane = tid & 63, lr = lane & 15, g = lane >> 4;
  const u32 w = __builtin_amdgcn_readfirstlane(tid >> 6), mt = w & 3, nh = w >> 2;
  const bool helper = nh != 0;
  const u32 b = blockIdx.x, xg = b & 7, rr = b >> 3, p = 8 * (xg >> 1) + (rr >> 2), j = 4 * (xg & 1) + (rr & 3);
  asm volatile("" ::"s"(x), "s"(wqkv), "s"(wo), "s"(w1), "s"(b1), "s"(w2), "s"(b2), "s"(out), "s"(ws));
  STAMP(0);
  const rsrc_t rW = __builtin_amdgcn_make_buffer_rsrc((void*)ws, 0, WS_END, 0x00020000);
  const u32 l16 = lane * 16, l8 = lane * 8;
  u2 ep;
  u64 tag;
#define LD_EPOCH() asm volatile("buffer_load_dwordx2 %0, off, %1, 0 sc1" : "=v"(ep) : "s"(rW) : "memory")
#define GET_TAG() do { u32 lo_, hi_; asm volatile("v_readfirstlane_b32 %0, %2\n\tv_readfirstlane_b32 %1, %3\n\ts_nop 3" : "=s"(lo_), "=s"(hi_) : "v"(ep.x), "v"(ep.y) : "memory"); \
    tag = (((u64)hi_ << 32) | lo_) + 1; } while (0)
  u32 rot = xg + 3 * (p & 3), rotq = xg + (p & 3);
  rot = rot >= 10 ? rot - 10 : rot; rotq = rotq >= 10 ? rotq - 10 : (rotq >= 5 ? rotq - 5 : rotq);
  const u32 P = p >> 1, c = ((p & 1) << 3) | j, rho = c >> 1, odd = c & 1;
  const u32 t0 = rho < 6 ? 14 * rho + 8 * odd : 12 * rho + 12 + 6 * odd, tl = rho < 6 ? 14 * rho + 6 + odd : t0;
  const u32 pD = p, jD = j;
  const u32 s0 = w < 6 ? 7 * w : 6 * w + 6;
  const u32 vlast = w >= 6 ? OOB : l16 + 3072, vmid = w >= 6 ? l16 : l16 + 1024, vsp = w >= 6 ? l16 + 2048 : l16, soW2 = WS_W2T + (jD * 5 * 54 + s0) * 1024;
  u4 w2f[35];
  auto ldw2 = [&](auto I) {
    constexpr int s = I / 5, n = I % 5;
    if constexpr (s < 2) ldv<s * 1024>(w2f[I], rW, l16, soW2 + n * 54 * 1024);
    else if constexpr (s < 4) ldv<(s - 2) * 1024>(w2f[I], rW, vmid, soW2 + (n * 54 + 3) * 1024);
    else if constexpr (s == 4) ldv<2048>(w2f[I], rW, l16, soW2 + n * 54 * 1024);
    else ldv<0>(w2f[I], rW, s == 5 ? vsp : vlast, soW2 + (n * 54 + 3) * 1024);
  };
#define RT_CHECK() do { waitv<0>(); landed(fr[0]); landed(fr[1]); const u32 tl32 = (u32)tag, th32 = (u32)(tag >> 32); \
    const u32 bad = ((fr[0].x ^ tl32) | (fr[0].z ^ tl32) | (fr[1].x ^ tl32) | (fr[1].z ^ tl32)) | ((fr[0].y ^ th32) | (fr[0].w ^ th32) | (fr[1].y ^ th32) | (fr[1].w ^ th32)); \
    if (__any(bad != 0)) { _Pragma("nounroll") for (u32 q = 0; q < 4; q++) poll(ws, WS_FRT + (64 * q + lane) * 8, tag); } } while (0)
  constexpr u32 L_RS = L_RING + 4096;

  if (!helper) {
    const rsrc_t rX = __builtin_amdgcn_make_buffer_rsrc((void*)x, 0, 2048 * 1280, 0x00020000);
    float rsd[2];
    const u32 ONE2 = opaque(0x3F803F80u);
    u4 xr[2][10];
    float dm[2];
    {
      const u32 vx = (lane >> 3) * 1280 + (lane & 7) * 16, sx = (p * 64 + 16 * mt) * 1280;
      sfor<0, 10>([&](auto I) { ldp<I * 128>(xr[0][I], rX, vx, sx); });
      sfor<0, 10>([&](auto I) { ldp<I * 128>(xr[1][I], rX, vx, sx + 8 * 1280); });
      LD_EPOCH();
    }
    const u32 xa0 = opaque(L_X + (lr >> 3) * 1024 + (lr & 7) * 128 + ((g ^ (lr & 7)) << 4));
    const u32 tr0 = opaque(L_RING + (4 * g + (lr >> 2)) * 160 + (lr & 3) * 8);
    const u32 mh = mt & 1, nq = mt >> 1;
    float qrs;
    u4 xq[20];
    {
      f4 acc[2][5];
#pragma unroll
      for (int i = 0; i < 5; i++) { zero(acc[0][i]); zero(acc[1][i]); }
      const u32 bA = opaque(tr0 + nq * 96), bC = opaque(tr0 + 64 + nq * 5120);
      const u32 xs0 = opaque(xa0 + mh * 4096);
      u32 lkv[6];
#pragma unroll
      for (int q = 0; q < 2; q++) {
        const u32 dq = 4 * q + g - mt, e0 = lr - 4 * mt, e1 = e0 + 16, sb = L_RING + mt * 5120;
        lkv[3 * q] = opaque(sb + ((dq < 5 && e0 < 20) ? e0 * 40 + dq * 8 : 896));
        lkv[3 * q + 1] = opaque(sb + ((dq < 5 && e1 < 20) ? e1 * 40 + dq * 8 : 896));
        lkv[3 * q + 2] = opaque(sb + ((dq < 5 && lr == 0) ? 800 + dq * 8 : (dq == 0 && lr == 1) ? 840 : 896));
      }
      {
        const u32 wa = opaque(L_X + mt * 2048 + (lane >> 3) * 128 + (((lane & 7) ^ (lane >> 3)) << 4));
        const u32 lo = opaque(0x00003F80u), hi = opaque(0x3F800000u);
        sfor<0, 2>([&](auto G) {
          waitv<(G == 0 ? 11 : 1)>();
          STAMP(20 + G);
          float s0 = 0.f, s1 = 0.f;
#pragma unroll
          for (int i = 0; i < 10; i++) {
            landed(xr[G][i]);
            s0 = dot2c(xr[G][i].x, ONE2, s0); s1 = dot2c(xr[G][i].y, ONE2, s1); s0 = dot2c(xr[G][i].z, ONE2, s0); s1 = dot2c(xr[G][i].w, ONE2, s1);
          }
          const float m = xsum8(s0 + s1) * (1.f / 640.f), nm = -rbf(m);
          dm[G] = m + nm;
#pragma unroll
          for (int i = 0; i < 10; i++) { xr[G][i] = center8(xr[G][i], lo, hi, nm); sts128(wa + G * 1024 + i * 8192, xr[G][i]); }
        });
      }
      STAMP(22);
      auto half = [&](u4* cur, u4* nxt, u32 xa, u32 a, u32 c, auto KS, auto LD) {
        constexpr int o = KS * 10240;
        mm(acc[0][0], cur[5], cur[0]);
        if constexpr (LD) nxt[5] = lds128(xa);
        mm(acc[1][0], cur[6], cur[0]);
        if constexpr (LD) nxt[6] = lds128(xa + 2048);
        mm(acc[0][1], cur[5], cur[1]);
        if constexpr (LD) nxt[0] = ldstr(a + o);
        mm(acc[1][1], cur[6], cur[1]);
        if constexpr (LD) nxt[1] = ldstr(a + o + 32);
        mm(acc[0][2], cur[5], cur[2]);
        if constexpr (LD) nxt[2] = ldstr(c + o);
        mm(acc[1][2], cur[6], cur[2]);
        if constexpr (LD) nxt[3] = ldstr(a + o + 5120);
        mm(acc[0][3], cur[5], cur[3]);
        if constexpr (LD) nxt[4] = ldstr(a + o + 5152);
        mm(acc[1][3], cur[6], cur[3]);
        mm(acc[0][4], cur[5], cur[4]); mm(acc[1][4], cur[6], cur[4]);
      };
      u4 fa[7], fb[7];
      u32 st = rot, xa = xs0 + st * 8192, a = bA, cc = bC;
      BAR();
      STAMP(1);
      fa[5] = lds128(xa); fa[6] = lds128(xa + 2048); fa[0] = ldstr(a); fa[1] = ldstr(a + 32); fa[2] = ldstr(cc);
      fa[3] = ldstr(a + 5120); fa[4] = ldstr(a + 5152);
      sfor<0, 2>([&](auto G) {
        float s0 = 0.f, s1 = 0.f;
#pragma unroll
        for (int i = 0; i < 10; i++) {
          landed(xr[G][i]);
          const u4 v = xr[G][i];
          s0 = dot2c(v.x, v.x, s0); s1 = dot2c(v.y, v.y, s1); s0 = dot2c(v.z, v.z, s0); s1 = dot2c(v.w, v.w, s1);
        }
        const float var = fmaxf(xsum8(s0 + s1) * (1.f / 640.f) - dm[G] * dm[G], 0.f);
        rsd[G] = __builtin_amdgcn_rcpf(rbf(__builtin_amdgcn_sqrtf(rbf(rbf(var) + 1e-5f))));
        landed(rsd[G]);
      });
      waitv<0>();
      GET_TAG();
#pragma nounroll
      for (u32 i = 1; i < 10; i++) {
        waitl<0>();
        half(fa, fb, xa ^ 64, a, cc, IC<1>{}, IC<1>{});
        st = st + 1 >= 10 ? 0 : st + 1;
        xa = opaque(xs0 + st * 8192); a = opaque(bA + (i & 3) * 20480); cc = opaque(bC + (i & 3) * 20480);
        BAR();
        half(fb, fa, xa, a, cc, IC<0>{}, IC<1>{});
      }
      if ((lane & 7) == 0) { sts32(L_RS + mt * 64 + (lane >> 3) * 4, rsd[0]); sts32(L_RS + mt * 64 + 32 + (lane >> 3) * 4, rsd[1]); }
      waitl<0>();
      half(fa, fb, xa ^ 64, a, cc, IC<1>{}, IC<1>{});
      {
        u32 qs = rotq;
#pragma unroll
        for (int q = 0; q < 5; q++) {
          const u32 xb = xa0 + mt * 2048 + qs * 16384, xc = xb ^ 64;
          xq[4 * q] = lds128(xb); xq[4 * q + 1] = lds128(xc); xq[4 * q + 2] = lds128(xb + 8192); xq[4 * q + 3] = lds128(xc + 8192);
          qs = qs + 1 >= 5 ? 0 : qs + 1;
        }
      }
      BAR();
      const f4 r0 = __builtin_bit_cast(f4, lds128(L_RS + mh * 128 + g * 16)), r1 = __builtin_bit_cast(f4, lds128(L_RS + mh * 128 + 64 + g * 16));
      qrs = *(LAS float*)(L_RS + mt * 64 + lr * 4);
      half(fb, fa, 0, 0, 0, IC<0>{}, IC<0>{});
      MM_DONE();
      __builtin_amdgcn_s_setprio(PR);
      STAMP(2);
      const u32 EX = L_RING + 20480;
      const u32 ew = EX + mh * 10240 + nq * 3072 + l16;
      auto put = [&](auto N, auto PHI, u32 a) {
        constexpr int n = N;
        pin(acc[0][n]); pin(acc[1][n]);
        sts128(a, u4{phi2<PHI>(acc[0][n][0], acc[0][n][1], r0[0], r0[1]), phi2<PHI>(acc[0][n][2], acc[0][n][3], r0[2], r0[3]),
                     phi2<PHI>(acc[1][n][0], acc[1][n][1], r1[0], r1[1]), phi2<PHI>(acc[1][n][2], acc[1][n][3], r1[2], r1[3])});
      };
      put(IC<0>{}, IC<1>{}, ew); put(IC<1>{}, IC<1>{}, ew + 1024); put(IC<3>{}, IC<0>{}, ew + 5120); put(IC<4>{}, IC<0>{}, ew + 6144);
      if (nq == 0) put(IC<2>{}, IC<1>{}, ew + 2048); else put(IC<2>{}, IC<0>{}, ew + 4096);
      STAMP(41);
      BAR();
      STAMP(42);
      const u32 ex = EX + mt * 1024 + l16;
      u4 kf[2][2], vf[2][2];
#pragma unroll
      for (int m = 0; m < 2; m++) {
        kf[m][0] = lds128(ex + m * 10240); vf[m][0] = lds128(ex + m * 10240 + 5120);
        vf[m][1] = lds128(ex + m * 10240 + 6144); kf[m][1] = lds128(ex + m * 10240 + 1024);
      }
      const u32 o0 = lr == 0 ? ONE2 : 0u;
      const u4 ONE0 = {o0, o0, o0, o0};
#pragma unroll
      for (int q = 0; q < 2; q++) {
        f4 z4 = {0, 0, 0, 0};
        f4 kv0 = mfma(kf[1][q], vf[1][0], mfma(kf[0][q], vf[0][0], z4)), kv1 = mfma(kf[1][q], vf[1][1], mfma(kf[0][q], vf[0][1], z4));
        f4 zz = mfma(kf[1][q], ONE0, mfma(kf[0][q], ONE0, z4));
        sts64(lkv[3 * q], u2{pk2(kv0[0], kv0[1]), pk2(kv0[2], kv0[3])});
        sts64(lkv[3 * q + 1], u2{pk2(kv1[0], kv1[1]), pk2(kv1[2], kv1[3])});
        sts64(lkv[3 * q + 2], u2{pk2(zz[0], zz[1]), pk2(zz[2], zz[3])});
      }
      asm volatile("" ::: "memory");
      st128(lds128(L_RING + mt * 5120 + l16), rW, lane < 56 ? l16 : OOB, WS_KVP + ((j * 32 + p) * 4 + mt) * 896);
    }
    u32 pq[5][2];
    u2 xres[5];
    {
      const u32 qa0 = opaque(tr0 + 2 * 20480), qa1 = opaque(tr0 - L_RING);
      u4 wf[2][5];
#pragma unroll
      for (int n = 0; n < 5; n++) wf[0][n] = ldstr(qa0 + n * 32);
      waitv<0>();
      setflag(ws, WS_FKV + ((j * 4 + mt) * 32 + p) * 8, tag, lane);
      STAMP(3);
      sfor<0, 5>([&](auto N) { ldp2<N * 32>(xres[N], rX, lr * 1280 + g * 8, (p * 64 + mt * 16) * 1280 + j * 160); });
      f4 qa[5];
      sfor<0, 20>([&](auto S) {
        constexpr int s = S, t = s + 1;
        if constexpr (s == 7) BAR();
        waitl<6>();
        sfor<0, 5>([&](auto N) {
          constexpr int n = N;
          if constexpr (n == 2) waitl<(s < 19 ? 4 : 0)>();
          if constexpr (s == 0) mm0(qa[n], wf[0][n], xq[0]); else mm(qa[n], wf[s & 1][n], xq[s]);
          if constexpr (s < 19) wf[t & 1][n] = ldstr((t < 8 ? qa0 + t * 5120 : qa1 + (t - 8) * 5120) + n * 32);
        });
      });
      MM_DONE();
      STAMP(4);
#pragma unroll
      for (int n = 0; n < 5; n++) {
        pin(qa[n]);
        pq[n][0] = opaque(phi2<1>(qa[n][0], qa[n][1], qrs, qrs)); pq[n][1] = opaque(phi2<1>(qa[n][2], qa[n][3], qrs, qrs));
      }
    }
    STAMP(5);
    __builtin_amdgcn_s_setprio(2);
    STAMP(23);
    BAR();
    STAMP(6);
    {
      float o[20];
#pragma unroll
      for (int h = 0; h < 4; h++) {
        u4 bq = {pq[h][0], pq[h][1], pq[h + 1][0], pq[h + 1][1]};
        f4 z4 = {0, 0, 0, 0};
        f4 d0 = mfma(lds128(L_IMG + h * 2048 + l16), bq, z4);
        f4 d1 = mfma(lds128(L_IMG + h * 2048 + 1024 + l16), bq, z4);
        float rd = __builtin_amdgcn_rcpf(d1[1] + 1e-6f);
#pragma unroll
        for (int i = 0; i < 4; i++) o[5 * h + i] = d0[i] * rd;
        o[5 * h + 4] = d1[0] * rd;
      }
      u32 ov[10];
#pragma unroll
      for (int i = 0; i < 10; i++) ov[i] = pk2(o[2 * i], o[2 * i + 1]);
      publish20(ov, rW, l16, WS_OB + ((p * 4 + mt) * 20 + 5 * (j >> 1)) * 1024, j);
    }
    waitv<0>();
    setflag(ws, WS_FO + ((p * 4 + mt) * 8 + j) * 8, tag, lane);
    BAR();
    STAMP(7);
    {
      u4 of[20], wf[2][5];
      u2 oh[4][2];
      const u32 so = WS_OB + (p * 4 + mt) * 20480;
      auto ldo = [&](auto S) {
        constexpr int s = S;
        if constexpr (s % 5 == 2) { ldv2<(s & 3) * 1024>(oh[s / 5][0], rW, l8, so + (s & ~3) * 1024); ldv2<(s & 3) * 1024 + 512>(oh[s / 5][1], rW, l8, so + (s & ~3) * 1024); }
        else ldv<(s & 3) * 1024>(of[s], rW, l16, so + (s & ~3) * 1024);
      };
      const u32 a0 = opaque(L_WO + l16), a1 = opaque(L_WO + 51200 + l16);
#pragma unroll
      for (int q = 0; q < 2; q++) {
#pragma unroll
        for (int n = 0; n < 5; n++) wf[q][n] = lds128(a0 + n * 10240 + q * 1024);
        asm volatile("" ::: "memory");
      }
      PP pp;
      poll2(pp, rW, WS_FO + ((p * 4 + mt) * 8 + (lane & 7)) * 8, tag);
      STAMP(8);
      sfor<0, OP>(ldo);
      f4 ha[1][5];
      sfor<0, 20>([&](auto S) {
        constexpr int s = S, e = OP + 2 * s < 20 ? OP + 2 * s : 20, f = OP + 2 + 2 * s < 20 ? OP + 2 + 2 * s : 20;
        constexpr int yo = (e - s - 1) + ((e + 2) / 5 - (s + 3) / 5);
        if constexpr (s == 8) { STAMP(38); lwait(2); STAMP(39); }
        if constexpr (s == 10) lset(1, mt);
        waitvl<yo, (s < 19 && s != 8 && s != 10 ? 5 : 0)>();
        if constexpr (s == 0) { pollend(pp); STAMP(37); }
        sfor<0, 5>([&](auto N) {
          constexpr int n = N, t = s + 2;
          if constexpr (s == 0) mm0(ha[0][n], wf[0][n], of[s]);
          else if constexpr (s % 5 == 2) mmh(ha[0][n], wf[s & 1][n], oh[s / 5][0], oh[s / 5][1]);
          else mm(ha[0][n], wf[s & 1][n], of[s]);
          if constexpr (t < 20) wf[s & 1][n] = lds128((t < 10 ? a0 : a1) + n * 10240 + (t % 10) * 1024);
          if constexpr (n == 1 && e < f) ldo(IC<e>{});
          if constexpr (n == 3 && e + 1 < f) ldo(IC<e + 1>{});
        });
      });
      MM_DONE();
#pragma unroll
      for (int n = 0; n < 5; n++) pin(ha[0][n]);
      STAMP(9);
      u32 hres[10];
      float sum = 0.f, sq = 0.f;
#pragma unroll
      for (int n = 0; n < 5; n++) {
        hres[2 * n] = pk2(unp(xres[n].x) + f2{ha[0][n][0], ha[0][n][1]});
        hres[2 * n + 1] = pk2(unp(xres[n].y) + f2{ha[0][n][2], ha[0][n][3]});
      }
      publish20(hres, rW, l16, WS_HB + ((p * 4 + mt) * 20 + 5 * (j >> 1)) * 1024, j);
#pragma unroll
      for (int i = 0; i < 10; i++) { sum = dot2c(hres[i], ONE2, sum); sq = dot2c(hres[i], hres[i], sq); }
      st64(u2{asu(xsum16_32(sum)), asu(xsum16_32(sq))}, rW, g == 0 ? l8 : OOB, WS_ST + ((p * 4 + mt) * 8 + j) * 128);
#pragma unroll
      for (int n = 0; n < 5; n++) sts64(L_HR + (mt * 5 + n) * 512 + l8, u2{hres[2 * n], hres[2 * n + 1]});
      waitv<0>();
      setflag(ws, WS_FH + ((p * 4 + mt) * 8 + j) * 8, tag, lane);
      lset(3, mt);
      STAMP(10);
      lwait(4);
    }
    {
      constexpr int HD = HDV;
      const u32 pc = 2 * P + (mt >> 1), mc = 2 * (mt & 1);
      f4 ca[2][7];
      u4 hf[2][20], wf[7];
      u2 hh[2][4][2];
      const u32 so = WS_HB + (pc * 4 + mc) * 20480;
      auto ldh = [&](auto S) {
        constexpr int s = S;
        sfor<0, 2>([&](auto M) {
          if constexpr (s % 5 == 2) {
            ldv2<(s & 3) * 1024>(hh[M][s / 5][0], rW, l8, so + M * 20480 + (s & ~3) * 1024);
            ldv2<(s & 3) * 1024 + 512>(hh[M][s / 5][1], rW, l8, so + M * 20480 + (s & ~3) * 1024);
          } else ldv<(s & 3) * 1024>(hf[M][s], rW, l16, so + M * 20480 + (s & ~3) * 1024);
        });
      };
      const u32 a0 = opaque(L_W1A + l16), a1 = opaque(L_W1B + l16), a2 = opaque(L_W1B + 35840 + l16), a3 = opaque(L_W1B + 71680 + l16);
#pragma unroll
      for (int n = 0; n < 7; n++) wf[n] = lds128(a0 + n * 5120);
      PP pp;
      poll2(pp, rW, WS_FH + ((pc * 4 + mc) * 8 + lr) * 8, tag);
      STAMP(11);
      sfor<0, 3>(ldh);
      sfor<0, 20>([&](auto S) {
        constexpr int s = S, e1 = 3 + 2 * s < s + HD ? 3 + 2 * s : s + HD, e = e1 < 20 ? e1 : 20;
        constexpr int f1 = 5 + 2 * s < s + 1 + HD ? 5 + 2 * s : s + 1 + HD, f = f1 < 20 ? f1 : 20;
        constexpr int yo = 2 * ((e - s - 1) + ((e + 2) / 5 - (s + 3) / 5));
        if constexpr (s == 9) { STAMP(33); lwait(5); STAMP(34); }
        waitvl<yo, 3>();
        if constexpr (s == 0) { pollend(pp); STAMP(31); }
        if constexpr (s == 5) STAMP(32);
        if constexpr (s == 14) STAMP(35);
        sfor<0, 7>([&](auto N) {
          constexpr int n = N, t = s + 1;
          if constexpr (n == 4) waitl<(s < 19 ? 4 : 0)>();
          sfor<0, 2>([&](auto M) {
            if constexpr (s == 0) mm0(ca[M][n], wf[n], hf[M][s]);
            else if constexpr (s % 5 == 2) mmh(ca[M][n], wf[n], hh[M][s / 5][0], hh[M][s / 5][1]);
            else mm(ca[M][n], wf[n], hf[M][s]);
          });
          if constexpr (s < 19) wf[n] = lds128((t < 5 ? a0 : t < 10 ? a1 : t < 15 ? a2 : a3) + n * 5120 + (t % 5) * 1024);
          if constexpr (n == 1 && e < f) ldh(IC<e>{});
          if constexpr (n == 4 && e + 1 < f) ldh(IC<e + 1>{});
        });
      });
      MM_DONE();
      STAMP(36);
      lset(7, mt);
      lwait(6);
      STAMP(12);
      const u32 sh = WS_HID + (pc * 4 + mc) * 54 * 1024, fh = WS_FHID + (((pc * 16 + c) * 2 + (mt & 1)) * 2) * 8;
      const u2 s0v = lds64(L_CB + 2048 + mt * 256 + lr * 8), s1v = lds64(L_CB + 2048 + mt * 256 + 128 + lr * 8);
      const float sc[2] = {asf(s0v.x), asf(s1v.x)}, sm[2] = {asf(s0v.y), asf(s1v.y)};
      u32 hp[2][2][2];
      sfor<0, 7>([&](auto N) {
        constexpr int n = N;
        const f4 c4 = __builtin_bit_cast(f4, lds128(L_CB + n * 64 + g * 16)), b4 = __builtin_bit_cast(f4, lds128(L_CB + 512 + n * 64 + g * 16));
        if constexpr (n == 6) { waitv<2>(); setflag(ws, fh, tag, lane); STAMP(15); }
        sfor<0, 2>([&](auto M) {
          pin(ca[M][n]);
#pragma unroll
          for (int i = 0; i < 2; i++) {
            const f2 v = f2{ca[M][n][2 * i], ca[M][n][2 * i + 1]} * sc[M] + (f2{b4[2 * i], b4[2 * i + 1]} - sm[M] * f2{c4[2 * i], c4[2 * i + 1]});
            const f2 u = v * (-2.302208198f - 0.1029432397f * v * v);
            const f2 d = f2{__builtin_amdgcn_exp2f(u[0]), __builtin_amdgcn_exp2f(u[1])} + 1.f;
            hp[M][n & 1][i] = pk2(v * f2{__builtin_amdgcn_rcpf(d[0]), __builtin_amdgcn_rcpf(d[1])});
          }
          if constexpr (n & 1) st128(u4{hp[M][0][0], hp[M][0][1], hp[M][1][0], hp[M][1][1]}, rW, l16, sh + M * 54 * 1024 + ((t0 >> 1) + n / 2) * 1024);
          if constexpr (n == 6) st64(u2{hp[M][0][0], hp[M][0][1]}, rW, rho < 6 ? l8 + odd * 512 : OOB, sh + M * 54 * 1024 + (tl >> 1) * 1024);
        });
      });
      STAMP(29);
      waitv<0>();
      setflag(ws, fh + 8, tag, lane);
      __builtin_amdgcn_s_setprio(0);
      STAMP(13);
      lwait(8);
      const u32 wa = opaque(mt * 35840 + l16);
#pragma unroll
      for (int i = 0; i < 35; i++) w2f[i] = lds128(wa + i * 1024);
      lset(9, mt);
    }
  } else {
    STAMP(20);
    LD_EPOCH();
    u4 sel[2];
    u32 dst[2];
    auto setup = [&]() {
#pragma unroll
    for (int q = 0; q < 2; q++) {
      const u32 k = lr + 16 * q - 8 * g, one = (k & 1) ? 0x3F800000u : 0x00003F80u;
      sel[q] = u4{k < 2 ? one : 0u, (k >> 1) == 1 ? one : 0u, (k >> 1) == 2 ? one : 0u, (k >> 1) == 3 ? one : 0u};
      landed(sel[q]);
    }
#pragma unroll
    for (int h = 0; h < 2; h++) {
      const u32 qd = 2 * (lr + 16 * (2 * h + (g >> 1))) + (g & 1), e = (qd * 205) >> 10, dq = qd - e * 5;
      const u32 gs = dq + mt, grp = (e * 13) >> 6, ii = e - grp * 5;
      const u32 base = L_IMG + mt * 2048 + (gs & 3) * 256 + (gs >> 2) * 8;
      dst[h] = opaque(e < 20 ? base + (ii < 4 ? (4 * grp + ii) * 16 : 1024 + 4 * grp * 16) : (e == 20 ? base + 1024 + 16 : L_CB + 1792));
    }
    };
    if (mt < 3) {
      const rsrc_t rQ = __builtin_amdgcn_make_buffer_rsrc((void*)((const char*)wqkv - 4096), 0, 640 * 3840 + 4096, 0x00020000);
      u32 voffW[7];
#pragma unroll
      for (int i = 0; i < 7; i++) {
        const u32 pi = i < 5 ? i : (i == 5 ? mt : (mt < 2 ? mt + 3 : 4u));
        const u32 f = 64 * pi + lane, sg = (f * 6554) >> 16, ch = f - sg * 10;
        voffW[i] = (((sg & 12) << 1) + ((sg >> 4) << 2) + (sg & 3)) * 3840 + ch * 16 + 4096 - (i < 4 ? i * 1024 : 0);
      }
      const u32 soW = (mt >> 1) * 32 * 3840 + ((mt & 1) + 1) * 1280 + j * 160, soW3 = 32 * 3840 + 2560 + j * 160;
      const u32 soQ = mt * 32 * 3840 + j * 160, soQ3 = 96 * 3840 + j * 160;
      const u32 l5 = 15360 + mt * 1024, l6 = 15360 + (mt < 2 ? mt + 3 : 4u) * 1024;
      u32 st = rot, qs = rotq;
      auto issueW = [&](u32 so, u32 so3, u32 ld) {
        dma<0>(rQ, voffW[0], so, ld + mt * 5120); dma_c<1024>(rQ, voffW[1], so); dma_c<2048>(rQ, voffW[2], so); dma_c<3072>(rQ, voffW[3], so);
        dma<0>(rQ, voffW[4], so, ld + mt * 5120 + 4096); dma<0>(rQ, voffW[5], so3, ld + l5);
        if (mt < 2) dma<0>(rQ, voffW[6], so3, ld + l6);
      };
      auto issueKV = [&](u32 slot) { issueW(soW + st * 245760, soW3 + st * 245760, L_RING + slot * 20480); st = st + 1 >= 10 ? 0 : st + 1; };
      auto issueQ = [&](u32 ld) { issueW(soQ + qs * 491520, soQ3 + qs * 491520, ld); qs = qs + 1 >= 5 ? 0 : qs + 1; };
      issueKV(0); issueKV(1); issueKV(2);
      STAMP(21);
      waitv<12>();
      BAR();
      issueKV(3);
#pragma nounroll
      for (u32 i = 0; i < 6; i++) { waitv<12>(); asm volatile("s_barrier" ::: "memory"); issueKV(i & 3); }
      auto issueQa = [&](u32 slot) {
        const u32 so = soQ + qs * 491520;
        dma<0>(rQ, voffW[0], so, L_RING + slot * 20480 + mt * 5120); dma_c<1024>(rQ, voffW[1], so); dma_c<2048>(rQ, voffW[2], so);
      };
      auto issueQb = [&](u32 slot) {
        const u32 so = soQ + qs * 491520, so3 = soQ3 + qs * 491520, ld = L_RING + slot * 20480;
        dma<3072>(rQ, voffW[3], so, ld + mt * 5120); dma<0>(rQ, voffW[4], so, ld + mt * 5120 + 4096); dma<0>(rQ, voffW[5], so3, ld + l5);
        if (mt < 2) dma<0>(rQ, voffW[6], so3, ld + l6);
        qs = qs + 1 >= 5 ? 0 : qs + 1;
      };
      waitv<12>(); BAR(); issueQa(2);
      waitv<9>(); BAR(); issueQb(2);
      waitv<6>(); BAR(); issueQa(3);
      BAR();
      issueQb(3);
      issueQ(0);
      waitv<6>();
      BAR();
      GET_TAG();
      setup();
      issueQ(20480); issueQ(40960);
    } else {
      const bool m1 = b < 108, mo = b >= 216;
      const u32 v = m1 ? b : (mo ? b - 216 : b - 108), vq = m1 ? (v * 2428) >> 16 : (v * 6554) >> 16;
      const u32 sA = uni((m1 || mo) ? 5 * vq : v >> 1), cA = uni(m1 ? v - 27 * vq : (mo ? v - 10 * vq : 5 * (v & 1))), sQ = uni((m1 || mo) ? 1u : 0u);
      const u32 pitch = uni(m1 ? 3456 : 1280);
      const u64 src = m1 ? (u64)w1 : (mo ? (u64)wo : (u64)w2);
      const rsrc_t rS = __builtin_amdgcn_make_buffer_rsrc((void*)src, 0, mo ? 640 * 1280 : 640 * 3456, 0x00020000);
      u32 vrt[20];
#pragma unroll
      for (int I = 0; I < 20; I++) {
        const u32 q = I >> 2, i = I & 3, ro = 8 * i + (lane >> 3);
        const u32 i40 = 8 * q + 4 * (i >> 1) + (ro & 3), hf = i40 >= 20 ? 1u : 0u, id = i40 - 20 * hf;
        const u32 rp = opaque(80 * hf + 15 * ((id * 13) >> 6) + 5 * ((ro >> 2) & 3) + id);
        vrt[I] = opaque((mo ? rp : 32 * sQ * q + ro) * pitch + (lane & 7) * 16 + (q - sQ * q) * 128);
      }
      u4 ONES = {0x3F803F80u, 0x3F803F80u, 0x3F803F80u, 0x3F803F80u};
      landed(ONES);
      const u32 sc = L_RING + 16384;
      const u32 wa = sc + (lane >> 3) * 128 + (((lane & 7) ^ (((lane >> 4) & 3) << 1)) << 4);
      const u32 r0 = 4 * g + (lr >> 2), ta = opaque(sc + r0 * 128 + (lr & 1) * 8 + ((((lr >> 1) & 1) ^ (((r0 >> 1) & 3) << 1)) << 4));
      const u32 dT = uni(m1 ? 20480u : (mo ? 10240u : 55296u)), dQ = uni(sQ ? 1024u : 4 * 55296u);
      const u32 dA = uni((m1 ? WS_W1T : (mo ? WS_WOT + (sA >= 10 ? 40960u : 0u) : WS_W2T)) + 4 * cA * dT + sA * 1024);
      u32 dJ[4];
#pragma unroll
      for (int n = 0; n < 4; n++) dJ[n] = uni(dA + n * dT + (mo ? (((4 * cA + n) * 52) >> 8) * 51200u : 0u));
      waitv<0>();
      GET_TAG();
      setup();
      STAMP(21);
#pragma nounroll
      for (u32 i = 0; i < 8; i++) BAR();
      u4 f[5][4];
      sfor<0, 7>([&](auto I) { ldp<0>(f[I >> 2][I & 3], rS, vrt[I], 32 * sA * pitch + cA * 128); });
      BAR();
      sfor<7, 14>([&](auto I) { ldp<0>(f[I >> 2][I & 3], rS, vrt[I], 32 * sA * pitch + cA * 128); });
      BAR();
      sfor<14, 20>([&](auto I) { ldp<0>(f[I >> 2][I & 3], rS, vrt[I], 32 * sA * pitch + cA * 128); });
      BAR();
      f4 cs[4];
      sfor<0, 5>([&](auto Q) {
        constexpr int q = Q;
        waitv<16 - 4 * q>();
#pragma unroll
        for (int i = 0; i < 4; i++) { landed(f[q][i]); sts128(wa + i * 1024, f[q][i]); }
        asm volatile("" ::: "memory");
#pragma unroll
        for (int n = 0; n < 4; n++) f[q][n] = ldstr(ta ^ (n << 5), (ta ^ (n << 5)) + 2048);
        asm volatile("" ::: "memory");
#pragma unroll
        for (int n = 0; n < 4; n++) cs[n] = mfma(f[q][n], ONES, q == 0 ? f4{0, 0, 0, 0} : cs[n]);
      });
      STAMP(22);
#pragma unroll
      for (int q = 0; q < 5; q++) {
        if (q == RTE) { BAR(); if (E2) BAR(); }
#pragma unroll
        for (int n = 0; n < 4; n++) st128(f[q][n], rW, l16, dJ[n] + q * dQ);
      }
#pragma unroll
      for (int n = 0; n < 4; n++) st128(__builtin_bit_cast(u4, cs[n]), rW, (m1 && lr == 0) ? g * 16 : OOB, WS_CS1 + vq * 6912 + (4 * cA + n) * 64);
      if (RTE == 5) { BAR(); if (E2) BAR(); }
    }
    STAMP(24);
    if (mt < 3) waitv<0>();
    BAR();
    if (mt == 3) { waitv<0>(); setflag(ws, WS_FRT + b * 8, tag, lane); }
    STAMP(4);
    PP pp;
    poll2(pp, rW, WS_FKV + ((j * 4 + mt) * 32 + (lane & 31)) * 8, tag);
    STAMP(5);
    {
      const u32 so = WS_KVP + (j * 32 * 4 + mt) * 896;
      const u32 v0 = lane < 53 ? l16 : OOB;
      u4 a[32], fr[2];
      auto lda = [&](auto Q) { ldv<(Q & 1) * 3584>(a[Q], rW, v0, so + (Q & ~1) * 3584); };
      sfor<0, 14>(lda);
      sts128(L_IMG + mt * 2048 + l16, u4{0, 0, 0, 0}); sts128(L_IMG + mt * 2048 + 1024 + l16, u4{0, 0, 0, 0});
      const bool zrow = (dst[1] & 0x430) == 0x410 && dst[1] < L_CB;
      f4 s0, s1, s2, s3;
      sfor<0, 16>([&](auto Q) {
        waitv<(Q < 9 ? 12 : 32 - 2 * Q)>();
        if constexpr (Q == 0) pollend(pp);
        if constexpr (Q == 0) { mm0(s0, sel[0], a[0]); mm0(s1, sel[1], a[0]); mm0(s2, sel[0], a[1]); mm0(s3, sel[1], a[1]); }
        else { mm(s0, sel[0], a[2 * Q]); mm(s1, sel[1], a[2 * Q]); mm(s2, sel[0], a[2 * Q + 1]); mm(s3, sel[1], a[2 * Q + 1]); }
        if constexpr (Q < 9) { lda(IC<14 + 2 * Q>{}); lda(IC<15 + 2 * Q>{}); }
        if constexpr (Q == 8) { ldv<0>(fr[0], rW, lane * 32, WS_FRT); ldv<16>(fr[1], rW, lane * 32, WS_FRT); }
      });
      MM_DONE();
      pin(s0); pin(s1); pin(s2); pin(s3);
      s0 += s2; s1 += s3;
      const u2 v1 = {pk2(s1[0], s1[1]), pk2(s1[2], s1[3])};
      sts64(dst[0], u2{pk2(s0[0], s0[1]), pk2(s0[2], s0[3])});
      sts64(dst[1], v1);
      if (zrow) { sts64(dst[1] + 64, v1); sts64(dst[1] + 128, v1); sts64(dst[1] + 192, v1); }
      STAMP(6);
      BAR();
      RT_CHECK();
    }
    auto issueW1 = [&](u32 it, u32 n, u32 ld) {
      const u32 so = WS_W1T + (n < 6 ? t0 + n : tl) * 20480 + it * 5120;
      dma4_sc1(rW, l16, so, ld + n * 5120); dma1_sc1(rW, l16, so + 4096, ld + n * 5120 + 4096);
    };
    const u32 o = 51200 + mt * 12288, o2 = 51200 + 49152 + (mt & 1) * 1024, sw = WS_WOT + j * 102400;
    {
      const u32 p0 = mt * 12288, p2 = 49152 + (mt & 1) * 1024;
      dma4_sc1(rW, l16, sw + p0, L_WO + p0); dma4_sc1(rW, l16, sw + p0 + 4096, L_WO + p0 + 4096); dma4_sc1(rW, l16, sw + p0 + 8192, L_WO + p0 + 8192);
      dma1_sc1(rW, l16, sw + p2, L_WO + p2);
    }
    if (b == 0 && tid == 256) __hip_atomic_store((GAS u64*)ws, tag, __ATOMIC_RELAXED, __HIP_MEMORY_SCOPE_AGENT);
    {
      f4 csum;
      u32 ci = lane + 64 * (mt & 1); ci = ci > 111 ? 111 : ci;
      const u32 n = ci >> 4, col = (n < 6 ? t0 + n : tl) * 16 + (ci & 15);
#pragma unroll
      for (int q = 0; q < 4; q++) csum[q] = asf(__builtin_amdgcn_raw_buffer_load_b32(rW, col * 4, WS_CS1 + q * 6912, SC1));
      const u32 bv = b1[col], b2v = ((const u32*)b2)[40 * jD + (lane < 40 ? lane : 0)];
      sts32(L_CB + ci * 4, (csum[0] + csum[1]) + (csum[2] + csum[3]));
      sts32(L_CB + 512 + ci * 4, blo(bv));
      if (lane < 40) *(LAS u32*)(L_CB + 1024 + lane * 4) = b2v;
    }
    waitv<0>();
    BAR();
    STAMP(9);
    auto w1t = [&](u32 it, u32 n, u32 ld) { waitv<TH>(); issueW1(it, n, ld); };
    auto w2t = [&](auto I) { waitv<TH + 3>(); ldw2(I); };
    dma4_sc1(rW, l16, sw + o, L_WO + o); waitv<TH>(); dma4_sc1(rW, l16, sw + o + 4096, L_WO + o + 4096);
    waitv<TH>(); dma4_sc1(rW, l16, sw + o + 8192, L_WO + o + 8192); dma1_sc1(rW, l16, sw + o2, L_WO + o2);
    w1t(0, mt, L_W1A);
    waitv<5>(); lset(2, mt);
    STAMP(10);
    w1t(0, mt < 3 ? mt + 4 : 6, L_W1A);
    sfor<0, HA>(w2t);
    lwait(1);
    STAMP(7);
    w1t(1, mt, L_W1B); w1t(1, mt < 3 ? mt + 4 : 6, L_W1B);
    sfor<HA, HA + 4>(w2t);
    waitv<4>(); lset(4, mt);
    STAMP(25);
    sfor<HA + 4, HB2>(w2t);
    lwait(3);
    STAMP(8);
#pragma nounroll
    for (u32 u = mt; u < 14; u += 4) { const u32 it = u >= 7 ? 1u : 0u; w1t(it + 2, u - 7 * it, L_W1B + 35840 + it * 35840); }
    waitv<0>(); lset(5, mt);
    STAMP(28);
    {
      const u32 pc = 2 * P + (mt >> 1), mc = 2 * (mt & 1);
      poll(ws, WS_FH + ((pc * 4 + mc) * 8 + lr) * 8, tag);
#pragma unroll
      for (int m = 0; m < 2; m++) {
        const u2 v0 = __builtin_amdgcn_raw_buffer_load_b64(rW, g * 256 + lr * 8, WS_ST + (pc * 4 + mc + m) * 1024, SC1);
        const u2 v1 = __builtin_amdgcn_raw_buffer_load_b64(rW, g * 256 + lr * 8 + 128, WS_ST + (pc * 4 + mc + m) * 1024, SC1);
        const float mean = xsum16_32(asf(v0.x) + asf(v1.x)) * (1.f / 640.f);
        const float var = fmaxf(xsum16_32(asf(v0.y) + asf(v1.y)) * (1.f / 640.f) - mean * mean, 0.f);
        const float m1 = rbf(mean), sd1 = rbf(__builtin_amdgcn_sqrtf(rbf(rbf(var) + 1e-5f))), r1 = __builtin_amdgcn_rcpf(sd1);
        const float m2 = rbf((mean - m1) * r1), sd2 = rbf(__builtin_amdgcn_sqrtf(rbf(rbf(var * r1 * r1) + 1e-5f)));
        const float sc = r1 * __builtin_amdgcn_rcpf(sd2);
        sts64(L_CB + 2048 + mt * 256 + m * 128 + lr * 8, u2{asu(sc), asu(sc * (m1 + m2 * sd1))});
      }
    }
    lset(6, mt);
    STAMP(27);
    sfor<HB2, 35>(w2t);
    STAMP(26);
    lwait(7);
    {
      const u32 so = WS_W2T + (jD * 5 * 54 + 7 * mt) * 1024, ld = mt * 35840;
#pragma nounroll
      for (u32 n = 0; n < 5; n++) {
        waitv<TH2>();
        sfor<0, 7>([&](auto S) {
          constexpr int k = S < 2 ? S : (S < 4 ? S + 2 : (S < 6 ? S - 2 : 6));
          dma1_sc1(rW, l16, so + (n * 54 + k) * 1024, ld + (S * 5 + n) * 1024);
        });
      }
    }
    waitv<0>();
    lset(8, mt);
    STAMP(30);
  }
  {
    f4 da[4][5];
    u4 xf[7][4];
    u2 xh[4][2], hr[3];
    const u32 so = WS_HID + (pD * 4 * 54 + s0) * 1024;
    const u32 vh0 = w < 6 ? l8 : l16 + 2048, vh1 = w < 6 ? l8 + 512 : l16 + 2056;
    auto ldx = [&](auto U) {
      constexpr int s = U / 4, a = U % 4;
      if constexpr (s < 2) ldv<s * 1024>(xf[s][a], rW, l16, so + a * 54 * 1024);
      else if constexpr (s < 4) ldv<(s - 2) * 1024>(xf[s][a], rW, vmid, so + (a * 54 + 3) * 1024);
      else if constexpr (s == 4) ldv<2048>(xf[s][a], rW, l16, so + a * 54 * 1024);
      else if constexpr (s == 5) { ldv2<0>(xh[a][0], rW, vh0, so + (a * 54 + 3) * 1024); ldv2<0>(xh[a][1], rW, vh1, so + (a * 54 + 3) * 1024); }
      else ldv<0>(xf[s][a], rW, vlast, so + (a * 54 + 3) * 1024);
    };
    const u32 fo = WS_FHID + ((pD * 16 + 2 * w) * 2 + (lane & 3)) * 16;
    const u32 rw = opaque(L_RED + (w >> 1) * 1024 + l16 + (w & 1) * 8);
    auto put = [&](auto A, auto N) {
      pin(da[A][N]);
      sts64(rw + (A * 5 + N) * 4096, u2{pk2(da[A][N][0], da[A][N][1]), pk2(da[A][N][2], da[A][N][3])});
    };
    {
      constexpr int XD = 6;
      PP pp;
      poll2(pp, rW, fo, tag);
      STAMP(14);
      sfor<0, XD>(ldx);
      sfor<0, 16>([&](auto U) {
        constexpr int u = U, s = u / 4, a = u % 4;
        constexpr int e0 = u + XD + (u >= 4 ? 4 : 0), e = e0 < 16 ? e0 : 16;
        constexpr int f0 = u + 1 + XD + (u + 1 >= 4 ? 4 : 0), f = f0 < 16 ? f0 : 16;
        waitv<e - u - 1>();
        if constexpr (u == 0) pollend(pp);
        sfor<0, 5>([&](auto N) { if constexpr (s == 0) mm0(da[a][N], w2f[N], xf[s][a]); else mm(da[a][N], w2f[5 * s + N], xf[s][a]); });
        sfor<e, f>(ldx);
      });
    }
    {
      PP pp;
      poll2(pp, rW, fo + 8, tag);
      STAMP(16);
      sfor<0, 4>([&](auto A) { ldx(IC<16 + A>{}); ldx(IC<20 + A>{}); ldx(IC<24 + A>{}); });
      constexpr int HX = 0;
      lwait(9);
      sfor<0, 4>([&](auto A) {
        constexpr int a = A;
        waitv<4 * (3 - a) + 3 + HX>();
        if constexpr (a == 0) pollend(pp);
        sfor<0, 5>([&](auto N) { mm(da[a][N], w2f[20 + N], xf[4][a]); });
        waitv<4 * (3 - a) + 1 + HX>();
        sfor<0, 5>([&](auto N) {
          mmh(da[a][N], w2f[25 + N], xh[a][0], xh[a][1]);
          if constexpr (a > 0 && N < 3) { put(IC<a - 1>{}, N); __builtin_amdgcn_sched_barrier(0); }
        });
        waitv<4 * (3 - a) + HX>();
        sfor<0, 5>([&](auto N) {
          mm(da[a][N], w2f[30 + N], xf[6][a]);
          if constexpr (a > 0 && N < 2) { put(IC<a - 1>{}, IC<3 + N>{}); __builtin_amdgcn_sched_barrier(0); }
        });
      });
    }
    MM_DONE();
    STAMP(17);
    sfor<0, 5>([&](auto N) { put(IC<3>{}, N); });
    const rsrc_t rOut = __builtin_amdgcn_make_buffer_rsrc((void*)out, 0, 2048 * 1280, 0x00020000);
    const u32 LO = opaque(0x00003F80u), HI = opaque(0x3F800000u), n0 = 3 * nh;
    const u32 ra = opaque(L_RED + (mt * 5 + n0) * 4096 + l16);
    u2 bb[3];
#pragma unroll
    for (int i = 0; i < 3; i++) {
      bb[i] = lds64(L_CB + 1024 + (n0 + i) * 32 + g * 8);
      hr[i] = lds64(L_HR + (mt * 5 + n0 + i) * 512 + l8);
    }
    BAR();
    STAMP(18);
    u4 pv[3][4];
#pragma unroll
    for (int i = 0; i < 3; i++)
#pragma unroll
      for (int q = 0; q < 4; q++) pv[i][q] = lds128(ra + i * 4096 + q * 1024);
#pragma unroll
    for (int i = 0; i < 3; i++) {
      float s0 = 0.f, s1 = 0.f, s2 = 0.f, s3 = 0.f;
#pragma unroll
      for (int q = 0; q < 4; q++) {
        s0 = dot2c(pv[i][q].x, LO, s0); s1 = dot2c(pv[i][q].x, HI, s1); s2 = dot2c(pv[i][q].y, LO, s2); s3 = dot2c(pv[i][q].y, HI, s3);
        s0 = dot2c(pv[i][q].z, LO, s0); s1 = dot2c(pv[i][q].z, HI, s1); s2 = dot2c(pv[i][q].w, LO, s2); s3 = dot2c(pv[i][q].w, HI, s3);
      }
      const u32 o0 = pk2(unp(hr[i].x) + unp(pk2(f2{s0, s1} + unp(bb[i].x)))), o1 = pk2(unp(hr[i].y) + unp(pk2(f2{s2, s3} + unp(bb[i].y))));
      __builtin_amdgcn_raw_buffer_store_b64(u2{o0, o1}, rOut, (i == 2 && helper) ? OOB : lr * 1280 + g * 8 + i * 32, (pD * 64 + mt * 16) * 1280 + jD * 160 + n0 * 32, 0);
    }
  }
}

extern "C" void kernel_launch(void* const* d_in, const int* in_sizes, int n_in, void* d_out, int out_size, void* d_ws, size_t ws_size,
                              hipStream_t stream) {
  fused_block<<<256, 512, 0, stream>>>((const u16*)d_in[0], (const u16*)d_in[1], (const u16*)d_in[2], (const u16*)d_in[4], (const u16*)d_in[5],
                                       (const u16*)d_in[6], (const u16*)d_in[7], (u16*)d_out, (char*)d_ws);
}
```

```cpp
#include <hip/hip_runtime.h>
#include <stdint.h>
#define PROF
#define PROBE 47
#define PROBE0 47
#define PW 0
#pragma clang diagnostic ignored "-Wint-to-pointer-cast"
#pragma clang diagnostic ignored "-Warray-bounds"

typedef unsigned short u16;
typedef unsigned u32;
typedef unsigned long long u64;
typedef float f2 __attribute__((ext_vector_type(2)));
typedef float f4 __attribute__((ext_vector_type(4)));
typedef unsigned u2 __attribute__((ext_vector_type(2)));
typedef unsigned u4 __attribute__((ext_vector_type(4)));
typedef short s4 __attribute__((ext_vector_type(4)));
typedef __bf16 bf2 __attribute__((ext_vector_type(2)));
typedef __bf16 bf8 __attribute__((ext_vector_type(8)));
typedef __amdgpu_buffer_rsrc_t rsrc_t;
#define LAS __attribute__((address_space(3)))
#define GAS __attribute__((address_space(1)))
#define DI __device__ __forceinline__

template <int I> struct IC { static constexpr int v = I; constexpr operator int() const { return I; } };
template <int B, int E, class F> DI void sfor(F&& f) {
  if constexpr (B < E) { f(IC<B>{}); sfor<B + 1, E>(f); }
}

constexpr u32 WS_FRT = 4096, WS_FKV = 8192, WS_FO = 16384, WS_FH = 24576, WS_FHID = 32768;
constexpr u32 WS_KVP = 65536;
constexpr u32 WS_OB = 1u << 20;
constexpr u32 WS_HB = WS_OB + 32 * 81920;
constexpr u32 WS_ST = WS_HB + 32 * 81920;
constexpr u32 WS_CS1 = WS_ST + 2048 * 64;
constexpr u32 WS_WOT = WS_CS1 + 32768;
constexpr u32 WS_W1T = WS_WOT + 40 * 20480;
constexpr u32 WS_W2T = WS_W1T + 108 * 20480;
constexpr u32 WS_HID = WS_W2T + 8 * 54 * 5120;
constexpr u32 WS_END = WS_HID + 32 * 4 * 54 * 1024;
constexpr u32 WS_PROF = WS_END;
constexpr u32 OOB = 0x7FFFFF00u;
constexpr int SC1 = 16;
#define MAP 1
#define ROT 2
#define WQ 6
#define E2 0
#define PR 0
#define DMAP 0
#define OP 6
#define PS 4
#define RTE 5
#define HDV 8
#define TH2 4
#define TH 4
#define HA 8
#define HB2 12
#define CW 5
#define CG 32

constexpr u32 L_X = 0, L_RING = 81920;
constexpr u32 L_WO = 0, L_IMG = 147456, L_W1A = 110592, L_HR = 147456, L_CB = 160768, L_W1B = 0, L_RED = 0;

DI f4 mfma(u4 a, u4 b, f4 c) {
  return __builtin_amdgcn_mfma_f32_16x16x32_bf16(__builtin_bit_cast(bf8, a), __builtin_bit_cast(bf8, b), c, 0, 0, 0);
}
DI void mm(f4& c, u4 a, u4 b) { asm volatile("v_mfma_f32_16x16x32_bf16 %0, %1, %2, %0" : "+v"(c) : "v"(a), "v"(b) : "memory"); }
DI void mm0(f4& c, u4 a, u4 b) { asm volatile("v_mfma_f32_16x16x32_bf16 %0, %1, %2, 0" : "=&v"(c) : "v"(a), "v"(b) : "memory"); }
#define MM_DONE() asm volatile("s_nop 7\n\ts_nop 1" ::: "memory")
DI void pin(f4& c) { asm volatile("" : "+v"(c)); }
DI void zero(f4& c) { c = f4{0, 0, 0, 0}; asm volatile("" : "+v"(c)); }
DI void undef(u4& d) { asm volatile("" : "=v"(d)); }
DI u32 uni(u32 v) { v = __builtin_amdgcn_readfirstlane(v); asm volatile("" : "+s"(v)); return v; }
DI u32 opaque(u32 v) { asm volatile("" : "+v"(v)); return v; }

DI u32 pk2(float a, float b) { f2 v = {a, b}; return __builtin_bit_cast(u32, __builtin_convertvector(v, bf2)); }
DI u32 pk2(f2 v) { return __builtin_bit_cast(u32, __builtin_convertvector(v, bf2)); }
DI float rbf(float a) { return __builtin_bit_cast(float, pk2(a, a) << 16); }
DI float blo(u32 v) { return __builtin_bit_cast(float, v << 16); }
DI float bhi(u32 v) { return __builtin_bit_cast(float, v & 0xFFFF0000u); }
DI f2 unp(u32 v) { return f2{blo(v), bhi(v)}; }
DI float asf(u32 v) { return __builtin_bit_cast(float, v); }
DI u32 asu(float v) { return __builtin_bit_cast(u32, v); }

template <int OFF> DI void dma(rsrc_t r, u32 voff, u32 soff, u32 lds) {
  asm volatile("s_mov_b32 m0, %0\n\ts_nop 0\n\tbuffer_load_dwordx4 %1, %2, %3 offen offset:%4 lds" ::"s"(lds), "v"(voff), "s"(r), "s"(soff), "n"(OFF) : "memory");
}
template <int OFF> DI void dma_c(rsrc_t r, u32 voff, u32 soff) {
  asm volatile("buffer_load_dwordx4 %0, %1, %2 offen offset:%3 lds" ::"v"(voff), "s"(r), "s"(soff), "n"(OFF) : "memory");
}
DI void dma4_sc1(rsrc_t r, u32 voff, u32 soff, u32 lds) {
  asm volatile("s_mov_b32 m0, %0\n\ts_nop 0\n\tbuffer_load_dwordx4 %1, %2, %3 offen sc1 lds\n\tbuffer_load_dwordx4 %1, %2, %3 offen offset:1024 sc1 lds\n\t"
               "buffer_load_dwordx4 %1, %2, %3 offen offset:2048 sc1 lds\n\tbuffer_load_dwordx4 %1, %2, %3 offen offset:3072 sc1 lds" ::"s"(lds), "v"(voff), "s"(r), "s"(soff) : "memory");
}
DI void dma2_sc1(rsrc_t r, u32 voff, u32 soff, u32 lds) {
  asm volatile("s_mov_b32 m0, %0\n\ts_nop 0\n\tbuffer_load_dwordx4 %1, %2, %3 offen sc1 lds\n\tbuffer_load_dwordx4 %1, %2, %3 offen offset:1024 sc1 lds" ::"s"(lds), "v"(voff), "s"(r), "s"(soff) : "memory");
}
DI void dma1_sc1(rsrc_t r, u32 voff, u32 soff, u32 lds) {
  asm volatile("s_mov_b32 m0, %0\n\ts_nop 0\n\tbuffer_load_dwordx4 %1, %2, %3 offen sc1 lds" ::"s"(lds), "v"(voff), "s"(r), "s"(soff) : "memory");
}
template <int OFF> DI void ldv(u4& d, rsrc_t r, u32 voff, u32 soff) {
  asm volatile("buffer_load_dwordx4 %0, %1, %2, %3 offen offset:%4 sc1" : "=v"(d) : "v"(voff), "s"(r), "s"(soff), "n"(OFF) : "memory");
}
template <int OFF> DI void ldv_rw(u4& d, rsrc_t r, u32 voff, u32 soff) {
  asm volatile("buffer_load_dwordx4 %0, %1, %2, %3 offen offset:%4 sc1" : "+v"(d) : "v"(voff), "s"(r), "s"(soff), "n"(OFF) : "memory");
}
template <int OFF> DI void ldp(u4& d, rsrc_t r, u32 voff, u32 soff) {
  asm volatile("buffer_load_dwordx4 %0, %1, %2, %3 offen offset:%4" : "=v"(d) : "v"(voff), "s"(r), "s"(soff), "n"(OFF) : "memory");
}
template <int OFF> DI void ldp2(u2& d, rsrc_t r, u32 voff, u32 soff) {
  asm volatile("buffer_load_dwordx2 %0, %1, %2, %3 offen offset:%4" : "=v"(d) : "v"(voff), "s"(r), "s"(soff), "n"(OFF) : "memory");
}
template <int N> DI void waitv() { asm volatile("s_waitcnt vmcnt(%0)" ::"n"(N) : "memory"); }
template <class T> DI void landed(T& d) { asm volatile("" : "+v"(d)); }
#define BAR() do { asm volatile("" ::: "memory"); __builtin_amdgcn_s_waitcnt(0xC07F); asm volatile("s_barrier" ::: "memory"); } while (0)
DI u4 lds128(u32 a) { return *(LAS u4*)(a); }
DI u2 lds64(u32 a) { return *(LAS u2*)(a); }
DI void sts128(u32 a, u4 v) { *(LAS u4*)(a) = v; }
DI void sts64(u32 a, u2 v) { *(LAS u2*)(a) = v; }
DI void sts32(u32 a, float v) { *(LAS float*)(a) = v; }
DI u4 ldstr(u32 a0, u32 a1) {
  u2 x = __builtin_bit_cast(u2, __builtin_amdgcn_ds_read_tr16_b64_v4i16((LAS s4*)(a0)));
  u2 y = __builtin_bit_cast(u2, __builtin_amdgcn_ds_read_tr16_b64_v4i16((LAS s4*)(a1)));
  return u4{x.x, x.y, y.x, y.y};
}
DI u4 ldstr(u32 a) { return ldstr(a, a + 2560); }
DI float xsum16_32(float v) {
  auto r = __builtin_amdgcn_permlane16_swap(asu(v), asu(v), false, false);
  v = asf((u32)r[0]) + asf((u32)r[1]);
  auto q = __builtin_amdgcn_permlane32_swap(asu(v), asu(v), false, false);
  return asf((u32)q[0]) + asf((u32)q[1]);
}
DI float xsum8(float v) {
  v += asf(__builtin_amdgcn_update_dpp(0, asu(v), 0xB1, 0xf, 0xf, true));
  v += asf(__builtin_amdgcn_update_dpp(0, asu(v), 0x4E, 0xf, 0xf, true));
  return v + asf(__builtin_amdgcn_update_dpp(0, asu(v), 0x141, 0xf, 0xf, true));
}
DI void poll(const char* ws, u32 off, u64 tag) {
  const GAS u64* f = (const GAS u64*)(ws + off);
#pragma nounroll
  for (u32 it = 0; it < (1u << 18); ++it) {
    u64 v = __hip_atomic_load(f, __ATOMIC_RELAXED, __HIP_MEMORY_SCOPE_AGENT);
    if (__all(v == tag)) break;
    __builtin_amdgcn_s_sleep(2);
  }
  __builtin_amdgcn_fence(__ATOMIC_ACQUIRE, "wavefront");
  asm volatile("" ::: "memory");
}
DI void lset_(u32 k, u32 mt, u32 tg) { __builtin_amdgcn_s_waitcnt(0xC07F); *(volatile LAS u32*)(L_CB + 1536 + k * 16 + mt * 4) = tg; asm volatile("" ::: "memory"); }
DI void lwait_(u32 k, u32 tg) {
  asm volatile("" ::: "memory");
  for (;;) {
    const u4 v = *(volatile LAS u4*)(L_CB + 1536 + k * 16);
    if (__builtin_amdgcn_readfirstlane((v.x ^ tg) | (v.y ^ tg) | (v.z ^ tg) | (v.w ^ tg)) == 0) break;
    __builtin_amdgcn_s_sleep(1);
  }
  asm volatile("" ::: "memory");
}
#define lset(k, mt) lset_(k, mt, (u32)tag)
#define lwait(k) lwait_(k, (u32)tag)
struct PP { u2 a, b; };
DI void ldf(u2& d, rsrc_t r, u32 voff) { asm volatile("buffer_load_dwordx2 %0, %1, %2, 0 offen sc1" : "+v"(d) : "v"(voff), "s"(r) : "memory"); }
DI void poll2(PP& pp, rsrc_t r, u32 voff, u64 tag) {
  const u32 tl = (u32)tag, th = (u32)(tag >> 32);
  asm volatile("" : "=v"(pp.a), "=v"(pp.b));
  ldf(pp.a, r, voff);
  __builtin_amdgcn_s_sleep(PS);
#pragma nounroll
  for (u32 it = 0; it < (1u << 18); ++it) {
    ldf(pp.b, r, voff);
    waitv<1>(); landed(pp.a);
    if (!__any(((pp.a.x ^ tl) | (pp.a.y ^ th)) != 0)) break;
    ldf(pp.a, r, voff);
    waitv<1>(); landed(pp.b);
    if (!__any(((pp.b.x ^ tl) | (pp.b.y ^ th)) != 0)) break;
  }
  asm volatile("" ::: "memory");
}
DI void pollend(PP& pp) { asm volatile("" ::"v"(pp.a), "v"(pp.b)); }
DI void setflag(char* ws, u32 off, u64 tag, u32 lane) {
  if (lane == 0) __hip_atomic_store((GAS u64*)(ws + off), tag, __ATOMIC_RELAXED, __HIP_MEMORY_SCOPE_AGENT);
  asm volatile("" ::: "memory");
}
template <bool PHI> DI u32 phi2(float a0, float a1, float r0, float r1) {
  const f2 a = {a0, a1}, r = {r0, r1};
  if constexpr (!PHI) return pk2(a * r);
  const f2 t = a * r + 1e-6f, u = a * (r * 1.4426950408889634f);
  return pk2(fmaxf(t[0], 1e-6f) + __builtin_amdgcn_fmed3f(__builtin_amdgcn_exp2f(u[0]), 0.f, 1.f),
             fmaxf(t[1], 1e-6f) + __builtin_amdgcn_fmed3f(__builtin_amdgcn_exp2f(u[1]), 0.f, 1.f));
}
DI float dot2c(u32 a, u32 b, float c) { return __builtin_amdgcn_fdot2_f32_bf16(__builtin_bit_cast(bf2, a), __builtin_bit_cast(bf2, b), c, false); }
DI u4 center8(u4 x, u32 lo, u32 hi, float nm) {
  u4 r; float t0, t1, t2, t3, t4, t5, t6, t7;
  asm("v_dot2_f32_bf16 %4, %12, %16, %18\n\tv_dot2_f32_bf16 %5, %12, %17, %18\n\tv_dot2_f32_bf16 %6, %13, %16, %18\n\tv_dot2_f32_bf16 %7, %13, %17, %18\n\t"
      "v_dot2_f32_bf16 %8, %14, %16, %18\n\tv_dot2_f32_bf16 %9, %14, %17, %18\n\tv_dot2_f32_bf16 %10, %15, %16, %18\n\tv_dot2_f32_bf16 %11, %15, %17, %18\n\t"
      "v_cvt_pk_bf16_f32 %0, %4, %5\n\tv_cvt_pk_bf16_f32 %1, %6, %7\n\tv_cvt_pk_bf16_f32 %2, %8, %9\n\tv_cvt_pk_bf16_f32 %3, %10, %11"
      : "=v"(r.x), "=v"(r.y), "=v"(r.z), "=v"(r.w), "=&v"(t0), "=&v"(t1), "=&v"(t2), "=&v"(t3), "=&v"(t4), "=&v"(t5), "=&v"(t6), "=&v"(t7)
      : "v"(x.x), "v"(x.y), "v"(x.z), "v"(x.w), "v"(lo), "v"(hi), "v"(nm));
  return r;
}
template <int N> DI void waitl() { asm volatile("" ::: "memory"); __builtin_amdgcn_s_waitcnt(0xC07F | (N << 8)); asm volatile("" ::: "memory"); }
DI void st128(u4 v, rsrc_t r, u32 voff, u32 soff) {
  __builtin_amdgcn_raw_buffer_store_b128(v, r, voff, soff, SC1);
  asm volatile("s_nop 1" ::"v"(v) : "memory");
}
DI void st64(u2 v, rsrc_t r, u32 voff, u32 soff) { __builtin_amdgcn_raw_buffer_store_b64(v, r, voff, soff, SC1); }
DI void publish20(const u32* v, rsrc_t r, u32 l16, u32 so, u32 j) {
  if ((j & 1) == 0) {
    st128(u4{v[0], v[1], v[2], v[3]}, r, l16, so);
    st128(u4{v[4], v[5], v[6], v[7]}, r, l16, so + 1024);
    st64(u2{v[8], v[9]}, r, l16 >> 1, so + 2048);
  } else {
    st64(u2{v[0], v[1]}, r, l16 >> 1, so + 2560);
    st128(u4{v[2], v[3], v[4], v[5]}, r, l16, so + 3072);
    st128(u4{v[6], v[7], v[8], v[9]}, r, l16, so + 4096);
  }
}
template <int OFF> DI void ldv2(u2& d, rsrc_t r, u32 voff, u32 soff) {
  asm volatile("buffer_load_dwordx2 %0, %1, %2, %3 offen offset:%4 sc1" : "=v"(d) : "v"(voff), "s"(r), "s"(soff), "n"(OFF) : "memory");
}
DI void mmh(f4& c, u4 a, u2 b0, u2 b1) { mm(c, a, u4{b0.x, b0.y, b1.x, b1.y}); }
template <int V, int L> DI void waitvl() {
  asm volatile("" ::: "memory"); __builtin_amdgcn_s_waitcnt(0x0070 | (V & 15) | ((V >> 4) << 14) | (L << 8)); asm volatile("" ::: "memory");
}

#define PROFW 3
#define TIMEOP "s_memrealtime"
#define STAMP(i) do { u64 t_; asm volatile(TIMEOP " %0\n\ts_waitcnt lgkmcnt(0)" : "=s"(t_) :: "memory"); asm volatile("s_nop 0\n\tv_writelane_b32 %0, %1, %2" : "+v"(tsv) : "s"((u32)t_), "n"(i)); } while (0)

__global__ __launch_bounds__(512, 2) void fused_block(const u16* __restrict__ x, const u16* __restrict__ wqkv, const u16* __restrict__ wo,
                                                      const u16* __restrict__ w1, const u16* __restrict__ b1, const u16* __restrict__ w2,
                                                      const u16* __restrict__ b2, u16* __restrict__ out, char* __restrict__ ws) {
  __shared__ __attribute__((aligned(1024))) char smem[163840];
  asm volatile("" ::"v"((u32)(uintptr_t)(LAS char*)smem));
  u32 tsv = 0;
  STAMP(47);
  const u32 tid = threadIdx.x, lane = tid & 63, lr = lane & 15, g = lane >> 4;
  const u32 w = __builtin_amdgcn_readfirstlane(tid >> 6), mt = w & 3, nh = w >> 2;
  const bool helper = nh != 0;
  const u32 b = blockIdx.x, xg = b & 7, rr = b >> 3, p = 8 * (xg >> 1) + (rr >> 2), j = 4 * (xg & 1) + (rr & 3);
  asm volatile("" ::"s"(x), "s"(wqkv), "s"(wo), "s"(w1), "s"(b1), "s"(w2), "s"(b2), "s"(out), "s"(ws));
  STAMP(0);
  const rsrc_t rW = __builtin_amdgcn_make_buffer_rsrc((void*)ws, 0, WS_END, 0x00020000);
  const u32 l16 = lane * 16, l8 = lane * 8;
  u2 ep;
  u64 tag;
#define LD_EPOCH() asm volatile("buffer_load_dwordx2 %0, off, %1, 0 sc1" : "=v"(ep) : "s"(rW) : "memory")
#define GET_TAG() do { u32 lo_, hi_; asm volatile("v_readfirstlane_b32 %0, %2\n\tv_readfirstlane_b32 %1, %3\n\ts_nop 3" : "=s"(lo_), "=s"(hi_) : "v"(ep.x), "v"(ep.y) : "memory"); \
    tag = (((u64)hi_ << 32) | lo_) + 1; } while (0)
  u32 rot = xg + 3 * (p & 3), rotq = xg + (p & 3);
  rot = rot >= 10 ? rot - 10 : rot; rotq = rotq >= 10 ? rotq - 10 : (rotq >= 5 ? rotq - 5 : rotq);
  const u32 P = p >> 1, c = ((p & 1) << 3) | j, rho = c >> 1, odd = c & 1;
  const u32 t0 = rho < 6 ? 14 * rho + 8 * odd : 12 * rho + 12 + 6 * odd, tl = rho < 6 ? 14 * rho + 6 + odd : t0;
  const u32 pD = p, jD = j;
  const u32 s0 = w < 6 ? 7 * w : 6 * w + 6;
  const u32 vlast = w >= 6 ? OOB : l16 + 3072, vmid = w >= 6 ? l16 : l16 + 1024, vsp = w >= 6 ? l16 + 2048 : l16, soW2 = WS_W2T + (jD * 5 * 54 + s0) * 1024;
  u4 w2f[35];
  auto ldw2 = [&](auto I) {
    constexpr int s = I / 5, n = I % 5;
    if constexpr (s < 2) ldv<s * 1024>(w2f[I], rW, l16, soW2 + n * 54 * 1024);
    else if constexpr (s < 4) ldv<(s - 2) * 1024>(w2f[I], rW, vmid, soW2 + (n * 54 + 3) * 1024);
    else if constexpr (s == 4) ldv<2048>(w2f[I], rW, l16, soW2 + n * 54 * 1024);
    else ldv<0>(w2f[I], rW, s == 5 ? vsp : vlast, soW2 + (n * 54 + 3) * 1024);
  };
#define RT_CHECK() do { waitv<0>(); landed(fr[0]); landed(fr[1]); const u32 tl32 = (u32)tag, th32 = (u32)(tag >> 32); \
    const u32 bad = ((fr[0].x ^ tl32) | (fr[0].z ^ tl32) | (fr[1].x ^ tl32) | (fr[1].z ^ tl32)) | ((fr[0].y ^ th32) | (fr[0].w ^ th32) | (fr[1].y ^ th32) | (fr[1].w ^ th32)); \
    if (__any(bad != 0)) { _Pragma("nounroll") for (u32 q = 0; q < 4; q++) poll(ws, WS_FRT + (64 * q + lane) * 8, tag); } } while (0)
  constexpr u32 L_RS = L_RING + 4096;

  if (!helper) {
    const rsrc_t rX = __builtin_amdgcn_make_buffer_rsrc((void*)x, 0, 2048 * 1280, 0x00020000);
    float rsd[2];
    const u32 ONE2 = opaque(0x3F803F80u);
    u4 xr[2][10];
    float dm[2];
    {
      const u32 vx = (lane >> 3) * 1280 + (lane & 7) * 16, sx = (p * 64 + 16 * mt) * 1280;
      sfor<0, 10>([&](auto I) { ldp<I * 128>(xr[0][I], rX, vx, sx); });
      sfor<0, 10>([&](auto I) { ldp<I * 128>(xr[1][I], rX, vx, sx + 8 * 1280); });
      LD_EPOCH();
    }
    const u32 xa0 = opaque(L_X + (lr >> 3) * 1024 + (lr & 7) * 128 + ((g ^ (lr & 7)) << 4));
    const u32 tr0 = opaque(L_RING + (4 * g + (lr >> 2)) * 160 + (lr & 3) * 8);
    const u32 mh = mt & 1, nq = mt >> 1;
    float qrs;
    u4 xq[20];
    {
      f4 acc[2][5];
#pragma unroll
      for (int i = 0; i < 5; i++) { zero(acc[0][i]); zero(acc[1][i]); }
      const u32 bA = opaque(tr0 + nq * 96), bC = opaque(tr0 + 64 + nq * 5120);
      const u32 xs0 = opaque(xa0 + mh * 4096);
      u32 lkv[6];
#pragma unroll
      for (int q = 0; q < 2; q++) {
        const u32 dq = 4 * q + g - mt, e0 = lr - 4 * mt, e1 = e0 + 16, sb = L_RING + mt * 5120;
        lkv[3 * q] = opaque(sb + ((dq < 5 && e0 < 20) ? e0 * 40 + dq * 8 : 896));
        lkv[3 * q + 1] = opaque(sb + ((dq < 5 && e1 < 20) ? e1 * 40 + dq * 8 : 896));
        lkv[3 * q + 2] = opaque(sb + ((dq < 5 && lr == 0) ? 800 + dq * 8 : (dq == 0 && lr == 1) ? 840 : 896));
      }
      {
        const u32 wa = opaque(L_X + mt * 2048 + (lane >> 3) * 128 + (((lane & 7) ^ (lane >> 3)) << 4));
        const u32 lo = opaque(0x00003F80u), hi = opaque(0x3F800000u);
        sfor<0, 2>([&](auto G) {
          waitv<(G == 0 ? 11 : 1)>();
          STAMP(20 + G);
          float s0 = 0.f, s1 = 0.f;
#pragma unroll
          for (int i = 0; i < 10; i++) {
            landed(xr[G][i]);
            s0 = dot2c(xr[G][i].x, ONE2, s0); s1 = dot2c(xr[G][i].y, ONE2, s1); s0 = dot2c(xr[G][i].z, ONE2, s0); s1 = dot2c(xr[G][i].w, ONE2, s1);
          }
          const float m = xsum8(s0 + s1) * (1.f / 640.f), nm = -rbf(m);
          dm[G] = m + nm;
#pragma unroll
          for (int i = 0; i < 10; i++) { xr[G][i] = center8(xr[G][i], lo, hi, nm); sts128(wa + G * 1024 + i * 8192, xr[G][i]); }
        });
      }
      STAMP(22);
      auto half = [&](u4* cur, u4* nxt, u32 xa, u32 a, u32 c, auto KS, auto LD) {
        constexpr int o = KS * 10240;
        mm(acc[0][0], cur[5], cur[0]);
        if constexpr (LD) nxt[5] = lds128(xa);
        mm(acc[1][0], cur[6], cur[0]);
        if constexpr (LD) nxt[6] = lds128(xa + 2048);
        mm(acc[0][1], cur[5], cur[1]);
        if constexpr (LD) nxt[0] = ldstr(a + o);
        mm(acc[1][1], cur[6], cur[1]);
        if constexpr (LD) nxt[1] = ldstr(a + o + 32);
        mm(acc[0][2], cur[5], cur[2]);
        if constexpr (LD) nxt[2] = ldstr(c + o);
        mm(acc[1][2], cur[6], cur[2]);
        if constexpr (LD) nxt[3] = ldstr(a + o + 5120);
        mm(acc[0][3], cur[5], cur[3]);
        if constexpr (LD) nxt[4] = ldstr(a + o + 5152);
        mm(acc[1][3], cur[6], cur[3]);
        mm(acc[0][4], cur[5], cur[4]); mm(acc[1][4], cur[6], cur[4]);
      };
      u4 fa[7], fb[7];
      u32 st = rot, xa = xs0 + st * 8192, a = bA, cc = bC;
      BAR();
      STAMP(1);
      fa[5] = lds128(xa); fa[6] = lds128(xa + 2048); fa[0] = ldstr(a); fa[1] = ldstr(a + 32); fa[2] = ldstr(cc);
      fa[3] = ldstr(a + 5120); fa[4] = ldstr(a + 5152);
      sfor<0, 2>([&](auto G) {
        float s0 = 0.f, s1 = 0.f;
#pragma unroll
        for (int i = 0; i < 10; i++) {
          landed(xr[G][i]);
          const u4 v = xr[G][i];
          s0 = dot2c(v.x, v.x, s0); s1 = dot2c(v.y, v.y, s1); s0 = dot2c(v.z, v.z, s0); s1 = dot2c(v.w, v.w, s1);
        }
        const float var = fmaxf(xsum8(s0 + s1) * (1.f / 640.f) - dm[G] * dm[G], 0.f);
        rsd[G] = __builtin_amdgcn_rcpf(rbf(__builtin_amdgcn_sqrtf(rbf(rbf(var) + 1e-5f))));
        landed(rsd[G]);
      });
      waitv<0>();
      GET_TAG();
#pragma nounroll
      for (u32 i = 1; i < 10; i++) {
        waitl<0>();
        half(fa, fb, xa ^ 64, a, cc, IC<1>{}, IC<1>{});
        st = st + 1 >= 10 ? 0 : st + 1;
        xa = opaque(xs0 + st * 8192); a = opaque(bA + (i & 3) * 20480); cc = opaque(bC + (i & 3) * 20480);
        BAR();
        half(fb, fa, xa, a, cc, IC<0>{}, IC<1>{});
      }
      if ((lane & 7) == 0) { sts32(L_RS + mt * 64 + (lane >> 3) * 4, rsd[0]); sts32(L_RS + mt * 64 + 32 + (lane >> 3) * 4, rsd[1]); }
      waitl<0>();
      half(fa, fb, xa ^ 64, a, cc, IC<1>{}, IC<1>{});
      {
        u32 qs = rotq;
#pragma unroll
        for (int q = 0; q < 5; q++) {
          const u32 xb = xa0 + mt * 2048 + qs * 16384, xc = xb ^ 64;
          xq[4 * q] = lds128(xb); xq[4 * q + 1] = lds128(xc); xq[4 * q + 2] = lds128(xb + 8192); xq[4 * q + 3] = lds128(xc + 8192);
          qs = qs + 1 >= 5 ? 0 : qs + 1;
        }
      }
      BAR();
      const f4 r0 = __builtin_bit_cast(f4, lds128(L_RS + mh * 128 + g * 16)), r1 = __builtin_bit_cast(f4, lds128(L_RS + mh * 128 + 64 + g * 16));
      qrs = *(LAS float*)(L_RS + mt * 64 + lr * 4);
      half(fb, fa, 0, 0, 0, IC<0>{}, IC<0>{});
      MM_DONE();
      __builtin_amdgcn_s_setprio(PR);
      STAMP(2);
      const u32 EX = L_RING + 20480;
      const u32 ew = EX + mh * 10240 + nq * 3072 + l16;
      auto put = [&](auto N, auto PHI, u32 a) {
        constexpr int n = N;
        pin(acc[0][n]); pin(acc[1][n]);
        sts128(a, u4{phi2<PHI>(acc[0][n][0], acc[0][n][1], r0[0], r0[1]), phi2<PHI>(acc[0][n][2], acc[0][n][3], r0[2], r0[3]),
                     phi2<PHI>(acc[1][n][0], acc[1][n][1], r1[0], r1[1]), phi2<PHI>(acc[1][n][2], acc[1][n][3], r1[2], r1[3])});
      };
      put(IC<0>{}, IC<1>{}, ew); put(IC<1>{}, IC<1>{}, ew + 1024); put(IC<3>{}, IC<0>{}, ew + 5120); put(IC<4>{}, IC<0>{}, ew + 6144);
      if (nq == 0) put(IC<2>{}, IC<1>{}, ew + 2048); else put(IC<2>{}, IC<0>{}, ew + 4096);
      STAMP(41);
      BAR();
      STAMP(42);
      const u32 ex = EX + mt * 1024 + l16;
      u4 kf[2][2], vf[2][2];
#pragma unroll
      for (int m = 0; m < 2; m++) {
        kf[m][0] = lds128(ex + m * 10240); vf[m][0] = lds128(ex + m * 10240 + 5120);
        vf[m][1] = lds128(ex + m * 10240 + 6144); kf[m][1] = lds128(ex + m * 10240 + 1024);
      }
      const u32 o0 = lr == 0 ? ONE2 : 0u;
      const u4 ONE0 = {o0, o0, o0, o0};
#pragma unroll
      for (int q = 0; q < 2; q++) {
        f4 z4 = {0, 0, 0, 0};
        f4 kv0 = mfma(kf[1][q], vf[1][0], mfma(kf[0][q], vf[0][0], z4)), kv1 = mfma(kf[1][q], vf[1][1], mfma(kf[0][q], vf[0][1], z4));
        f4 zz = mfma(kf[1][q], ONE0, mfma(kf[0][q], ONE0, z4));
        sts64(lkv[3 * q], u2{pk2(kv0[0], kv0[1]), pk2(kv0[2], kv0[3])});
        sts64(lkv[3 * q + 1], u2{pk2(kv1[0], kv1[1]), pk2(kv1[2], kv1[3])});
        sts64(lkv[3 * q + 2], u2{pk2(zz[0], zz[1]), pk2(zz[2], zz[3])});
      }
      asm volatile("" ::: "memory");
      st128(lds128(L_RING + mt * 5120 + l16), rW, lane < 56 ? l16 : OOB, WS_KVP + ((j * 32 + p) * 4 + mt) * 896);
    }
    u32 pq[5][2];
    u2 xres[5];
    {
      const u32 qa0 = opaque(tr0 + 2 * 20480), qa1 = opaque(tr0 - L_RING);
      u4 wf[2][5];
#pragma unroll
      for (int n = 0; n < 5; n++) wf[0][n] = ldstr(qa0 + n * 32);
      waitv<0>();
      setflag(ws, WS_FKV + ((j * 4 + mt) * 32 + p) * 8, tag, lane);
      STAMP(3);
      sfor<0, 5>([&](auto N) { ldp2<N * 32>(xres[N], rX, lr * 1280 + g * 8, (p * 64 + mt * 16) * 1280 + j * 160); });
      f4 qa[5];
      sfor<0, 20>([&](auto S) {
        constexpr int s = S, t = s + 1;
        if constexpr (s == 7) BAR();
        waitl<6>();
        sfor<0, 5>([&](auto N) {
          constexpr int n = N;
          if constexpr (n == 2) waitl<(s < 19 ? 4 : 0)>();
          if constexpr (s == 0) mm0(qa[n], wf[0][n], xq[0]); else mm(qa[n], wf[s & 1][n], xq[s]);
          if constexpr (s < 19) wf[t & 1][n] = ldstr((t < 8 ? qa0 + t * 5120 : qa1 + (t - 8) * 5120) + n * 32);
        });
      });
      MM_DONE();
      STAMP(4);
#pragma unroll
      for (int n = 0; n < 5; n++) {
        pin(qa[n]);
        pq[n][0] = opaque(phi2<1>(qa[n][0], qa[n][1], qrs, qrs)); pq[n][1] = opaque(phi2<1>(qa[n][2], qa[n][3], qrs, qrs));
      }
    }
    STAMP(5);
    __builtin_amdgcn_s_setprio(2);
    STAMP(23);
    BAR();
    STAMP(6);
    {
      float o[20];
#pragma unroll
      for (int h = 0; h < 4; h++) {
        u4 bq = {pq[h][0], pq[h][1], pq[h + 1][0], pq[h + 1][1]};
        f4 z4 = {0, 0, 0, 0};
        f4 d0 = mfma(lds128(L_IMG + h * 2048 + l16), bq, z4);
        f4 d1 = mfma(lds128(L_IMG + h * 2048 + 1024 + l16), bq, z4);
        float rd = __builtin_amdgcn_rcpf(d1[1] + 1e-6f);
#pragma unroll
        for (int i = 0; i < 4; i++) o[5 * h + i] = d0[i] * rd;
        o[5 * h + 4] = d1[0] * rd;
      }
      u32 ov[10];
#pragma unroll
      for (int i = 0; i < 10; i++) ov[i] = pk2(o[2 * i], o[2 * i + 1]);
      publish20(ov, rW, l16, WS_OB + ((p * 4 + mt) * 20 + 5 * (j >> 1)) * 1024, j);
    }
    waitv<0>();
    setflag(ws, WS_FO + ((p * 4 + mt) * 8 + j) * 8, tag, lane);
    BAR();
    STAMP(7);
    {
      u4 of[20], wf[2][5];
      u2 oh[4][2];
      const u32 so = WS_OB + (p * 4 + mt) * 20480;
      auto ldo = [&](auto S) {
        constexpr int s = S;
        if constexpr (s % 5 == 2) { ldv2<(s & 3) * 1024>(oh[s / 5][0], rW, l8, so + (s & ~3) * 1024); ldv2<(s & 3) * 1024 + 512>(oh[s / 5][1], rW, l8, so + (s & ~3) * 1024); }
        else ldv<(s & 3) * 1024>(of[s], rW, l16, so + (s & ~3) * 1024);
      };
      const u32 a0 = opaque(L_WO + l16), a1 = opaque(L_WO + 51200 + l16);
#pragma unroll
      for (int q = 0; q < 2; q++) {
#pragma unroll
        for (int n = 0; n < 5; n++) wf[q][n] = lds128(a0 + n * 10240 + q * 1024);
        asm volatile("" ::: "memory");
      }
      PP pp;
      poll2(pp, rW, WS_FO + ((p * 4 + mt) * 8 + (lane & 7)) * 8, tag);
      STAMP(8);
      sfor<0, OP>(ldo);
      f4 ha[1][5];
      sfor<0, 20>([&](auto S) {
        constexpr int s = S, e = OP + 2 * s < 20 ? OP + 2 * s : 20, f = OP + 2 + 2 * s < 20 ? OP + 2 + 2 * s : 20;
        constexpr int yo = (e - s - 1) + ((e + 2) / 5 - (s + 3) / 5);
        if constexpr (s == 8) { STAMP(38); lwait(2); STAMP(39); }
        if constexpr (s == 10) lset(1, mt);
        waitvl<yo, (s < 19 && s != 8 && s != 10 ? 5 : 0)>();
        if constexpr (s == 0) { pollend(pp); STAMP(37); }
        sfor<0, 5>([&](auto N) {
          constexpr int n = N, t = s + 2;
          if constexpr (s == 0) mm0(ha[0][n], wf[0][n], of[s]);
          else if constexpr (s % 5 == 2) mmh(ha[0][n], wf[s & 1][n], oh[s / 5][0], oh[s / 5][1]);
          else mm(ha[0][n], wf[s & 1][n], of[s]);
          if constexpr (t < 20) wf[s & 1][n] = lds128((t < 10 ? a0 : a1) + n * 10240 + (t % 10) * 1024);
          if constexpr (n == 1 && e < f) ldo(IC<e>{});
          if constexpr (n == 3 && e + 1 < f) ldo(IC<e + 1>{});
        });
      });
      MM_DONE();
#pragma unroll
      for (int n = 0; n < 5; n++) pin(ha[0][n]);
      STAMP(9);
      u32 hres[10];
      float sum = 0.f, sq = 0.f;
#pragma unroll
      for (int n = 0; n < 5; n++) {
        hres[2 * n] = pk2(unp(xres[n].x) + f2{ha[0][n][0], ha[0][n][1]});
        hres[2 * n + 1] = pk2(unp(xres[n].y) + f2{ha[0][n][2], ha[0][n][3]});
      }
      publish20(hres, rW, l16, WS_HB + ((p * 4 + mt) * 20 + 5 * (j >> 1)) * 1024, j);
#pragma unroll
      for (int i = 0; i < 10; i++) { sum = dot2c(hres[i], ONE2, sum); sq = dot2c(hres[i], hres[i], sq); }
      st64(u2{asu(xsum16_32(sum)), asu(xsum16_32(sq))}, rW, g == 0 ? l8 : OOB, WS_ST + ((p * 4 + mt) * 8 + j) * 128);
#pragma unroll
      for (int n = 0; n < 5; n++) sts64(L_HR + (mt * 5 + n) * 512 + l8, u2{hres[2 * n], hres[2 * n + 1]});
      waitv<0>();
      setflag(ws, WS_FH + ((p * 4 + mt) * 8 + j) * 8, tag, lane);
      lset(3, mt);
      STAMP(10);
      lwait(4);
    }
    {
      constexpr int HD = HDV;
      const u32 pc = 2 * P + (mt >> 1), mc = 2 * (mt & 1);
      f4 ca[2][7];
      u4 hf[2][20], wf[7];
      u2 hh[2][4][2];
      const u32 so = WS_HB + (pc * 4 + mc) * 20480;
      auto ldh = [&](auto S) {
        constexpr int s = S;
        sfor<0, 2>([&](auto M) {
          if constexpr (s % 5 == 2) {
            ldv2<(s & 3) * 1024>(hh[M][s / 5][0], rW, l8, so + M * 20480 + (s & ~3) * 1024);
            ldv2<(s & 3) * 1024 + 512>(hh[M][s / 5][1], rW, l8, so + M * 20480 + (s & ~3) * 1024);
          } else ldv<(s & 3) * 1024>(hf[M][s], rW, l16, so + M * 20480 + (s & ~3) * 1024);
        });
      };
      const u32 a0 = opaque(L_W1A + l16), a1 = opaque(L_W1B + l16), a2 = opaque(L_W1B + 35840 + l16), a3 = opaque(L_W1B + 71680 + l16);
#pragma unroll
      for (int n = 0; n < 7; n++) wf[n] = lds128(a0 + n * 5120);
      PP pp;
      poll2(pp, rW, WS_FH + ((pc * 4 + mc) * 8 + lr) * 8, tag);
      STAMP(11);
      sfor<0, 3>(ldh);
      sfor<0, 20>([&](auto S) {
        constexpr int s = S, e1 = 3 + 2 * s < s + HD ? 3 + 2 * s : s + HD, e = e1 < 20 ? e1 : 20;
        constexpr int f1 = 5 + 2 * s < s + 1 + HD ? 5 + 2 * s : s + 1 + HD, f = f1 < 20 ? f1 : 20;
        constexpr int yo = 2 * ((e - s - 1) + ((e + 2) / 5 - (s + 3) / 5));
        if constexpr (s == 9) { STAMP(33); lwait(5); STAMP(34); }
        waitvl<yo, 3>();
        if constexpr (s == 0) { pollend(pp); STAMP(31); }
        if constexpr (s == 5) STAMP(32);
        if constexpr (s == 14) STAMP(35);
        sfor<0, 7>([&](auto N) {
          constexpr int n = N, t = s + 1;
          if constexpr (n == 4) waitl<(s < 19 ? 4 : 0)>();
          sfor<0, 2>([&](auto M) {
            if constexpr (s == 0) mm0(ca[M][n], wf[n], hf[M][s]);
            else if constexpr (s % 5 == 2) mmh(ca[M][n], wf[n], hh[M][s / 5][0], hh[M][s / 5][1]);
            else mm(ca[M][n], wf[n], hf[M][s]);
          });
          if constexpr (s < 19) wf[n] = lds128((t < 5 ? a0 : t < 10 ? a1 : t < 15 ? a2 : a3) + n * 5120 + (t % 5) * 1024);
          if constexpr (n == 1 && e < f) ldh(IC<e>{});
          if constexpr (n == 4 && e + 1 < f) ldh(IC<e + 1>{});
        });
      });
      MM_DONE();
      STAMP(36);
      lset(7, mt);
      lwait(6);
      STAMP(12);
      const u32 sh = WS_HID + (pc * 4 + mc) * 54 * 1024, fh = WS_FHID + (((pc * 16 + c) * 2 + (mt & 1)) * 2) * 8;
      const u2 s0v = lds64(L_CB + 2048 + mt * 256 + lr * 8), s1v = lds64(L_CB + 2048 + mt * 256 + 128 + lr * 8);
      const float sc[2] = {asf(s0v.x), asf(s1v.x)}, sm[2] = {asf(s0v.y), asf(s1v.y)};
      u32 hp[2][2][2];
      sfor<0, 7>([&](auto N) {
        constexpr int n = N;
        const f4 c4 = __builtin_bit_cast(f4, lds128(L_CB + n * 64 + g * 16)), b4 = __builtin_bit_cast(f4, lds128(L_CB + 512 + n * 64 + g * 16));
        if constexpr (n == 6) { waitv<2>(); setflag(ws, fh, tag, lane); STAMP(15); }
        sfor<0, 2>([&](auto M) {
          pin(ca[M][n]);
#pragma unroll
          for (int i = 0; i < 2; i++) {
            const f2 v = f2{ca[M][n][2 * i], ca[M][n][2 * i + 1]} * sc[M] + (f2{b4[2 * i], b4[2 * i + 1]} - sm[M] * f2{c4[2 * i], c4[2 * i + 1]});
            const f2 u = v * (-2.302208198f - 0.1029432397f * v * v);
            const f2 d = f2{__builtin_amdgcn_exp2f(u[0]), __builtin_amdgcn_exp2f(u[1])} + 1.f;
            hp[M][n & 1][i] = pk2(v * f2{__builtin_amdgcn_rcpf(d[0]), __builtin_amdgcn_rcpf(d[1])});
          }
          if constexpr (n & 1) st128(u4{hp[M][0][0], hp[M][0][1], hp[M][1][0], hp[M][1][1]}, rW, l16, sh + M * 54 * 1024 + ((t0 >> 1) + n / 2) * 1024);
          if constexpr (n == 6) st64(u2{hp[M][0][0], hp[M][0][1]}, rW, rho < 6 ? l8 + odd * 512 : OOB, sh + M * 54 * 1024 + (tl >> 1) * 1024);
        });
      });
      STAMP(29);
      waitv<0>();
      setflag(ws, fh + 8, tag, lane);
      __builtin_amdgcn_s_setprio(0);
      STAMP(13);
      lwait(8);
      const u32 wa = opaque(mt * 35840 + l16);
#pragma unroll
      for (int i = 0; i < 35; i++) w2f[i] = lds128(wa + i * 1024);
      lset(9, mt);
    }
  } else {
    STAMP(20);
    LD_EPOCH();
    u4 sel[2];
    u32 dst[2];
    auto setup = [&]() {
#pragma unroll
    for (int q = 0; q < 2; q++) {
      const u32 k = lr + 16 * q - 8 * g, one = (k & 1) ? 0x3F800000u : 0x00003F80u;
      sel[q] = u4{k < 2 ? one : 0u, (k >> 1) == 1 ? one : 0u, (k >> 1) == 2 ? one : 0u, (k >> 1) == 3 ? one : 0u};
      landed(sel[q]);
    }
#pragma unroll
    for (int h = 0; h < 2; h++) {
      const u32 qd = 2 * (lr + 16 * (2 * h + (g >> 1))) + (g & 1), e = (qd * 205) >> 10, dq = qd - e * 5;
      const u32 gs = dq + mt, grp = (e * 13) >> 6, ii = e - grp * 5;
      const u32 base = L_IMG + mt * 2048 + (gs & 3) * 256 + (gs >> 2) * 8;
      dst[h] = opaque(e < 20 ? base + (ii < 4 ? (4 * grp + ii) * 16 : 1024 + 4 * grp * 16) : (e == 20 ? base + 1024 + 16 : L_CB + 1792));
    }
    };
    if (mt < 3) {
      const rsrc_t rQ = __builtin_amdgcn_make_buffer_rsrc((void*)((const char*)wqkv - 4096), 0, 640 * 3840 + 4096, 0x00020000);
      u32 voffW[7];
#pragma unroll
      for (int i = 0; i < 7; i++) {
        const u32 pi = i < 5 ? i : (i == 5 ? mt : (mt < 2 ? mt + 3 : 4u));
        const u32 f = 64 * pi + lane, sg = (f * 6554) >> 16, ch = f - sg * 10;
        voffW[i] = (((sg & 12) << 1) + ((sg >> 4) << 2) + (sg & 3)) * 3840 + ch * 16 + 4096 - (i < 4 ? i * 1024 : 0);
      }
      const u32 soW = (mt >> 1) * 32 * 3840 + ((mt & 1) + 1) * 1280 + j * 160, soW3 = 32 * 3840 + 2560 + j * 160;
      const u32 soQ = mt * 32 * 3840 + j * 160, soQ3 = 96 * 3840 + j * 160;
      const u32 l5 = 15360 + mt * 1024, l6 = 15360 + (mt < 2 ? mt + 3 : 4u) * 1024;
      u32 st = rot, qs = rotq;
      auto issueW = [&](u32 so, u32 so3, u32 ld) {
        dma<0>(rQ, voffW[0], so, ld + mt * 5120); dma_c<1024>(rQ, voffW[1], so); dma_c<2048>(rQ, voffW[2], so); dma_c<3072>(rQ, voffW[3], so);
        dma<0>(rQ, voffW[4], so, ld + mt * 5120 + 4096); dma<0>(rQ, voffW[5], so3, ld + l5);
        if (mt < 2) dma<0>(rQ, voffW[6], so3, ld + l6);
      };
      auto issueKV = [&](u32 slot) { issueW(soW + st * 245760, soW3 + st * 245760, L_RING + slot * 20480); st = st + 1 >= 10 ? 0 : st + 1; };
      auto issueQ = [&](u32 ld) { issueW(soQ + qs * 491520, soQ3 + qs * 491520, ld); qs = qs + 1 >= 5 ? 0 : qs + 1; };
      issueKV(0); issueKV(1); issueKV(2);
      STAMP(21);
      waitv<12>();
      BAR();
      issueKV(3);
#pragma nounroll
      for (u32 i = 0; i < 6; i++) { waitv<12>(); asm volatile("s_barrier" ::: "memory"); issueKV(i & 3); }
      auto issueQa = [&](u32 slot) {
        const u32 so = soQ + qs * 491520;
        dma<0>(rQ, voffW[0], so, L_RING + slot * 20480 + mt * 5120); dma_c<1024>(rQ, voffW[1], so); dma_c<2048>(rQ, voffW[2], so);
      };
      auto issueQb = [&](u32 slot) {
        const u32 so = soQ + qs * 491520, so3 = soQ3 + qs * 491520, ld = L_RING + slot * 20480;
        dma<3072>(rQ, voffW[3], so, ld + mt * 5120); dma<0>(rQ, voffW[4], so, ld + mt * 5120 + 4096); dma<0>(rQ, voffW[5], so3, ld + l5);
        if (mt < 2) dma<0>(rQ, voffW[6], so3, ld + l6);
        qs = qs + 1 >= 5 ? 0 : qs + 1;
      };
      waitv<12>(); BAR(); issueQa(2);
      waitv<9>(); BAR(); issueQb(2);
      waitv<6>(); BAR(); issueQa(3);
      BAR();
      issueQb(3);
      issueQ(0);
      waitv<6>();
      BAR();
      GET_TAG();
      setup();
      issueQ(20480); issueQ(40960);
    } else {
      const bool m1 = b < 108, mo = b >= 216;
      const u32 v = m1 ? b : (mo ? b - 216 : b - 108), vq = m1 ? (v * 2428) >> 16 : (v * 6554) >> 16;
      const u32 sA = uni((m1 || mo) ? 5 * vq : v >> 1), cA = uni(m1 ? v - 27 * vq : (mo ? v - 10 * vq : 5 * (v & 1))), sQ = uni((m1 || mo) ? 1u : 0u);
      const u32 pitch = uni(m1 ? 3456 : 1280);
      const u64 src = m1 ? (u64)w1 : (mo ? (u64)wo : (u64)w2);
      const rsrc_t rS = __builtin_amdgcn_make_buffer_rsrc((void*)src, 0, mo ? 640 * 1280 : 640 * 3456, 0x00020000);
      u32 vrt[20];
#pragma unroll
      for (int I = 0; I < 20; I++) {
        const u32 q = I >> 2, i = I & 3, ro = 8 * i + (lane >> 3);
        const u32 i40 = 8 * q + 4 * (i >> 1) + (ro & 3), hf = i40 >= 20 ? 1u : 0u, id = i40 - 20 * hf;
        const u32 rp = opaque(80 * hf + 15 * ((id * 13) >> 6) + 5 * ((ro >> 2) & 3) + id);
        vrt[I] = opaque((mo ? rp : 32 * sQ * q + ro) * pitch + (lane & 7) * 16 + (q - sQ * q) * 128);
      }
      u4 ONES = {0x3F803F80u, 0x3F803F80u, 0x3F803F80u, 0x3F803F80u};
      landed(ONES);
      const u32 sc = L_RING + 16384;
      const u32 wa = sc + (lane >> 3) * 128 + (((lane & 7) ^ (((lane >> 4) & 3) << 1)) << 4);
      const u32 r0 = 4 * g + (lr >> 2), ta = opaque(sc + r0 * 128 + (lr & 1) * 8 + ((((lr >> 1) & 1) ^ (((r0 >> 1) & 3) << 1)) << 4));
      const u32 dT = uni(m1 ? 20480u : (mo ? 10240u : 55296u)), dQ = uni(sQ ? 1024u : 4 * 55296u);
      const u32 dA = uni((m1 ? WS_W1T : (mo ? WS_WOT + (sA >= 10 ? 40960u : 0u) : WS_W2T)) + 4 * cA * dT + sA * 1024);
      u32 dJ[4];
#pragma unroll
      for (int n = 0; n < 4; n++) dJ[n] = uni(dA + n * dT + (mo ? (((4 * cA + n) * 52) >> 8) * 51200u : 0u));
      waitv<0>();
      GET_TAG();
      setup();
      STAMP(21);
#pragma nounroll
      for (u32 i = 0; i < 8; i++) BAR();
      u4 f[5][4];
      sfor<0, 7>([&](auto I) { ldp<0>(f[I >> 2][I & 3], rS, vrt[I], 32 * sA * pitch + cA * 128); });
      BAR();
      sfor<7, 14>([&](auto I) { ldp<0>(f[I >> 2][I & 3], rS, vrt[I], 32 * sA * pitch + cA * 128); });
      BAR();
      sfor<14, 20>([&](auto I) { ldp<0>(f[I >> 2][I & 3], rS, vrt[I], 32 * sA * pitch + cA * 128); });
      BAR();
      f4 cs[4];
      sfor<0, 5>([&](auto Q) {
        constexpr int q = Q;
        waitv<16 - 4 * q>();
#pragma unroll
        for (int i = 0; i < 4; i++) { landed(f[q][i]); sts128(wa + i * 1024, f[q][i]); }
        asm volatile("" ::: "memory");
#pragma unroll
        for (int n = 0; n < 4; n++) f[q][n] = ldstr(ta ^ (n << 5), (ta ^ (n << 5)) + 2048);
        asm volatile("" ::: "memory");
#pragma unroll
        for (int n = 0; n < 4; n++) cs[n] = mfma(f[q][n], ONES, q == 0 ? f4{0, 0, 0, 0} : cs[n]);
      });
      STAMP(22);
#pragma unroll
      for (int q = 0; q < 5; q++) {
        if (q == RTE) { BAR(); if (E2) BAR(); }
#pragma unroll
        for (int n = 0; n < 4; n++) st128(f[q][n], rW, l16, dJ[n] + q * dQ);
      }
#pragma unroll
      for (int n = 0; n < 4; n++) st128(__builtin_bit_cast(u4, cs[n]), rW, (m1 && lr == 0) ? g * 16 : OOB, WS_CS1 + vq * 6912 + (4 * cA + n) * 64);
      if (RTE == 5) { BAR(); if (E2) BAR(); }
    }
    STAMP(24);
    if (mt < 3) waitv<0>();
    BAR();
    if (mt == 3) { waitv<0>(); setflag(ws, WS_FRT + b * 8, tag, lane); }
    STAMP(4);
    PP pp;
    poll2(pp, rW, WS_FKV + ((j * 4 + mt) * 32 + (lane & 31)) * 8, tag);
    STAMP(5);
    {
      const u32 so = WS_KVP + (j * 32 * 4 + mt) * 896;
      const u32 v0 = lane < 53 ? l16 : OOB;
      u4 a[32], fr[2];
      auto lda = [&](auto Q) { ldv<(Q & 1) * 3584>(a[Q], rW, v0, so + (Q & ~1) * 3584); };
      sfor<0, 14>(lda);
      sts128(L_IMG + mt * 2048 + l16, u4{0, 0, 0, 0}); sts128(L_IMG + mt * 2048 + 1024 + l16, u4{0, 0, 0, 0});
      const bool zrow = (dst[1] & 0x430) == 0x410 && dst[1] < L_CB;
      f4 s0, s1, s2, s3;
      sfor<0, 16>([&](auto Q) {
        waitv<(Q < 9 ? 12 : 32 - 2 * Q)>();
        if constexpr (Q == 0) pollend(pp);
        if constexpr (Q == 0) { mm0(s0, sel[0], a[0]); mm0(s1, sel[1], a[0]); mm0(s2, sel[0], a[1]); mm0(s3, sel[1], a[1]); }
        else { mm(s0, sel[0], a[2 * Q]); mm(s1, sel[1], a[2 * Q]); mm(s2, sel[0], a[2 * Q + 1]); mm(s3, sel[1], a[2 * Q + 1]); }
        if constexpr (Q < 9) { lda(IC<14 + 2 * Q>{}); lda(IC<15 + 2 * Q>{}); }
        if constexpr (Q == 8) { ldv<0>(fr[0], rW, lane * 32, WS_FRT); ldv<16>(fr[1], rW, lane * 32, WS_FRT); }
      });
      MM_DONE();
      pin(s0); pin(s1); pin(s2); pin(s3);
      s0 += s2; s1 += s3;
      const u2 v1 = {pk2(s1[0], s1[1]), pk2(s1[2], s1[3])};
      sts64(dst[0], u2{pk2(s0[0], s0[1]), pk2(s0[2], s0[3])});
      sts64(dst[1], v1);
      if (zrow) { sts64(dst[1] + 64, v1); sts64(dst[1] + 128, v1); sts64(dst[1] + 192, v1); }
      STAMP(6);
      BAR();
      RT_CHECK();
    }
    auto issueW1 = [&](u32 it, u32 n, u32 ld) {
      const u32 so = WS_W1T + (n < 6 ? t0 + n : tl) * 20480 + it * 5120;
      dma4_sc1(rW, l16, so, ld + n * 5120); dma1_sc1(rW, l16, so + 4096, ld + n * 5120 + 4096);
    };
    const u32 o = 51200 + mt * 12288, o2 = 51200 + 49152 + (mt & 1) * 1024, sw = WS_WOT + j * 102400;
    {
      const u32 p0 = mt * 12288, p2 = 49152 + (mt & 1) * 1024;
      dma4_sc1(rW, l16, sw + p0, L_WO + p0); dma4_sc1(rW, l16, sw + p0 + 4096, L_WO + p0 + 4096); dma4_sc1(rW, l16, sw + p0 + 8192, L_WO + p0 + 8192);
      dma1_sc1(rW, l16, sw + p2, L_WO + p2);
    }
    if (b == 0 && tid == 256) __hip_atomic_store((GAS u64*)ws, tag, __ATOMIC_RELAXED, __HIP_MEMORY_SCOPE_AGENT);
    {
      f4 csum;
      u32 ci = lane + 64 * (mt & 1); ci = ci > 111 ? 111 : ci;
      const u32 n = ci >> 4, col = (n < 6 ? t0 + n : tl) * 16 + (ci & 15);
#pragma unroll
      for (int q = 0; q < 4; q++) csum[q] = asf(__builtin_amdgcn_raw_buffer_load_b32(rW, col * 4, WS_CS1 + q * 6912, SC1));
      const u32 bv = b1[col], b2v = ((const u32*)b2)[40 * jD + (lane < 40 ? lane : 0)];
      sts32(L_CB + ci * 4, (csum[0] + csum[1]) + (csum[2] + csum[3]));
      sts32(L_CB + 512 + ci * 4, blo(bv));
      if (lane < 40) *(LAS u32*)(L_CB + 1024 + lane * 4) = b2v;
    }
    waitv<0>();
    BAR();
    STAMP(9);
    auto w1t = [&](u32 it, u32 n, u32 ld) { waitv<TH>(); issueW1(it, n, ld); };
    auto w2t = [&](auto I) { waitv<TH + 3>(); ldw2(I); };
    dma4_sc1(rW, l16, sw + o, L_WO + o); waitv<TH>(); dma4_sc1(rW, l16, sw + o + 4096, L_WO + o + 4096);
    waitv<TH>(); dma4_sc1(rW, l16, sw + o + 8192, L_WO + o + 8192); dma1_sc1(rW, l16, sw + o2, L_WO + o2);
    w1t(0, mt, L_W1A);
    waitv<5>(); lset(2, mt);
    STAMP(10);
    w1t(0, mt < 3 ? mt + 4 : 6, L_W1A);
    sfor<0, HA>(w2t);
    lwait(1);
    STAMP(7);
    w1t(1, mt, L_W1B); w1t(1, mt < 3 ? mt + 4 : 6, L_W1B);
    sfor<HA, HA + 4>(w2t);
    waitv<4>(); lset(4, mt);
    STAMP(25);
    sfor<HA + 4, HB2>(w2t);
    lwait(3);
    STAMP(8);
#pragma nounroll
    for (u32 u = mt; u < 14; u += 4) { const u32 it = u >= 7 ? 1u : 0u; w1t(it + 2, u - 7 * it, L_W1B + 35840 + it * 35840); }
    waitv<0>(); lset(5, mt);
    STAMP(28);
    {
      const u32 pc = 2 * P + (mt >> 1), mc = 2 * (mt & 1);
      poll(ws, WS_FH + ((pc * 4 + mc) * 8 + lr) * 8, tag);
#pragma unroll
      for (int m = 0; m < 2; m++) {
        const u2 v0 = __builtin_amdgcn_raw_buffer_load_b64(rW, g * 256 + lr * 8, WS_ST + (pc * 4 + mc + m) * 1024, SC1);
        const u2 v1 = __builtin_amdgcn_raw_buffer_load_b64(rW, g * 256 + lr * 8 + 128, WS_ST + (pc * 4 + mc + m) * 1024, SC1);
        const float mean = xsum16_32(asf(v0.x) + asf(v1.x)) * (1.f / 640.f);
        const float var = fmaxf(xsum16_32(asf(v0.y) + asf(v1.y)) * (1.f / 640.f) - mean * mean, 0.f);
        const float m1 = rbf(mean), sd1 = rbf(__builtin_amdgcn_sqrtf(rbf(rbf(var) + 1e-5f))), r1 = __builtin_amdgcn_rcpf(sd1);
        const float m2 = rbf((mean - m1) * r1), sd2 = rbf(__builtin_amdgcn_sqrtf(rbf(rbf(var * r1 * r1) + 1e-5f)));
        const float sc = r1 * __builtin_amdgcn_rcpf(sd2);
        sts64(L_CB + 2048 + mt * 256 + m * 128 + lr * 8, u2{asu(sc), asu(sc * (m1 + m2 * sd1))});
      }
    }
    lset(6, mt);
    STAMP(27);
    sfor<HB2, 35>(w2t);
    STAMP(26);
    lwait(7);
    {
      const u32 so = WS_W2T + (jD * 5 * 54 + 7 * mt) * 1024, ld = mt * 35840;
#pragma nounroll
      for (u32 n = 0; n < 5; n++) {
        waitv<TH2>();
        sfor<0, 7>([&](auto S) {
          constexpr int k = S < 2 ? S : (S < 4 ? S + 2 : (S < 6 ? S - 2 : 6));
          dma1_sc1(rW, l16, so + (n * 54 + k) * 1024, ld + (S * 5 + n) * 1024);
        });
      }
    }
    waitv<0>();
    lset(8, mt);
    STAMP(30);
  }
  {
    f4 da[4][5];
    u4 xf[7][4];
    u2 xh[4][2], hr[3];
    const u32 so = WS_HID + (pD * 4 * 54 + s0) * 1024;
    const u32 vh0 = w < 6 ? l8 : l16 + 2048, vh1 = w < 6 ? l8 + 512 : l16 + 2056;
    auto ldx = [&](auto U) {
      constexpr int s = U / 4, a = U % 4;
      if constexpr (s < 2) ldv<s * 1024>(xf[s][a], rW, l16, so + a * 54 * 1024);
      else if constexpr (s < 4) ldv<(s - 2) * 1024>(xf[s][a], rW, vmid, so + (a * 54 + 3) * 1024);
      else if constexpr (s == 4) ldv<2048>(xf[s][a], rW, l16, so + a * 54 * 1024);
      else if constexpr (s == 5) { ldv2<0>(xh[a][0], rW, vh0, so + (a * 54 + 3) * 1024); ldv2<0>(xh[a][1], rW, vh1, so + (a * 54 + 3) * 1024); }
      else ldv<0>(xf[s][a], rW, vlast, so + (a * 54 + 3) * 1024);
    };
    const u32 fo = WS_FHID + ((pD * 16 + 2 * w) * 2 + (lane & 3)) * 16;
    const u32 rw = opaque(L_RED + (w >> 1) * 1024 + l16 + (w & 1) * 8);
    auto put = [&](auto A, auto N) {
      pin(da[A][N]);
      sts64(rw + (A * 5 + N) * 4096, u2{pk2(da[A][N][0], da[A][N][1]), pk2(da[A][N][2], da[A][N][3])});
    };
    {
      constexpr int XD = 6;
      PP pp;
      poll2(pp, rW, fo, tag);
      STAMP(14);
      sfor<0, XD>(ldx);
      sfor<0, 16>([&](auto U) {
        constexpr int u = U, s = u / 4, a = u % 4;
        constexpr int e0 = u + XD + (u >= 4 ? 4 : 0), e = e0 < 16 ? e0 : 16;
        constexpr int f0 = u + 1 + XD + (u + 1 >= 4 ? 4 : 0), f = f0 < 16 ? f0 : 16;
        waitv<e - u - 1>();
        if constexpr (u == 0) pollend(pp);
        sfor<0, 5>([&](auto N) { if constexpr (s == 0) mm0(da[a][N], w2f[N], xf[s][a]); else mm(da[a][N], w2f[5 * s + N], xf[s][a]); });
        sfor<e, f>(ldx);
      });
    }
    {
      PP pp;
      poll2(pp, rW, fo + 8, tag);
      STAMP(16);
      sfor<0, 4>([&](auto A) { ldx(IC<16 + A>{}); ldx(IC<20 + A>{}); ldx(IC<24 + A>{}); });
      constexpr int HX = 0;
      lwait(9);
      sfor<0, 4>([&](auto A) {
        constexpr int a = A;
        waitv<4 * (3 - a) + 3 + HX>();
        if constexpr (a == 0) pollend(pp);
        sfor<0, 5>([&](auto N) { mm(da[a][N], w2f[20 + N], xf[4][a]); });
        waitv<4 * (3 - a) + 1 + HX>();
        sfor<0, 5>([&](auto N) {
          mmh(da[a][N], w2f[25 + N], xh[a][0], xh[a][1]);
          if constexpr (a > 0 && N < 3) { put(IC<a - 1>{}, N); __builtin_amdgcn_sched_barrier(0); }
        });
        waitv<4 * (3 - a) + HX>();
        sfor<0, 5>([&](auto N) {
          mm(da[a][N], w2f[30 + N], xf[6][a]);
          if constexpr (a > 0 && N < 2) { put(IC<a - 1>{}, IC<3 + N>{}); __builtin_amdgcn_sched_barrier(0); }
        });
      });
    }
    MM_DONE();
    STAMP(17);
    sfor<0, 5>([&](auto N) { put(IC<3>{}, N); });
    const rsrc_t rOut = __builtin_amdgcn_make_buffer_rsrc((void*)out, 0, 2048 * 1280, 0x00020000);
    const u32 LO = opaque(0x00003F80u), HI = opaque(0x3F800000u), n0 = 3 * nh;
    const u32 ra = opaque(L_RED + (mt * 5 + n0) * 4096 + l16);
    u2 bb[3];
#pragma unroll
    for (int i = 0; i < 3; i++) {
      bb[i] = lds64(L_CB + 1024 + (n0 + i) * 32 + g * 8);
      hr[i] = lds64(L_HR + (mt * 5 + n0 + i) * 512 + l8);
    }
    BAR();
    STAMP(18);
    u4 pv[3][4];
#pragma unroll
    for (int i = 0; i < 3; i++)
#pragma unroll
      for (int q = 0; q < 4; q++) pv[i][q] = lds128(ra + i * 4096 + q * 1024);
#pragma unroll
    for (int i = 0; i < 3; i++) {
      float s0 = 0.f, s1 = 0.f, s2 = 0.f, s3 = 0.f;
#pragma unroll
      for (int q = 0; q < 4; q++) {
        s0 = dot2c(pv[i][q].x, LO, s0); s1 = dot2c(pv[i][q].x, HI, s1); s2 = dot2c(pv[i][q].y, LO, s2); s3 = dot2c(pv[i][q].y, HI, s3);
        s0 = dot2c(pv[i][q].z, LO, s0); s1 = dot2c(pv[i][q].z, HI, s1); s2 = dot2c(pv[i][q].w, LO, s2); s3 = dot2c(pv[i][q].w, HI, s3);
      }
      const u32 o0 = pk2(unp(hr[i].x) + unp(pk2(f2{s0, s1} + unp(bb[i].x)))), o1 = pk2(unp(hr[i].y) + unp(pk2(f2{s2, s3} + unp(bb[i].y))));
      __builtin_amdgcn_raw_buffer_store_b64(u2{o0, o1}, rOut, (i == 2 && helper) ? OOB : lr * 1280 + g * 8 + i * 32, (pD * 64 + mt * 16) * 1280 + jD * 160 + n0 * 32, 0);
    }
  }
  waitv<0>();
  STAMP(19);
  if (b == 77 && w == PW) {
    u32 d = __builtin_amdgcn_readlane(tsv, PROBE) - __builtin_amdgcn_readlane(tsv, PROBE0);
    u64 t0 = __builtin_amdgcn_s_memrealtime();
    while (__builtin_amdgcn_s_memrealtime() - t0 < 50ull * d) __builtin_amdgcn_s_sleep(2);
  }
}

extern "C" void kernel_launch(void* const* d_in, const int* in_sizes, int n_in, void* d_out, int out_size, void* d_ws, size_t ws_size,
                              hipStream_t stream) {
  fused_block<<<256, 512, 0, stream>>>((const u16*)d_in[0], (const u16*)d_in[1], (const u16*)d_in[2], (const u16*)d_in[4], (const u16*)d_in[5],
                                       (const u16*)d_in[6], (const u16*)d_in[7], (u16*)d_out, (char*)d_ws);
}
```

```cpp
#include <hip/hip_runtime.h>
#include <stdint.h>
#pragma clang diagnostic ignored "-Wint-to-pointer-cast"
#pragma clang diagnostic ignored "-Warray-bounds"

typedef unsigned short u16;
typedef unsigned u32;
typedef unsigned long long u64;
typedef float f2 __attribute__((ext_vector_type(2)));
typedef float f4 __attribute__((ext_vector_type(4)));
typedef unsigned u2 __attribute__((ext_vector_type(2)));
typedef unsigned u4 __attribute__((ext_vector_type(4)));
typedef short s4 __attribute__((ext_vector_type(4)));
typedef __bf16 bf2 __attribute__((ext_vector_type(2)));
typedef __bf16 bf8 __attribute__((ext_vector_type(8)));
typedef __amdgpu_buffer_rsrc_t rsrc_t;
#define LAS __attribute__((address_space(3)))
#define GAS __attribute__((address_space(1)))
#define DI __device__ __forceinline__

template <int I> struct IC { static constexpr int v = I; constexpr operator int() const { return I; } };
template <int B, int E, class F> DI void sfor(F&& f) {
  if constexpr (B < E) { f(IC<B>{}); sfor<B + 1, E>(f); }
}

constexpr u32 WS_FRT = 4096, WS_FKV = 8192, WS_FO = 16384, WS_FH = 24576, WS_FHID = 32768;
constexpr u32 WS_KVP = 65536;
constexpr u32 WS_OB = 1u << 20;
constexpr u32 WS_HB = WS_OB + 32 * 81920;
constexpr u32 WS_ST = WS_HB + 32 * 81920;
constexpr u32 WS_CS1 = WS_ST + 2048 * 64;
constexpr u32 WS_WOT = WS_CS1 + 32768;
constexpr u32 WS_W1T = WS_WOT + 40 * 20480;
constexpr u32 WS_W2T = WS_W1T + 108 * 20480;
constexpr u32 WS_HID = WS_W2T + 8 * 54 * 5120;
constexpr u32 WS_END = WS_HID + 32 * 4 * 54 * 1024;
constexpr u32 OOB = 0x7FFFFF00u;
constexpr int SC1 = 16;
constexpr int TH = 4, TH2 = 6, TQ = 2, HA = 10, HB2 = 14, OP = 6, HDV = 8, PS = 4, DLP = 7, DX0 = 6, DB = 7, DG = 5;

constexpr u32 L_X = 0, L_RING = 81920;
constexpr u32 L_WO = 0, L_IMG = 147456, L_W1A = 110592, L_HR = 147456, L_CB = 160768, L_W1B = 0, L_RED = 0;

DI f4 mfma(u4 a, u4 b, f4 c) {
  return __builtin_amdgcn_mfma_f32_16x16x32_bf16(__builtin_bit_cast(bf8, a), __builtin_bit_cast(bf8, b), c, 0, 0, 0);
}
DI void mm(f4& c, u4 a, u4 b) { asm volatile("v_mfma_f32_16x16x32_bf16 %0, %1, %2, %0" : "+v"(c) : "v"(a), "v"(b) : "memory"); }
DI void mm0(f4& c, u4 a, u4 b) { asm volatile("v_mfma_f32_16x16x32_bf16 %0, %1, %2, 0" : "=&v"(c) : "v"(a), "v"(b) : "memory"); }
#define MM_DONE() asm volatile("s_nop 7\n\ts_nop 1" ::: "memory")
DI void pin(f4& c) { asm volatile("" : "+v"(c)); }
DI void zero(f4& c) { c = f4{0, 0, 0, 0}; asm volatile("" : "+v"(c)); }
DI void undef(u4& d) { asm volatile("" : "=v"(d)); }
DI u32 uni(u32 v) { v = __builtin_amdgcn_readfirstlane(v); asm volatile("" : "+s"(v)); return v; }
DI u32 opaque(u32 v) { asm volatile("" : "+v"(v)); return v; }

DI u32 pk2(float a, float b) { f2 v = {a, b}; return __builtin_bit_cast(u32, __builtin_convertvector(v, bf2)); }
DI u32 pk2(f2 v) { return __builtin_bit_cast(u32, __builtin_convertvector(v, bf2)); }
DI float rbf(float a) { return __builtin_bit_cast(float, pk2(a, a) << 16); }
DI float blo(u32 v) { return __builtin_bit_cast(float, v << 16); }
DI float bhi(u32 v) { return __builtin_bit_cast(float, v & 0xFFFF0000u); }
DI f2 unp(u32 v) { return f2{blo(v), bhi(v)}; }
DI float asf(u32 v) { return __builtin_bit_cast(float, v); }
DI u32 asu(float v) { return __builtin_bit_cast(u32, v); }

template <int OFF> DI void dma(rsrc_t r, u32 voff, u32 soff, u32 lds) {
  asm volatile("s_mov_b32 m0, %0\n\ts_nop 0\n\tbuffer_load_dwordx4 %1, %2, %3 offen offset:%4 lds" ::"s"(lds), "v"(voff), "s"(r), "s"(soff), "n"(OFF) : "memory");
}
template <int OFF> DI void dma_c(rsrc_t r, u32 voff, u32 soff) {
  asm volatile("buffer_load_dwordx4 %0, %1, %2 offen offset:%3 lds" ::"v"(voff), "s"(r), "s"(soff), "n"(OFF) : "memory");
}
DI void dma4_sc1(rsrc_t r, u32 voff, u32 soff, u32 lds) {
  asm volatile("s_mov_b32 m0, %0\n\ts_nop 0\n\tbuffer_load_dwordx4 %1, %2, %3 offen sc1 lds\n\tbuffer_load_dwordx4 %1, %2, %3 offen offset:1024 sc1 lds\n\t"
               "buffer_load_dwordx4 %1, %2, %3 offen offset:2048 sc1 lds\n\tbuffer_load_dwordx4 %1, %2, %3 offen offset:3072 sc1 lds" ::"s"(lds), "v"(voff), "s"(r), "s"(soff) : "memory");
}
DI void dma1_sc1(rsrc_t r, u32 voff, u32 soff, u32 lds) {
  asm volatile("s_mov_b32 m0, %0\n\ts_nop 0\n\tbuffer_load_dwordx4 %1, %2, %3 offen sc1 lds" ::"s"(lds), "v"(voff), "s"(r), "s"(soff) : "memory");
}
template <int OFF> DI void ldv(u4& d, rsrc_t r, u32 voff, u32 soff) {
  asm volatile("buffer_load_dwordx4 %0, %1, %2, %3 offen offset:%4 sc1" : "=v"(d) : "v"(voff), "s"(r), "s"(soff), "n"(OFF) : "memory");
}
template <int OFF> DI void ldp(u4& d, rsrc_t r, u32 voff, u32 soff) {
  asm volatile("buffer_load_dwordx4 %0, %1, %2, %3 offen offset:%4" : "=v"(d) : "v"(voff), "s"(r), "s"(soff), "n"(OFF) : "memory");
}
template <int OFF> DI void ldp2(u2& d, rsrc_t r, u32 voff, u32 soff) {
  asm volatile("buffer_load_dwordx2 %0, %1, %2, %3 offen offset:%4" : "=v"(d) : "v"(voff), "s"(r), "s"(soff), "n"(OFF) : "memory");
}
template <int N> DI void waitv() { asm volatile("s_waitcnt vmcnt(%0)" ::"n"(N) : "memory"); }
template <class T> DI void landed(T& d) { asm volatile("" : "+v"(d)); }
#define BAR() do { asm volatile("" ::: "memory"); __builtin_amdgcn_s_waitcnt(0xC07F); asm volatile("s_barrier" ::: "memory"); } while (0)
DI u4 lds128(u32 a) { return *(LAS u4*)(a); }
DI u2 lds64(u32 a) { return *(LAS u2*)(a); }
DI void sts128(u32 a, u4 v) { *(LAS u4*)(a) = v; }
DI void sts64(u32 a, u2 v) { *(LAS u2*)(a) = v; }
DI void sts32(u32 a, float v) { *(LAS float*)(a) = v; }
DI u4 ldstr(u32 a0, u32 a1) {
  u2 x = __builtin_bit_cast(u2, __builtin_amdgcn_ds_read_tr16_b64_v4i16((LAS s4*)(a0)));
  u2 y = __builtin_bit_cast(u2, __builtin_amdgcn_ds_read_tr16_b64_v4i16((LAS s4*)(a1)));
  return u4{x.x, x.y, y.x, y.y};
}
DI u4 ldstr(u32 a) { return ldstr(a, a + 2560); }
DI float xsum16_32(float v) {
  auto r = __builtin_amdgcn_permlane16_swap(asu(v), asu(v), false, false);
  v = asf((u32)r[0]) + asf((u32)r[1]);
  auto q = __builtin_amdgcn_permlane32_swap(asu(v), asu(v), false, false);
  return asf((u32)q[0]) + asf((u32)q[1]);
}
DI float xsum8(float v) {
  v += asf(__builtin_amdgcn_update_dpp(0, asu(v), 0xB1, 0xf, 0xf, true));
  v += asf(__builtin_amdgcn_update_dpp(0, asu(v), 0x4E, 0xf, 0xf, true));
  return v + asf(__builtin_amdgcn_update_dpp(0, asu(v), 0x141, 0xf, 0xf, true));
}
DI void poll(const char* ws, u32 off, u64 tag) {
  const GAS u64* f = (const GAS u64*)(ws + off);
#pragma nounroll
  for (u32 it = 0; it < (1u << 18); ++it) {
    u64 v = __hip_atomic_load(f, __ATOMIC_RELAXED, __HIP_MEMORY_SCOPE_AGENT);
    if (__all(v == tag)) break;
    __builtin_amdgcn_s_sleep(2);
  }
  __builtin_amdgcn_fence(__ATOMIC_ACQUIRE, "wavefront");
  asm volatile("" ::: "memory");
}
DI void lset_(u32 k, u32 mt, u32 tg) { __builtin_amdgcn_s_waitcnt(0xC07F); *(volatile LAS u32*)(L_CB + 1536 + k * 16 + mt * 4) = tg; asm volatile("" ::: "memory"); }
DI void lwait_(u32 k, u32 tg) {
  asm volatile("" ::: "memory");
  for (;;) {
    const u4 v = *(volatile LAS u4*)(L_CB + 1536 + k * 16);
    if (__builtin_amdgcn_readfirstlane((v.x ^ tg) | (v.y ^ tg) | (v.z ^ tg) | (v.w ^ tg)) == 0) break;
    __builtin_amdgcn_s_sleep(1);
  }
  asm volatile("" ::: "memory");
}
DI u4 lpeek(u32 k) { asm volatile("" ::: "memory"); const u4 v = *(volatile LAS u4*)(L_CB + 1536 + k * 16); asm volatile("" ::: "memory"); return v; }
DI void lcheck(u4 v, u32 k) { if (__builtin_amdgcn_readfirstlane(v.x & v.y & v.z & v.w) == 0) lwait_(k, 1u); asm volatile("" ::: "memory"); }
DI void lwait1(u32 k, u32 mt) {
  asm volatile("" ::: "memory");
  while (__builtin_amdgcn_readfirstlane(*(volatile LAS u32*)(L_CB + 1536 + k * 16 + mt * 4)) == 0) __builtin_amdgcn_s_sleep(1);
  asm volatile("" ::: "memory");
}
#define lset(k, mt) lset_(k, mt, 1u)
#define lwait(k) lwait_(k, 1u)
struct PP { u2 a, b; };
DI void ldf(u2& d, rsrc_t r, u32 voff) { asm volatile("buffer_load_dwordx2 %0, %1, %2, 0 offen sc1" : "+v"(d) : "v"(voff), "s"(r) : "memory"); }
DI void poll2(PP& pp, rsrc_t r, u32 voff, u64 tag) {
  const u32 tl = (u32)tag, th = (u32)(tag >> 32);
  asm volatile("" : "=v"(pp.a), "=v"(pp.b));
  ldf(pp.a, r, voff);
  __builtin_amdgcn_s_sleep(PS);
#pragma nounroll
  for (u32 it = 0; it < (1u << 18); ++it) {
    ldf(pp.b, r, voff);
    waitv<1>(); landed(pp.a);
    if (!__any(((pp.a.x ^ tl) | (pp.a.y ^ th)) != 0)) break;
    ldf(pp.a, r, voff);
    waitv<1>(); landed(pp.b);
    if (!__any(((pp.b.x ^ tl) | (pp.b.y ^ th)) != 0)) break;
  }
  asm volatile("" ::: "memory");
}
DI void pollend(PP& pp) { asm volatile("" ::"v"(pp.a), "v"(pp.b)); }
DI void setflag(char* ws, u32 off, u64 tag, u32 lane) {
  if (lane == 0) __hip_atomic_store((GAS u64*)(ws + off), tag, __ATOMIC_RELAXED, __HIP_MEMORY_SCOPE_AGENT);
  asm volatile("" ::: "memory");
}
template <bool PHI> DI u32 phi2(float a0, float a1, float r0, float r1) {
  const f2 a = {a0, a1}, r = {r0, r1};
  if constexpr (!PHI) return pk2(a * r);
  const f2 t = a * r + 1e-6f, u = a * (r * 1.4426950408889634f);
  return pk2(fmaxf(t[0], 1e-6f) + __builtin_amdgcn_fmed3f(__builtin_amdgcn_exp2f(u[0]), 0.f, 1.f),
             fmaxf(t[1], 1e-6f) + __builtin_amdgcn_fmed3f(__builtin_amdgcn_exp2f(u[1]), 0.f, 1.f));
}
DI float dot2c(u32 a, u32 b, float c) { return __builtin_amdgcn_fdot2_f32_bf16(__builtin_bit_cast(bf2, a), __builtin_bit_cast(bf2, b), c, false); }
DI u4 center8(u4 x, u32 lo, u32 hi, float nm) {
  u4 r; float t0, t1, t2, t3, t4, t5, t6, t7;
  asm("v_dot2_f32_bf16 %4, %12, %16, %18\n\tv_dot2_f32_bf16 %5, %12, %17, %18\n\tv_dot2_f32_bf16 %6, %13, %16, %18\n\tv_dot2_f32_bf16 %7, %13, %17, %18\n\t"
      "v_dot2_f32_bf16 %8, %14, %16, %18\n\tv_dot2_f32_bf16 %9, %14, %17, %18\n\tv_dot2_f32_bf16 %10, %15, %16, %18\n\tv_dot2_f32_bf16 %11, %15, %17, %18\n\t"
      "v_cvt_pk_bf16_f32 %0, %4, %5\n\tv_cvt_pk_bf16_f32 %1, %6, %7\n\tv_cvt_pk_bf16_f32 %2, %8, %9\n\tv_cvt_pk_bf16_f32 %3, %10, %11"
      : "=v"(r.x), "=v"(r.y), "=v"(r.z), "=v"(r.w), "=&v"(t0), "=&v"(t1), "=&v"(t2), "=&v"(t3), "=&v"(t4), "=&v"(t5), "=&v"(t6), "=&v"(t7)
      : "v"(x.x), "v"(x.y), "v"(x.z), "v"(x.w), "v"(lo), "v"(hi), "v"(nm));
  return r;
}
template <int N> DI void waitl() { asm volatile("" ::: "memory"); __builtin_amdgcn_s_waitcnt(0xC07F | (N << 8)); asm volatile("" ::: "memory"); }
DI void st128(u4 v, rsrc_t r, u32 voff, u32 soff) {
  __builtin_amdgcn_raw_buffer_store_b128(v, r, voff, soff, SC1);
  asm volatile("s_nop 1" ::"v"(v) : "memory");
}
DI void st64(u2 v, rsrc_t r, u32 voff, u32 soff) { __builtin_amdgcn_raw_buffer_store_b64(v, r, voff, soff, SC1); }
DI void publish20(const u32* v, rsrc_t r, u32 l16, u32 so, u32 j) {
  if ((j & 1) == 0) {
    st128(u4{v[0], v[1], v[2], v[3]}, r, l16, so);
    st128(u4{v[4], v[5], v[6], v[7]}, r, l16, so + 1024);
    st64(u2{v[8], v[9]}, r, l16 >> 1, so + 2048);
  } else {
    st64(u2{v[0], v[1]}, r, l16 >> 1, so + 2560);
    st128(u4{v[2], v[3], v[4], v[5]}, r, l16, so + 3072);
    st128(u4{v[6], v[7], v[8], v[9]}, r, l16, so + 4096);
  }
}
template <int OFF> DI void ldv2(u2& d, rsrc_t r, u32 voff, u32 soff) {
  asm volatile("buffer_load_dwordx2 %0, %1, %2, %3 offen offset:%4 sc1" : "=v"(d) : "v"(voff), "s"(r), "s"(soff), "n"(OFF) : "memory");
}
DI void mmh(f4& c, u4 a, u2 b0, u2 b1) { mm(c, a, u4{b0.x, b0.y, b1.x, b1.y}); }
template <int V, int L> DI void waitvl() {
  asm volatile("" ::: "memory"); __builtin_amdgcn_s_waitcnt(0x0070 | (V & 15) | ((V >> 4) << 14) | (L << 8)); asm volatile("" ::: "memory");
}

constexpr int d_issued(int u) {
  int k = DG * (u / 4 < 4 ? u / 4 : 4) - DB, e = u + DX0 + (k > 0 ? k : 0);
  if (u <= DLP && e > 16) e = 16;
  return e < 28 ? e : 28;
}
constexpr int d_younger(int u) {
  int n = 0;
  for (int v = u + 1; v < d_issued(u); v++) n += (v >= 16 && (v - 16) % 3 == 1) ? 2 : 1;
  return n;
}

__global__ __launch_bounds__(512, 2) void fused_block(const u16* __restrict__ x, const u16* __restrict__ wqkv, const u16* __restrict__ wo,
                                                      const u16* __restrict__ w1, const u16* __restrict__ b1, const u16* __restrict__ w2,
                                                      const u16* __restrict__ b2, u16* __restrict__ out, char* __restrict__ ws) {
  __shared__ __attribute__((aligned(1024))) char smem[163840];
  asm volatile("" ::"v"((u32)(uintptr_t)(LAS char*)smem));
  const u32 tid = threadIdx.x, lane = tid & 63, lr = lane & 15, g = lane >> 4;
  const u32 w = __builtin_amdgcn_readfirstlane(tid >> 6), mt = w & 3, nh = w >> 2;
  const bool helper = nh != 0;
  const rsrc_t rX = __builtin_amdgcn_make_buffer_rsrc((void*)x, 0, 2048 * 1280, 0x00020000);
  u4 xr[2][10];
  if (!helper) {
    const u32 p_ = ((blockIdx.x & 6) << 2) + (blockIdx.x >> 5);
    const u32 vx = (lane >> 3) * 1280 + (lane & 7) * 16, sx = (p_ * 64 + 16 * mt) * 1280;
    sfor<0, 10>([&](auto I) { ldp<I * 128>(xr[0][I], rX, vx, sx); });
    sfor<0, 10>([&](auto I) { ldp<I * 128>(xr[1][I], rX, vx, sx + 8 * 1280); });
  } else {
    sfor<0, 10>([&](auto I) { undef(xr[0][I]); undef(xr[1][I]); });
  }
  u32 b = blockIdx.x;
  asm volatile("" : "+s"(b) : "s"(wqkv), "s"(wo), "s"(w1), "s"(b1), "s"(w2), "s"(b2), "s"(out), "s"(ws) : "memory");
  const u32 xg = b & 7, rr = b >> 3, p = 8 * (xg >> 1) + (rr >> 2), j = 4 * (xg & 1) + (rr & 3);
  const rsrc_t rW = __builtin_amdgcn_make_buffer_rsrc((void*)ws, 0, WS_END, 0x00020000);
  const u32 l16 = lane * 16, l8 = lane * 8;
  u2 ep;
  u64 tag;
#define LD_EPOCH() asm volatile("buffer_load_dwordx2 %0, off, %1, 0 sc1" : "=v"(ep) : "s"(rW) : "memory")
#define GET_TAG() do { u32 lo_, hi_; asm volatile("v_readfirstlane_b32 %0, %2\n\tv_readfirstlane_b32 %1, %3\n\ts_nop 3" : "=s"(lo_), "=s"(hi_) : "v"(ep.x), "v"(ep.y) : "memory"); \
    tag = (((u64)hi_ << 32) | lo_) + 1; } while (0)
  u32 rot = xg + 3 * (p & 3), rotq = xg + (p & 3);
  rot = rot >= 10 ? rot - 10 : rot; rotq = rotq >= 10 ? rotq - 10 : (rotq >= 5 ? rotq - 5 : rotq);
  const u32 P = 4 * (xg >> 1) + (rr >> 3), c = 8 * (xg & 1) + (rr & 7), rho = c >> 1, odd = c & 1;
  const u32 t0 = rho < 6 ? 14 * rho + 8 * odd : 12 * rho + 12 + 6 * odd, tl = rho < 6 ? 14 * rho + 6 + odd : t0;
  const u32 s0 = w < 6 ? 7 * w : 6 * w + 6;
  const u32 vlast = w >= 6 ? OOB : l16 + 3072, vmid = w >= 6 ? l16 : l16 + 1024, vsp = w >= 6 ? l16 + 2048 : l16, soW2 = WS_W2T + (j * 5 * 54 + s0) * 1024;
  u4 w2f[35];
  auto ldw2 = [&](auto I) {
    constexpr int s = I / 5, n = I % 5;
    if constexpr (s < 2) ldv<s * 1024>(w2f[I], rW, l16, soW2 + n * 54 * 1024);
    else if constexpr (s < 4) ldv<(s - 2) * 1024>(w2f[I], rW, vmid, soW2 + (n * 54 + 3) * 1024);
    else if constexpr (s == 4) ldv<2048>(w2f[I], rW, l16, soW2 + n * 54 * 1024);
    else ldv<0>(w2f[I], rW, s == 5 ? vsp : vlast, soW2 + (n * 54 + 3) * 1024);
  };
#define RT_CHECK() do { waitv<0>(); landed(fr[0]); landed(fr[1]); const u32 tl32 = (u32)tag, th32 = (u32)(tag >> 32); \
    const u32 bad = ((fr[0].x ^ tl32) | (fr[0].z ^ tl32) | (fr[1].x ^ tl32) | (fr[1].z ^ tl32)) | ((fr[0].y ^ th32) | (fr[0].w ^ th32) | (fr[1].y ^ th32) | (fr[1].w ^ th32)); \
    if (__any(bad != 0)) { _Pragma("nounroll") for (u32 q = 0; q < 4; q++) poll(ws, WS_FRT + (64 * q + lane) * 8, tag); } } while (0)
  constexpr u32 L_RS = L_RING + 4096;

  if (!helper) {
    float rsd[2];
    const u32 ONE2 = opaque(0x3F803F80u);
    float dm[2];
    LD_EPOCH();
    const u32 xa0 = opaque(L_X + (lr >> 3) * 1024 + (lr & 7) * 128 + ((g ^ (lr & 7)) << 4));
    const u32 tr0 = opaque(L_RING + (4 * g + (lr >> 2)) * 160 + (lr & 3) * 8);
    const u32 mh = mt & 1, nq = mt >> 1;
    float qrs;
    u4 xq[20];
    {
      f4 acc[2][5];
#pragma unroll
      for (int i = 0; i < 5; i++) { zero(acc[0][i]); zero(acc[1][i]); }
      const u32 bA = opaque(tr0 + nq * 96), bC = opaque(tr0 + 64 + nq * 5120);
      const u32 xs0 = opaque(xa0 + mh * 4096);
      u32 lkv[6];
#pragma unroll
      for (int q = 0; q < 2; q++) {
        const u32 dq = 4 * q + g - mt, e0 = lr - 4 * mt, e1 = e0 + 16, sb = L_RING + mt * 5120;
        lkv[3 * q] = opaque(sb + ((dq < 5 && e0 < 20) ? e0 * 40 + dq * 8 : 896));
        lkv[3 * q + 1] = opaque(sb + ((dq < 5 && e1 < 20) ? e1 * 40 + dq * 8 : 896));
        lkv[3 * q + 2] = opaque(sb + ((dq < 5 && lr == 0) ? 800 + dq * 8 : (dq == 0 && lr == 1) ? 840 : 896));
      }
      {
        const u32 wa = opaque(L_X + mt * 2048 + (lane >> 3) * 128 + (((lane & 7) ^ (lane >> 3)) << 4));
        const u32 lo = opaque(0x00003F80u), hi = opaque(0x3F800000u);
        sfor<0, 2>([&](auto G) {
          waitv<(G == 0 ? 11 : 1)>();
          float s0 = 0.f, s1 = 0.f;
#pragma unroll
          for (int i = 0; i < 10; i++) {
            landed(xr[G][i]);
            s0 = dot2c(xr[G][i].x, ONE2, s0); s1 = dot2c(xr[G][i].y, ONE2, s1); s0 = dot2c(xr[G][i].z, ONE2, s0); s1 = dot2c(xr[G][i].w, ONE2, s1);
          }
          const float m = xsum8(s0 + s1) * (1.f / 640.f), nm = -rbf(m);
          dm[G] = m + nm;
#pragma unroll
          for (int i = 0; i < 10; i++) { xr[G][i] = center8(xr[G][i], lo, hi, nm); sts128(wa + G * 1024 + i * 8192, xr[G][i]); }
        });
      }
      auto half = [&](u4* cur, u4* nxt, u32 xa, u32 a, u32 c, auto KS, auto LD) {
        constexpr int o = KS * 10240;
        mm(acc[0][0], cur[5], cur[0]);
        if constexpr (LD) nxt[5] = lds128(xa);
        mm(acc[1][0], cur[6], cur[0]);
        if constexpr (LD) nxt[6] = lds128(xa + 2048);
        mm(acc[0][1], cur[5], cur[1]);
        if constexpr (LD) nxt[0] = ldstr(a + o);
        mm(acc[1][1], cur[6], cur[1]);
        if constexpr (LD) nxt[1] = ldstr(a + o + 32);
        mm(acc[0][2], cur[5], cur[2]);
        if constexpr (LD) nxt[2] = ldstr(c + o);
        mm(acc[1][2], cur[6], cur[2]);
        if constexpr (LD) nxt[3] = ldstr(a + o + 5120);
        mm(acc[0][3], cur[5], cur[3]);
        if constexpr (LD) nxt[4] = ldstr(a + o + 5152);
        mm(acc[1][3], cur[6], cur[3]);
        mm(acc[0][4], cur[5], cur[4]); mm(acc[1][4], cur[6], cur[4]);
      };
      u4 fa[7], fb[7];
      u32 st = rot, xa = xs0 + st * 8192, a = bA, cc = bC;
      BAR();
      fa[5] = lds128(xa); fa[6] = lds128(xa + 2048); fa[0] = ldstr(a); fa[1] = ldstr(a + 32); fa[2] = ldstr(cc);
      fa[3] = ldstr(a + 5120); fa[4] = ldstr(a + 5152);
      sfor<0, 2>([&](auto G) {
        float s0 = 0.f, s1 = 0.f;
#pragma unroll
        for (int i = 0; i < 10; i++) {
          landed(xr[G][i]);
          const u4 v = xr[G][i];
          s0 = dot2c(v.x, v.x, s0); s1 = dot2c(v.y, v.y, s1); s0 = dot2c(v.z, v.z, s0); s1 = dot2c(v.w, v.w, s1);
        }
        const float var = fmaxf(xsum8(s0 + s1) * (1.f / 640.f) - dm[G] * dm[G], 0.f);
        rsd[G] = __builtin_amdgcn_rcpf(rbf(__builtin_amdgcn_sqrtf(rbf(rbf(var) + 1e-5f))));
        landed(rsd[G]);
      });
      waitv<0>();
      GET_TAG();
#pragma nounroll
      for (u32 i = 1; i < 10; i++) {
        waitl<0>();
        half(fa, fb, xa ^ 64, a, cc, IC<1>{}, IC<1>{});
        st = st + 1 >= 10 ? 0 : st + 1;
        xa = opaque(xs0 + st * 8192); a = opaque(bA + (i & 3) * 20480); cc = opaque(bC + (i & 3) * 20480);
        BAR();
        half(fb, fa, xa, a, cc, IC<0>{}, IC<1>{});
      }
      if ((lane & 7) == 0) { sts32(L_RS + mt * 64 + (lane >> 3) * 4, rsd[0]); sts32(L_RS + mt * 64 + 32 + (lane >> 3) * 4, rsd[1]); }
      waitl<0>();
      half(fa, fb, xa ^ 64, a, cc, IC<1>{}, IC<1>{});
      {
        u32 qs = rotq;
#pragma unroll
        for (int q = 0; q < 5; q++) {
          const u32 xb = xa0 + mt * 2048 + qs * 16384, xc = xb ^ 64;
          xq[4 * q] = lds128(xb); xq[4 * q + 1] = lds128(xc); xq[4 * q + 2] = lds128(xb + 8192); xq[4 * q + 3] = lds128(xc + 8192);
          qs = qs + 1 >= 5 ? 0 : qs + 1;
        }
      }
      BAR();
      const f4 r0 = __builtin_bit_cast(f4, lds128(L_RS + mh * 128 + g * 16)), r1 = __builtin_bit_cast(f4, lds128(L_RS + mh * 128 + 64 + g * 16));
      qrs = *(LAS float*)(L_RS + mt * 64 + lr * 4);
      half(fb, fa, 0, 0, 0, IC<0>{}, IC<0>{});
      MM_DONE();
      const u32 EX = L_RING + 20480;
      const u32 ew = EX + mh * 10240 + nq * 3072 + l16;
      auto put = [&](auto N, auto PHI, u32 a) {
        constexpr int n = N;
        pin(acc[0][n]); pin(acc[1][n]);
        sts128(a, u4{phi2<PHI>(acc[0][n][0], acc[0][n][1], r0[0], r0[1]), phi2<PHI>(acc[0][n][2], acc[0][n][3], r0[2], r0[3]),
                     phi2<PHI>(acc[1][n][0], acc[1][n][1], r1[0], r1[1]), phi2<PHI>(acc[1][n][2], acc[1][n][3], r1[2], r1[3])});
      };
      put(IC<0>{}, IC<1>{}, ew); put(IC<1>{}, IC<1>{}, ew + 1024); put(IC<3>{}, IC<0>{}, ew + 5120); put(IC<4>{}, IC<0>{}, ew + 6144);
      if (nq == 0) put(IC<2>{}, IC<1>{}, ew + 2048); else put(IC<2>{}, IC<0>{}, ew + 4096);
      BAR();
      const u32 ex = EX + mt * 1024 + l16;
      u4 kf[2][2], vf[2][2];
#pragma unroll
      for (int m = 0; m < 2; m++) {
        kf[m][0] = lds128(ex + m * 10240); vf[m][0] = lds128(ex + m * 10240 + 5120);
        vf[m][1] = lds128(ex + m * 10240 + 6144); kf[m][1] = lds128(ex + m * 10240 + 1024);
      }
      const u32 o0 = lr == 0 ? ONE2 : 0u;
      const u4 ONE0 = {o0, o0, o0, o0};
#pragma unroll
      for (int q = 0; q < 2; q++) {
        f4 z4 = {0, 0, 0, 0};
        f4 kv0 = mfma(kf[1][q], vf[1][0], mfma(kf[0][q], vf[0][0], z4)), kv1 = mfma(kf[1][q], vf[1][1], mfma(kf[0][q], vf[0][1], z4));
        f4 zz = mfma(kf[1][q], ONE0, mfma(kf[0][q], ONE0, z4));
        sts64(lkv[3 * q], u2{pk2(kv0[0], kv0[1]), pk2(kv0[2], kv0[3])});
        sts64(lkv[3 * q + 1], u2{pk2(kv1[0], kv1[1]), pk2(kv1[2], kv1[3])});
        sts64(lkv[3 * q + 2], u2{pk2(zz[0], zz[1]), pk2(zz[2], zz[3])});
      }
      asm volatile("" ::: "memory");
      st128(lds128(L_RING + mt * 5120 + l16), rW, lane < 56 ? l16 : OOB, WS_KVP + ((j * 32 + p) * 4 + mt) * 896);
    }
    u32 pq[5][2];
    u2 xres[5];
    {
      const u32 qa0 = opaque(tr0 + 2 * 20480), qa1 = opaque(tr0 - L_RING);
      u4 wf[2][5];
#pragma unroll
      for (int n = 0; n < 5; n++) wf[0][n] = ldstr(qa0 + n * 32);
      waitv<0>();
      setflag(ws, WS_FKV + ((j * 4 + mt) * 32 + p) * 8, tag, lane);
      sfor<0, 5>([&](auto N) { ldp2<N * 32>(xres[N], rX, lr * 1280 + g * 8, (p * 64 + mt * 16) * 1280 + j * 160); });
      f4 qa[5];
      sfor<0, 20>([&](auto S) {
        constexpr int s = S, t = s + 1;
        if constexpr (s == 7) BAR();
        waitl<6>();
        sfor<0, 5>([&](auto N) {
          constexpr int n = N;
          if constexpr (n == 2) waitl<(s < 19 ? 4 : 0)>();
          if constexpr (s == 0) mm0(qa[n], wf[0][n], xq[0]); else mm(qa[n], wf[s & 1][n], xq[s]);
          if constexpr (s < 19) wf[t & 1][n] = ldstr((t < 8 ? qa0 + t * 5120 : qa1 + (t - 8) * 5120) + n * 32);
        });
      });
      MM_DONE();
#pragma unroll
      for (int n = 0; n < 5; n++) {
        pin(qa[n]);
        pq[n][0] = opaque(phi2<1>(qa[n][0], qa[n][1], qrs, qrs)); pq[n][1] = opaque(phi2<1>(qa[n][2], qa[n][3], qrs, qrs));
      }
    }
    __builtin_amdgcn_s_setprio(2);
    BAR();
    {
      float o[20];
#pragma unroll
      for (int h = 0; h < 4; h++) {
        u4 bq = {pq[h][0], pq[h][1], pq[h + 1][0], pq[h + 1][1]};
        f4 z4 = {0, 0, 0, 0};
        f4 d0 = mfma(lds128(L_IMG + h * 2048 + l16), bq, z4);
        f4 d1 = mfma(lds128(L_IMG + h * 2048 + 1024 + l16), bq, z4);
        float rd = __builtin_amdgcn_rcpf(d1[1] + 1e-6f);
#pragma unroll
        for (int i = 0; i < 4; i++) o[5 * h + i] = d0[i] * rd;
        o[5 * h + 4] = d1[0] * rd;
      }
      u32 ov[10];
#pragma unroll
      for (int i = 0; i < 10; i++) ov[i] = pk2(o[2 * i], o[2 * i + 1]);
      publish20(ov, rW, l16, WS_OB + ((p * 4 + mt) * 20 + 5 * (j >> 1)) * 1024, j);
    }
    waitv<0>();
    setflag(ws, WS_FO + ((p * 4 + mt) * 8 + j) * 8, tag, lane);
#pragma unroll
    for (int n = 0; n < 5; n++) landed(xres[n]);
    BAR();
    {
      u4 of[20], wf[2][5];
      u2 oh[4][2];
      const u32 so = WS_OB + (p * 4 + mt) * 20480;
      auto ldo = [&](auto S) {
        constexpr int s = S;
        if constexpr (s % 5 == 2) { ldv2<(s & 3) * 1024>(oh[s / 5][0], rW, l8, so + (s & ~3) * 1024); ldv2<(s & 3) * 1024 + 512>(oh[s / 5][1], rW, l8, so + (s & ~3) * 1024); }
        else ldv<(s & 3) * 1024>(of[s], rW, l16, so + (s & ~3) * 1024);
      };
      const u32 a0 = opaque(L_WO + l16), a1 = opaque(L_WO + 51200 + l16);
#pragma unroll
      for (int q = 0; q < 2; q++) {
#pragma unroll
        for (int n = 0; n < 5; n++) wf[q][n] = lds128(a0 + n * 10240 + q * 1024);
        asm volatile("" ::: "memory");
      }
      PP pp;
      poll2(pp, rW, WS_FO + ((p * 4 + mt) * 8 + (lane & 7)) * 8, tag);
      sfor<0, OP>(ldo);
      f4 ha[1][5];
      u4 pk_;
      sfor<0, 20>([&](auto S) {
        constexpr int s = S, e = OP + 2 * s < 20 ? OP + 2 * s : 20, f = OP + 2 + 2 * s < 20 ? OP + 2 + 2 * s : 20;
        constexpr int yo = (e - s - 1) + ((e + 2) / 5 - (s + 3) / 5);
        if constexpr (s == 7) pk_ = lpeek(2);
        if constexpr (s == 8) { lcheck(pk_, 2); }
        if constexpr (s == 10) lset(1, mt);
        waitvl<yo, (s == 7 ? 6 : s < 19 && s != 10 ? 5 : 0)>();
        if constexpr (s == 0) { pollend(pp); }
        sfor<0, 5>([&](auto N) {
          constexpr int n = N, t = s + 2;
          if constexpr (s == 0) mm0(ha[0][n], wf[0][n], of[s]);
          else if constexpr (s % 5 == 2) mmh(ha[0][n], wf[s & 1][n], oh[s / 5][0], oh[s / 5][1]);
          else mm(ha[0][n], wf[s & 1][n], of[s]);
          if constexpr (t < 20) wf[s & 1][n] = lds128((t < 10 ? a0 : a1) + n * 10240 + (t % 10) * 1024);
          if constexpr (n == 1 && e < f) ldo(IC<e>{});
          if constexpr (n == 3 && e + 1 < f) ldo(IC<e + 1>{});
        });
      });
      MM_DONE();
#pragma unroll
      for (int n = 0; n < 5; n++) pin(ha[0][n]);
      u32 hres[10];
      float sum = 0.f, sq = 0.f;
#pragma unroll
      for (int n = 0; n < 5; n++) {
        hres[2 * n] = pk2(unp(xres[n].x) + f2{ha[0][n][0], ha[0][n][1]});
        hres[2 * n + 1] = pk2(unp(xres[n].y) + f2{ha[0][n][2], ha[0][n][3]});
      }
      publish20(hres, rW, l16, WS_HB + ((p * 4 + mt) * 20 + 5 * (j >> 1)) * 1024, j);
#pragma unroll
      for (int i = 0; i < 10; i++) { sum = dot2c(hres[i], ONE2, sum); sq = dot2c(hres[i], hres[i], sq); }
      st64(u2{asu(xsum16_32(sum)), asu(xsum16_32(sq))}, rW, g == 0 ? l8 : OOB, WS_ST + ((p * 4 + mt) * 8 + j) * 128);
#pragma unroll
      for (int n = 0; n < 5; n++) sts64(L_HR + (mt * 5 + n) * 512 + l8, u2{hres[2 * n], hres[2 * n + 1]});
      pk_ = lpeek(4);
      waitv<0>();
      setflag(ws, WS_FH + ((p * 4 + mt) * 8 + j) * 8, tag, lane);
      lset(3, mt);
      lcheck(pk_, 4);
    }
    {
      constexpr int HD = HDV;
      const u32 pc = 2 * P + (mt >> 1), mc = 2 * (mt & 1);
      f4 ca[2][7];
      u4 hf[2][20], wf[7];
      u2 hh[2][4][2], s0v, s1v;
      u4 pk_;
      u32 pk1;
      const u32 so = WS_HB + (pc * 4 + mc) * 20480;
      auto ldh = [&](auto S) {
        constexpr int s = S;
        sfor<0, 2>([&](auto M) {
          if constexpr (s % 5 == 2) {
            ldv2<(s & 3) * 1024>(hh[M][s / 5][0], rW, l8, so + M * 20480 + (s & ~3) * 1024);
            ldv2<(s & 3) * 1024 + 512>(hh[M][s / 5][1], rW, l8, so + M * 20480 + (s & ~3) * 1024);
          } else ldv<(s & 3) * 1024>(hf[M][s], rW, l16, so + M * 20480 + (s & ~3) * 1024);
        });
      };
      const u32 a0 = opaque(L_W1A + l16), a1 = opaque(L_W1B + l16), a2 = opaque(L_W1B + 35840 + l16), a3 = opaque(L_W1B + 71680 + l16);
#pragma unroll
      for (int n = 0; n < 7; n++) wf[n] = lds128(a0 + n * 5120);
      PP pp;
      poll2(pp, rW, WS_FH + ((pc * 4 + mc) * 8 + lr) * 8, tag);
      sfor<0, 3>(ldh);
      sfor<0, 20>([&](auto S) {
        constexpr int s = S, e1 = 3 + 2 * s < s + HD ? 3 + 2 * s : s + HD, e = e1 < 20 ? e1 : 20;
        constexpr int f1 = 5 + 2 * s < s + 1 + HD ? 5 + 2 * s : s + 1 + HD, f = f1 < 20 ? f1 : 20;
        constexpr int yo = 2 * ((e - s - 1) + ((e + 2) / 5 - (s + 3) / 5));
        if constexpr (s == 8) pk_ = lpeek(5);
        if constexpr (s == 9) { lcheck(pk_, 5); }
        if constexpr (s == 17) pk1 = *(volatile LAS u32*)(L_CB + 1536 + 96 + mt * 4);
        if constexpr (s == 18) { if (__builtin_amdgcn_readfirstlane(pk1) == 0) lwait1(6, mt); }
        if constexpr (s == 19) { s0v = lds64(L_CB + 2048 + mt * 256 + lr * 8); s1v = lds64(L_CB + 2048 + mt * 256 + 128 + lr * 8); }
        waitvl<yo, (s == 8 || s == 17 ? 4 : s == 19 ? 5 : 3)>();
        if constexpr (s == 0) { pollend(pp); }
        sfor<0, 7>([&](auto N) {
          constexpr int n = N, t = s + 1;
          if constexpr (n == 4) waitl<(s < 19 ? 4 : 0)>();
          sfor<0, 2>([&](auto M) {
            if constexpr (s == 0) mm0(ca[M][n], wf[n], hf[M][s]);
            else if constexpr (s % 5 == 2) mmh(ca[M][n], wf[n], hh[M][s / 5][0], hh[M][s / 5][1]);
            else mm(ca[M][n], wf[n], hf[M][s]);
          });
          if constexpr (s < 19) wf[n] = lds128((t < 5 ? a0 : t < 10 ? a1 : t < 15 ? a2 : a3) + n * 5120 + (t % 5) * 1024);
          if constexpr (n == 1 && e < f) ldh(IC<e>{});
          if constexpr (n == 4 && e + 1 < f) ldh(IC<e + 1>{});
        });
      });
      MM_DONE();
      lset(7, mt);
      const u32 sh = WS_HID + (pc * 4 + mc) * 54 * 1024, fh = WS_FHID + (((pc * 16 + c) * 2 + (mt & 1)) * 2) * 8;
      const float sc[2] = {asf(s0v.x), asf(s1v.x)}, sm[2] = {asf(s0v.y), asf(s1v.y)};
      u32 hp[2][2][2];
      sfor<0, 7>([&](auto N) {
        constexpr int n = N;
        const f4 c4 = __builtin_bit_cast(f4, lds128(L_CB + n * 64 + g * 16)), b4 = __builtin_bit_cast(f4, lds128(L_CB + 512 + n * 64 + g * 16));
        if constexpr (n == 6) { waitv<2>(); setflag(ws, fh, tag, lane); }
        sfor<0, 2>([&](auto M) {
          pin(ca[M][n]);
#pragma unroll
          for (int i = 0; i < 2; i++) {
            const f2 v = f2{ca[M][n][2 * i], ca[M][n][2 * i + 1]} * sc[M] + (f2{b4[2 * i], b4[2 * i + 1]} - sm[M] * f2{c4[2 * i], c4[2 * i + 1]});
            const f2 u = v * (-2.302208198f - 0.1029432397f * v * v);
            const f2 d = f2{__builtin_amdgcn_exp2f(u[0]), __builtin_amdgcn_exp2f(u[1])} + 1.f;
            hp[M][n & 1][i] = pk2(v * f2{__builtin_amdgcn_rcpf(d[0]), __builtin_amdgcn_rcpf(d[1])});
          }
          if constexpr (n & 1) st128(u4{hp[M][0][0], hp[M][0][1], hp[M][1][0], hp[M][1][1]}, rW, l16, sh + M * 54 * 1024 + ((t0 >> 1) + n / 2) * 1024);
          if constexpr (n == 6) st64(u2{hp[M][0][0], hp[M][0][1]}, rW, rho < 6 ? l8 + odd * 512 : OOB, sh + M * 54 * 1024 + (tl >> 1) * 1024);
        });
      });
      waitv<0>();
      setflag(ws, fh + 8, tag, lane);
      __builtin_amdgcn_s_setprio(0);
      lwait1(8, mt);
      const u32 wa = opaque(mt * 35840 + l16);
#pragma unroll
      for (int i = 0; i < 35; i++) w2f[i] = lds128(wa + i * 1024);
      lset(9, mt);
    }
  } else {
    LD_EPOCH();
    u4 sel[2];
    u32 dst[2];
    auto setup = [&]() {
#pragma unroll
    for (int q = 0; q < 2; q++) {
      const u32 k = lr + 16 * q - 8 * g, one = (k & 1) ? 0x3F800000u : 0x00003F80u;
      sel[q] = u4{k < 2 ? one : 0u, (k >> 1) == 1 ? one : 0u, (k >> 1) == 2 ? one : 0u, (k >> 1) == 3 ? one : 0u};
      landed(sel[q]);
    }
#pragma unroll
    for (int h = 0; h < 2; h++) {
      const u32 qd = 2 * (lr + 16 * (2 * h + (g >> 1))) + (g & 1), e = (qd * 205) >> 10, dq = qd - e * 5;
      const u32 gs = dq + mt, grp = (e * 13) >> 6, ii = e - grp * 5;
      const u32 base = L_IMG + mt * 2048 + (gs & 3) * 256 + (gs >> 2) * 8;
      dst[h] = opaque(e < 20 ? base + (ii < 4 ? (4 * grp + ii) * 16 : 1024 + 4 * grp * 16) : (e == 20 ? base + 1024 + 16 : L_CB + 1792));
    }
    };
    if (mt < 3) {
      const rsrc_t rQ = __builtin_amdgcn_make_buffer_rsrc((void*)((const char*)wqkv - 4096), 0, 640 * 3840 + 4096, 0x00020000);
      u32 voffW[7];
#pragma unroll
      for (int i = 0; i < 7; i++) {
        const u32 pi = i < 5 ? i : (i == 5 ? mt : (mt < 2 ? mt + 3 : 4u));
        const u32 f = 64 * pi + lane, sg = (f * 6554) >> 16, ch = f - sg * 10;
        voffW[i] = (((sg & 12) << 1) + ((sg >> 4) << 2) + (sg & 3)) * 3840 + ch * 16 + 4096 - (i < 4 ? i * 1024 : 0);
      }
      const u32 soW = (mt >> 1) * 32 * 3840 + ((mt & 1) + 1) * 1280 + j * 160, soW3 = 32 * 3840 + 2560 + j * 160;
      const u32 soQ = mt * 32 * 3840 + j * 160, soQ3 = 96 * 3840 + j * 160;
      const u32 l5 = 15360 + mt * 1024, l6 = 15360 + (mt < 2 ? mt + 3 : 4u) * 1024;
      u32 st = rot, qs = rotq;
      auto issueW = [&](u32 so, u32 so3, u32 ld) {
        dma<0>(rQ, voffW[0], so, ld + mt * 5120); dma_c<1024>(rQ, voffW[1], so); dma_c<2048>(rQ, voffW[2], so); dma_c<3072>(rQ, voffW[3], so);
        dma<0>(rQ, voffW[4], so, ld + mt * 5120 + 4096); dma<0>(rQ, voffW[5], so3, ld + l5);
        if (mt < 2) dma<0>(rQ, voffW[6], so3, ld + l6);
      };
      auto issueKV = [&](u32 slot) { issueW(soW + st * 245760, soW3 + st * 245760, L_RING + slot * 20480); st = st + 1 >= 10 ? 0 : st + 1; };
      auto issueQ = [&](u32 ld) { issueW(soQ + qs * 491520, soQ3 + qs * 491520, ld); qs = qs + 1 >= 5 ? 0 : qs + 1; };
      issueKV(0); issueKV(1); issueKV(2);
      waitv<12>();
      BAR();
      issueKV(3);
#pragma nounroll
      for (u32 i = 0; i < 6; i++) { waitv<12>(); asm volatile("s_barrier" ::: "memory"); issueKV(i & 3); }
      auto issueQa = [&](u32 slot) {
        const u32 so = soQ + qs * 491520;
        dma<0>(rQ, voffW[0], so, L_RING + slot * 20480 + mt * 5120); dma_c<1024>(rQ, voffW[1], so); dma_c<2048>(rQ, voffW[2], so);
      };
      auto issueQb = [&](u32 slot) {
        const u32 so = soQ + qs * 491520, so3 = soQ3 + qs * 491520, ld = L_RING + slot * 20480;
        dma<3072>(rQ, voffW[3], so, ld + mt * 5120); dma<0>(rQ, voffW[4], so, ld + mt * 5120 + 4096); dma<0>(rQ, voffW[5], so3, ld + l5);
        if (mt < 2) dma<0>(rQ, voffW[6], so3, ld + l6);
        qs = qs + 1 >= 5 ? 0 : qs + 1;
      };
      waitv<12>(); BAR(); issueQa(2);
      waitv<9>(); BAR(); issueQb(2);
      waitv<6>(); BAR(); issueQa(3);
      BAR();
      issueQb(3);
      issueQ(0);
      waitv<6>();
      BAR();
      GET_TAG();
      setup();
      waitv<TQ>(); issueQ(20480); waitv<TQ>(); issueQ(40960);
    } else {
      const bool m1 = b < 108, mo = b >= 216;
      const u32 v = m1 ? b : (mo ? b - 216 : b - 108), vq = m1 ? (v * 2428) >> 16 : (v * 6554) >> 16;
      const u32 sA = uni((m1 || mo) ? 5 * vq : v >> 1), cA = uni(m1 ? v - 27 * vq : (mo ? v - 10 * vq : 5 * (v & 1))), sQ = uni((m1 || mo) ? 1u : 0u);
      const u32 pitch = uni(m1 ? 3456 : 1280);
      const u64 src = m1 ? (u64)w1 : (mo ? (u64)wo : (u64)w2);
      const rsrc_t rS = __builtin_amdgcn_make_buffer_rsrc((void*)src, 0, mo ? 640 * 1280 : 640 * 3456, 0x00020000);
      u32 vrt[20];
#pragma unroll
      for (int I = 0; I < 20; I++) {
        const u32 q = I >> 2, i = I & 3, ro = 8 * i + (lane >> 3);
        const u32 i40 = 8 * q + 4 * (i >> 1) + (ro & 3), hf = i40 >= 20 ? 1u : 0u, id = i40 - 20 * hf;
        const u32 rp = opaque(80 * hf + 15 * ((id * 13) >> 6) + 5 * ((ro >> 2) & 3) + id);
        vrt[I] = opaque((mo ? rp : 32 * sQ * q + ro) * pitch + (lane & 7) * 16 + (q - sQ * q) * 128);
      }
      u4 ONES = {0x3F803F80u, 0x3F803F80u, 0x3F803F80u, 0x3F803F80u};
      landed(ONES);
      const u32 sc = L_RING + 16384;
      const u32 wa = sc + (lane >> 3) * 128 + (((lane & 7) ^ (((lane >> 4) & 3) << 1)) << 4);
      const u32 r0 = 4 * g + (lr >> 2), ta = opaque(sc + r0 * 128 + (lr & 1) * 8 + ((((lr >> 1) & 1) ^ (((r0 >> 1) & 3) << 1)) << 4));
      const u32 dT = uni(m1 ? 20480u : (mo ? 10240u : 55296u)), dQ = uni(sQ ? 1024u : 4 * 55296u);
      const u32 dA = uni((m1 ? WS_W1T : (mo ? WS_WOT + (sA >= 10 ? 40960u : 0u) : WS_W2T)) + 4 * cA * dT + sA * 1024);
      u32 dJ[4];
#pragma unroll
      for (int n = 0; n < 4; n++) dJ[n] = uni(dA + n * dT + (mo ? (((4 * cA + n) * 52) >> 8) * 51200u : 0u));
      waitv<0>();
      GET_TAG();
      setup();
#pragma nounroll
      for (u32 i = 0; i < 8; i++) BAR();
      u4 f[5][4];
      sfor<0, 7>([&](auto I) { ldp<0>(f[I >> 2][I & 3], rS, vrt[I], 32 * sA * pitch + cA * 128); });
      BAR();
      sfor<7, 14>([&](auto I) { ldp<0>(f[I >> 2][I & 3], rS, vrt[I], 32 * sA * pitch + cA * 128); });
      BAR();
      sfor<14, 20>([&](auto I) { ldp<0>(f[I >> 2][I & 3], rS, vrt[I], 32 * sA * pitch + cA * 128); });
      BAR();
      f4 cs[4];
      sfor<0, 5>([&](auto Q) {
        constexpr int q = Q;
        waitv<16 - 4 * q>();
#pragma unroll
        for (int i = 0; i < 4; i++) { landed(f[q][i]); sts128(wa + i * 1024, f[q][i]); }
        asm volatile("" ::: "memory");
#pragma unroll
        for (int n = 0; n < 4; n++) f[q][n] = ldstr(ta ^ (n << 5), (ta ^ (n << 5)) + 2048);
        asm volatile("" ::: "memory");
#pragma unroll
        for (int n = 0; n < 4; n++) cs[n] = mfma(f[q][n], ONES, q == 0 ? f4{0, 0, 0, 0} : cs[n]);
      });
#pragma unroll
      for (int q = 0; q < 5; q++) {
#pragma unroll
        for (int n = 0; n < 4; n++) st128(f[q][n], rW, l16, dJ[n] + q * dQ);
      }
#pragma unroll
      for (int n = 0; n < 4; n++) st128(__builtin_bit_cast(u4, cs[n]), rW, (m1 && lr == 0) ? g * 16 : OOB, WS_CS1 + vq * 6912 + (4 * cA + n) * 64);
      BAR();
    }
    if (mt < 3) waitv<0>();
    BAR();
    if (mt == 3) { waitv<0>(); setflag(ws, WS_FRT + b * 8, tag, lane); }
    PP pp;
    poll2(pp, rW, WS_FKV + ((j * 4 + mt) * 32 + (lane & 31)) * 8, tag);
    {
      const u32 so = WS_KVP + (j * 32 * 4 + mt) * 896;
      const u32 v0 = lane < 53 ? l16 : OOB;
      u4 a[32], fr[2];
      auto lda = [&](auto Q) { ldv<(Q & 1) * 3584>(a[Q], rW, v0, so + (Q & ~1) * 3584); };
      sfor<0, 14>(lda);
      sts128(L_IMG + mt * 2048 + l16, u4{0, 0, 0, 0}); sts128(L_IMG + mt * 2048 + 1024 + l16, u4{0, 0, 0, 0});
      if (lane < 12) sts128(L_CB + 1536 + l16, u4{0, 0, 0, 0});
      const bool zrow = (dst[1] & 0x430) == 0x410 && dst[1] < L_CB;
      f4 s0, s1, s2, s3;
      sfor<0, 16>([&](auto Q) {
        waitv<(Q < 9 ? 12 : 32 - 2 * Q)>();
        if constexpr (Q == 0) pollend(pp);
        if constexpr (Q == 0) { mm0(s0, sel[0], a[0]); mm0(s1, sel[1], a[0]); mm0(s2, sel[0], a[1]); mm0(s3, sel[1], a[1]); }
        else { mm(s0, sel[0], a[2 * Q]); mm(s1, sel[1], a[2 * Q]); mm(s2, sel[0], a[2 * Q + 1]); mm(s3, sel[1], a[2 * Q + 1]); }
        if constexpr (Q < 9) { lda(IC<14 + 2 * Q>{}); lda(IC<15 + 2 * Q>{}); }
        if constexpr (Q == 8) { ldv<0>(fr[0], rW, lane * 32, WS_FRT); ldv<16>(fr[1], rW, lane * 32, WS_FRT); }
      });
      MM_DONE();
      pin(s0); pin(s1); pin(s2); pin(s3);
      s0 += s2; s1 += s3;
      const u2 v1 = {pk2(s1[0], s1[1]), pk2(s1[2], s1[3])};
      sts64(dst[0], u2{pk2(s0[0], s0[1]), pk2(s0[2], s0[3])});
      sts64(dst[1], v1);
      if (zrow) { sts64(dst[1] + 64, v1); sts64(dst[1] + 128, v1); sts64(dst[1] + 192, v1); }
      BAR();
      RT_CHECK();
    }
    auto issueW1 = [&](u32 it, u32 n, u32 ld) {
      const u32 so = WS_W1T + (n < 6 ? t0 + n : tl) * 20480 + it * 5120;
      dma4_sc1(rW, l16, so, ld + n * 5120); dma1_sc1(rW, l16, so + 4096, ld + n * 5120 + 4096);
    };
    const u32 o = 51200 + mt * 12288, o2 = 51200 + 49152 + (mt & 1) * 1024, sw = WS_WOT + j * 102400;
    {
      const u32 p0 = mt * 12288, p2 = 49152 + (mt & 1) * 1024;
      dma4_sc1(rW, l16, sw + p0, L_WO + p0); dma4_sc1(rW, l16, sw + p0 + 4096, L_WO + p0 + 4096); dma4_sc1(rW, l16, sw + p0 + 8192, L_WO + p0 + 8192);
      dma1_sc1(rW, l16, sw + p2, L_WO + p2);
    }
    if (b == 0 && tid == 256) __hip_atomic_store((GAS u64*)ws, tag, __ATOMIC_RELAXED, __HIP_MEMORY_SCOPE_AGENT);
    {
      f4 csum;
      u32 ci = lane + 64 * (mt & 1); ci = ci > 111 ? 111 : ci;
      const u32 n = ci >> 4, col = (n < 6 ? t0 + n : tl) * 16 + (ci & 15);
#pragma unroll
      for (int q = 0; q < 4; q++) csum[q] = asf(__builtin_amdgcn_raw_buffer_load_b32(rW, col * 4, WS_CS1 + q * 6912, SC1));
      const u32 bv = b1[col], b2v = ((const u32*)b2)[40 * j + (lane < 40 ? lane : 0)];
      sts32(L_CB + ci * 4, (csum[0] + csum[1]) + (csum[2] + csum[3]));
      sts32(L_CB + 512 + ci * 4, blo(bv));
      if (lane < 40) *(LAS u32*)(L_CB + 1024 + lane * 4) = b2v;
    }
    waitv<0>();
    BAR();
    auto w1t = [&](u32 it, u32 n, u32 ld) { waitv<TH>(); issueW1(it, n, ld); };
    auto w2t = [&](auto I) { waitv<TH + 3>(); ldw2(I); };
    dma4_sc1(rW, l16, sw + o, L_WO + o); waitv<TH>(); dma4_sc1(rW, l16, sw + o + 4096, L_WO + o + 4096);
    waitv<TH>(); dma4_sc1(rW, l16, sw + o + 8192, L_WO + o + 8192); dma1_sc1(rW, l16, sw + o2, L_WO + o2);
    w1t(0, mt, L_W1A);
    waitv<5>(); lset(2, mt);
    w1t(0, mt < 3 ? mt + 4 : 6, L_W1A);
    sfor<0, HA>(w2t);
    lwait(1);
    w1t(1, mt, L_W1B); w1t(1, mt < 3 ? mt + 4 : 6, L_W1B);
    sfor<HA, HA + 4>(w2t);
    waitv<4>(); lset(4, mt);
    sfor<HA + 4, HB2>(w2t);
    lwait(3);
    w1t(2, mt, L_W1B + 35840); w1t(2, mt < 3 ? mt + 4 : 6, L_W1B + 35840);
    {
      const u32 pc = 2 * P + (mt >> 1), mc = 2 * (mt & 1);
      poll(ws, WS_FH + ((pc * 4 + mc) * 8 + lr) * 8, tag);
      u2 sv[2][2];
      sfor<0, 2>([&](auto M) {
        ldv2<M * 1024>(sv[M][0], rW, g * 256 + lr * 8, WS_ST + (pc * 4 + mc) * 1024); ldv2<M * 1024 + 128>(sv[M][1], rW, g * 256 + lr * 8, WS_ST + (pc * 4 + mc) * 1024);
      });
      issueW1(3, mt, L_W1B + 71680); w1t(3, mt < 3 ? mt + 4 : 6, L_W1B + 71680);
      waitv<0>(); lset(5, mt);
      __builtin_amdgcn_s_setprio(3);
#pragma unroll
      for (int m = 0; m < 2; m++) {
        landed(sv[m][0]); landed(sv[m][1]);
        const float mean = xsum16_32(asf(sv[m][0].x) + asf(sv[m][1].x)) * (1.f / 640.f);
        const float var = fmaxf(xsum16_32(asf(sv[m][0].y) + asf(sv[m][1].y)) * (1.f / 640.f) - mean * mean, 0.f);
        const float m1 = rbf(mean), sd1 = rbf(__builtin_amdgcn_sqrtf(rbf(rbf(var) + 1e-5f))), r1 = __builtin_amdgcn_rcpf(sd1);
        const float m2 = rbf((mean - m1) * r1), sd2 = rbf(__builtin_amdgcn_sqrtf(rbf(rbf(var * r1 * r1) + 1e-5f)));
        const float sc = r1 * __builtin_amdgcn_rcpf(sd2);
        sts64(L_CB + 2048 + mt * 256 + m * 128 + lr * 8, u2{asu(sc), asu(sc * (m1 + m2 * sd1))});
      }
      __builtin_amdgcn_s_setprio(0);
    }
    lset(6, mt);
    sfor<HB2, 35>(w2t);
    lwait(7);
    {
      const u32 so = WS_W2T + (j * 5 * 54 + 7 * mt) * 1024, ld = mt * 35840;
#pragma nounroll
      for (u32 n = 0; n < 5; n++) {
        waitv<TH2>();
        sfor<0, 7>([&](auto S) {
          constexpr int k = S < 2 ? S : (S < 4 ? S + 2 : (S < 6 ? S - 2 : 6));
          dma1_sc1(rW, l16, so + (n * 54 + k) * 1024, ld + (S * 5 + n) * 1024);
        });
      }
    }
    waitv<0>();
    lset(8, mt);
  }
  {
    f4 da[4][5];
    u4 xf[7][4];
    u2 xh[4][2], hr[3];
    const u32 so = WS_HID + (p * 4 * 54 + s0) * 1024;
    const u32 vh0 = w < 6 ? l8 : l16 + 2048, vh1 = w < 6 ? l8 + 512 : l16 + 2056;
    auto ldx = [&](auto U) {
      constexpr int s = U / 4, a = U % 4;
      if constexpr (s < 2) ldv<s * 1024>(xf[s][a], rW, l16, so + a * 54 * 1024);
      else if constexpr (s < 4) ldv<(s - 2) * 1024>(xf[s][a], rW, vmid, so + (a * 54 + 3) * 1024);
      else if constexpr (s == 4) ldv<2048>(xf[s][a], rW, l16, so + a * 54 * 1024);
      else if constexpr (s == 5) { ldv2<0>(xh[a][0], rW, vh0, so + (a * 54 + 3) * 1024); ldv2<0>(xh[a][1], rW, vh1, so + (a * 54 + 3) * 1024); }
      else ldv<0>(xf[s][a], rW, vlast, so + (a * 54 + 3) * 1024);
    };
    const u32 fo = WS_FHID + ((p * 16 + 2 * w) * 2 + (lane & 3)) * 16;
    const u32 rw = opaque(L_RED + (w >> 1) * 1024 + l16 + (w & 1) * 8);
    auto put = [&](auto A, auto N) {
      pin(da[A][N]);
      sts64(rw + (A * 5 + N) * 4096, u2{pk2(da[A][N][0], da[A][N][1]), pk2(da[A][N][2], da[A][N][3])});
    };
    {
      auto ldu = [&](auto V) { constexpr int v = V; ldx(IC<(v < 16 ? v : 16 + 4 * ((v - 16) % 3) + (v - 16) / 3)>{}); };
      PP pp, pq2;
      poll2(pp, rW, fo, tag);
      sfor<0, d_issued(0)>(ldu);
      sfor<0, 28>([&](auto U) {
        constexpr int u = U, s = u < 16 ? u / 4 : 4 + (u - 16) % 3, a = u < 16 ? u % 4 : (u - 16) / 3;
        waitv<d_younger(u)>();
        if constexpr (u == 0) pollend(pp);
        if constexpr (u == 16) { pollend(pq2); lwait(9); }
        sfor<0, 5>([&](auto N) {
          if constexpr (s == 0) mm0(da[a][N], w2f[N], xf[s][a]);
          else if constexpr (s == 5) mmh(da[a][N], w2f[5 * s + N], xh[a][0], xh[a][1]);
          else mm(da[a][N], w2f[5 * s + N], xf[s][a]);
          if constexpr (s == 5 && a > 0 && N < 3) { put(IC<a - 1>{}, N); __builtin_amdgcn_sched_barrier(0); }
          if constexpr (s == 6 && a > 0 && N < 2) { put(IC<a - 1>{}, IC<3 + N>{}); __builtin_amdgcn_sched_barrier(0); }
        });
        if constexpr (u == DLP) { poll2(pq2, rW, fo + 8, tag); }
        sfor<d_issued(u), d_issued(u + 1)>(ldu);
      });
    }
    MM_DONE();
    sfor<0, 5>([&](auto N) { put(IC<3>{}, N); });
    const rsrc_t rOut = __builtin_amdgcn_make_buffer_rsrc((void*)out, 0, 2048 * 1280, 0x00020000);
    const u32 LO = opaque(0x00003F80u), HI = opaque(0x3F800000u), n0 = 3 * nh;
    const u32 ra = opaque(L_RED + (mt * 5 + n0) * 4096 + l16);
    u2 bb[3];
#pragma unroll
    for (int i = 0; i < 3; i++) {
      bb[i] = lds64(L_CB + 1024 + (n0 + i) * 32 + g * 8);
      hr[i] = lds64(L_HR + (mt * 5 + n0 + i) * 512 + l8);
    }
    BAR();
    u4 pv[3][4];
#pragma unroll
    for (int i = 0; i < 3; i++)
#pragma unroll
      for (int q = 0; q < 4; q++) pv[i][q] = lds128(ra + i * 4096 + q * 1024);
#pragma unroll
    for (int i = 0; i < 3; i++) {
      float s0 = 0.f, s1 = 0.f, s2 = 0.f, s3 = 0.f;
#pragma unroll
      for (int q = 0; q < 4; q++) {
        s0 = dot2c(pv[i][q].x, LO, s0); s1 = dot2c(pv[i][q].x, HI, s1); s2 = dot2c(pv[i][q].y, LO, s2); s3 = dot2c(pv[i][q].y, HI, s3);
        s0 = dot2c(pv[i][q].z, LO, s0); s1 = dot2c(pv[i][q].z, HI, s1); s2 = dot2c(pv[i][q].w, LO, s2); s3 = dot2c(pv[i][q].w, HI, s3);
      }
      const u32 o0 = pk2(unp(hr[i].x) + unp(pk2(f2{s0, s1} + unp(bb[i].x)))), o1 = pk2(unp(hr[i].y) + unp(pk2(f2{s2, s3} + unp(bb[i].y))));
      __builtin_amdgcn_raw_buffer_store_b64(u2{o0, o1}, rOut, (i == 2 && helper) ? OOB : lr * 1280 + g * 8 + i * 32, (p * 64 + mt * 16) * 1280 + j * 160 + n0 * 32, 0);
    }
  }
}

extern "C" void kernel_launch(void* const* d_in, const int* in_sizes, int n_in, void* d_out, int out_size, void* d_ws, size_t ws_size,
                              hipStream_t stream) {
  fused_block<<<256, 512, 0, stream>>>((const u16*)d_in[0], (const u16*)d_in[1], (const u16*)d_in[2], (const u16*)d_in[4], (const u16*)d_in[5],
                                       (const u16*)d_in[6], (const u16*)d_in[7], (u16*)d_out, (char*)d_ws);
}
```

```cpp
#include <hip/hip_runtime.h>
#include <stdint.h>
#pragma clang diagnostic ignored "-Wint-to-pointer-cast"
#pragma clang diagnostic ignored "-Warray-bounds"

typedef unsigned short u16;
typedef unsigned u32;
typedef unsigned long long u64;
typedef float f2 __attribute__((ext_vector_type(2)));
typedef float f4 __attribute__((ext_vector_type(4)));
typedef unsigned u2 __attribute__((ext_vector_type(2)));
typedef unsigned u4 __attribute__((ext_vector_type(4)));
typedef short s4 __attribute__((ext_vector_type(4)));
typedef __bf16 bf2 __attribute__((ext_vector_type(2)));
typedef __bf16 bf8 __attribute__((ext_vector_type(8)));
typedef __amdgpu_buffer_rsrc_t rsrc_t;
#define LAS __attribute__((address_space(3)))
#define GAS __attribute__((address_space(1)))
#define DI __device__ __forceinline__

template <int I> struct IC { static constexpr int v = I; constexpr operator int() const { return I; } };
template <int B, int E, class F> DI void sfor(F&& f) {
  if constexpr (B < E) { f(IC<B>{}); sfor<B + 1, E>(f); }
}

constexpr u32 WS_FRT = 4096, WS_FKV = 8192, WS_FO = 16384, WS_FH = 24576, WS_FHID = 32768;
constexpr u32 WS_KVP = 65536;
constexpr u32 WS_OB = 1u << 20;
constexpr u32 WS_HB = WS_OB + 32 * 81920;
constexpr u32 WS_ST = WS_HB + 32 * 81920;
constexpr u32 WS_CS1 = WS_ST + 2048 * 64;
constexpr u32 WS_WOT = WS_CS1 + 32768;
constexpr u32 WS_W1T = WS_WOT + 40 * 20480;
constexpr u32 WS_W2T = WS_W1T + 108 * 20480;
constexpr u32 WS_HID = WS_W2T + 8 * 54 * 5120;
constexpr u32 WS_END = WS_HID + 32 * 4 * 54 * 1024;
constexpr u32 OOB = 0x7FFFFF00u;
constexpr int SC1 = 16;
constexpr int TH = 4, TH2 = 6, TQ = 2, HA = 10, HB2 = 14, OP = 6, HDV = 8, DLP = 7, DX0 = 6, DB = 7, DG = 5;

constexpr u32 L_X = 0, L_RING = 81920;
constexpr u32 L_WO = 0, L_IMG = 147456, L_W1A = 110592, L_HR = 147456, L_CB = 160768, L_W1B = 0, L_RED = 0;

DI f4 mfma(u4 a, u4 b, f4 c) {
  return __builtin_amdgcn_mfma_f32_16x16x32_bf16(__builtin_bit_cast(bf8, a), __builtin_bit_cast(bf8, b), c, 0, 0, 0);
}
DI void mm(f4& c, u4 a, u4 b) { asm volatile("v_mfma_f32_16x16x32_bf16 %0, %1, %2, %0" : "+v"(c) : "v"(a), "v"(b) : "memory"); }
DI void mm0(f4& c, u4 a, u4 b) { asm volatile("v_mfma_f32_16x16x32_bf16 %0, %1, %2, 0" : "=&v"(c) : "v"(a), "v"(b) : "memory"); }
#define MM_DONE() asm volatile("s_nop 7\n\ts_nop 1" ::: "memory")
DI void pin(f4& c) { asm volatile("" : "+v"(c)); }
DI void zero(f4& c) { c = f4{0, 0, 0, 0}; asm volatile("" : "+v"(c)); }
DI void undef(u4& d) { asm volatile("" : "=v"(d)); }
DI u32 uni(u32 v) { v = __builtin_amdgcn_readfirstlane(v); asm volatile("" : "+s"(v)); return v; }
DI u32 opaque(u32 v) { asm volatile("" : "+v"(v)); return v; }

DI u32 pk2(float a, float b) { f2 v = {a, b}; return __builtin_bit_cast(u32, __builtin_convertvector(v, bf2)); }
DI u32 pk2(f2 v) { return __builtin_bit_cast(u32, __builtin_convertvector(v, bf2)); }
DI float rbf(float a) { return __builtin_bit_cast(float, pk2(a, a) << 16); }
DI float blo(u32 v) { return __builtin_bit_cast(float, v << 16); }
DI float bhi(u32 v) { return __builtin_bit_cast(float, v & 0xFFFF0000u); }
DI f2 unp(u32 v) { return f2{blo(v), bhi(v)}; }
DI float asf(u32 v) { return __builtin_bit_cast(float, v); }
DI u32 asu(float v) { return __builtin_bit_cast(u32, v); }

template <int OFF> DI void dma(rsrc_t r, u32 voff, u32 soff, u32 lds) {
  asm volatile("s_mov_b32 m0, %0\n\ts_nop 0\n\tbuffer_load_dwordx4 %1, %2, %3 offen offset:%4 lds" ::"s"(lds), "v"(voff), "s"(r), "s"(soff), "n"(OFF) : "memory");
}
template <int OFF> DI void dma_c(rsrc_t r, u32 voff, u32 soff) {
  asm volatile("buffer_load_dwordx4 %0, %1, %2 offen offset:%3 lds" ::"v"(voff), "s"(r), "s"(soff), "n"(OFF) : "memory");
}
DI void dma4_sc1(rsrc_t r, u32 voff, u32 soff, u32 lds) {
  asm volatile("s_mov_b32 m0, %0\n\ts_nop 0\n\tbuffer_load_dwordx4 %1, %2, %3 offen sc1 lds\n\tbuffer_load_dwordx4 %1, %2, %3 offen offset:1024 sc1 lds\n\t"
               "buffer_load_dwordx4 %1, %2, %3 offen offset:2048 sc1 lds\n\tbuffer_load_dwordx4 %1, %2, %3 offen offset:3072 sc1 lds" ::"s"(lds), "v"(voff), "s"(r), "s"(soff) : "memory");
}
DI void dma1_sc1(rsrc_t r, u32 voff, u32 soff, u32 lds) {
  asm volatile("s_mov_b32 m0, %0\n\ts_nop 0\n\tbuffer_load_dwordx4 %1, %2, %3 offen sc1 lds" ::"s"(lds), "v"(voff), "s"(r), "s"(soff) : "memory");
}
template <int OFF> DI void ldv(u4& d, rsrc_t r, u32 voff, u32 soff) {
  asm volatile("buffer_load_dwordx4 %0, %1, %2, %3 offen offset:%4 sc1" : "=v"(d) : "v"(voff), "s"(r), "s"(soff), "n"(OFF) : "memory");
}
template <int OFF> DI void ldp(u4& d, rsrc_t r, u32 voff, u32 soff) {
  asm volatile("buffer_load_dwordx4 %0, %1, %2, %3 offen offset:%4" : "=v"(d) : "v"(voff), "s"(r), "s"(soff), "n"(OFF) : "memory");
}
template <int OFF> DI void ldp2(u2& d, rsrc_t r, u32 voff, u32 soff) {
  asm volatile("buffer_load_dwordx2 %0, %1, %2, %3 offen offset:%4" : "=v"(d) : "v"(voff), "s"(r), "s"(soff), "n"(OFF) : "memory");
}
template <int N> DI void waitv() { asm volatile("s_waitcnt vmcnt(%0)" ::"n"(N) : "memory"); }
template <class T> DI void landed(T& d) { asm volatile("" : "+v"(d)); }
#define BAR() do { asm volatile("" ::: "memory"); __builtin_amdgcn_s_waitcnt(0xC07F); asm volatile("s_barrier" ::: "memory"); } while (0)
DI u4 lds128(u32 a) { return *(LAS u4*)(a); }
DI u2 lds64(u32 a) { return *(LAS u2*)(a); }
DI void sts128(u32 a, u4 v) { *(LAS u4*)(a) = v; }
DI void sts64(u32 a, u2 v) { *(LAS u2*)(a) = v; }
DI void sts32(u32 a, float v) { *(LAS float*)(a) = v; }
DI u4 ldstr(u32 a0, u32 a1) {
  u2 x = __builtin_bit_cast(u2, __builtin_amdgcn_ds_read_tr16_b64_v4i16((LAS s4*)(a0)));
  u2 y = __builtin_bit_cast(u2, __builtin_amdgcn_ds_read_tr16_b64_v4i16((LAS s4*)(a1)));
  return u4{x.x, x.y, y.x, y.y};
}
DI u4 ldstr(u32 a) { return ldstr(a, a + 2560); }
DI float xsum16_32(float v) {
  auto r = __builtin_amdgcn_permlane16_swap(asu(v), asu(v), false, false);
  v = asf((u32)r[0]) + asf((u32)r[1]);
  auto q = __builtin_amdgcn_permlane32_swap(asu(v), asu(v), false, false);
  return asf((u32)q[0]) + asf((u32)q[1]);
}
DI float xsum8(float v) {
  v += asf(__builtin_amdgcn_update_dpp(0, asu(v), 0xB1, 0xf, 0xf, true));
  v += asf(__builtin_amdgcn_update_dpp(0, asu(v), 0x4E, 0xf, 0xf, true));
  return v + asf(__builtin_amdgcn_update_dpp(0, asu(v), 0x141, 0xf, 0xf, true));
}
DI void poll(rsrc_t r, u32 voff, u64 tag) {
  const u32 tl = (u32)tag, th = (u32)(tag >> 32);
#pragma nounroll
  for (u32 it = 0; it < (1u << 18); ++it) {
    u2 v;
    asm volatile("buffer_load_dwordx2 %0, %1, %2, 0 offen sc1\n\ts_waitcnt vmcnt(0)" : "=&v"(v) : "v"(voff), "s"(r) : "memory");
    if (!__any(((v.x ^ tl) | (v.y ^ th)) != 0)) break;
    __builtin_amdgcn_s_sleep(2);
  }
  asm volatile("" ::: "memory");
}
DI void lset_(u32 k, u32 mt, u32 tg) { __builtin_amdgcn_s_waitcnt(0xC07F); *(volatile LAS u32*)(L_CB + 1536 + k * 16 + mt * 4) = tg; asm volatile("" ::: "memory"); }
DI void lwait_(u32 k, u32 tg) {
  asm volatile("" ::: "memory");
  for (;;) {
    const u4 v = *(volatile LAS u4*)(L_CB + 1536 + k * 16);
    if (__builtin_amdgcn_readfirstlane((v.x ^ tg) | (v.y ^ tg) | (v.z ^ tg) | (v.w ^ tg)) == 0) break;
    __builtin_amdgcn_s_sleep(1);
  }
  asm volatile("" ::: "memory");
}
DI u4 lpeek(u32 k) { asm volatile("" ::: "memory"); const u4 v = *(volatile LAS u4*)(L_CB + 1536 + k * 16); asm volatile("" ::: "memory"); return v; }
DI void lcheck(u4 v, u32 k) { if (__builtin_amdgcn_readfirstlane(v.x & v.y & v.z & v.w) == 0) lwait_(k, 1u); asm volatile("" ::: "memory"); }
DI void lwait1(u32 k, u32 mt) {
  asm volatile("" ::: "memory");
  while (__builtin_amdgcn_readfirstlane(*(volatile LAS u32*)(L_CB + 1536 + k * 16 + mt * 4)) == 0) __builtin_amdgcn_s_sleep(1);
  asm volatile("" ::: "memory");
}
#define lset(k, mt) lset_(k, mt, 1u)
#define lwait(k) lwait_(k, 1u)
DI void setflag(char* ws, u32 off, u64 tag, u32 lane) {
  if (lane == 0) __hip_atomic_store((GAS u64*)(ws + off), tag, __ATOMIC_RELAXED, __HIP_MEMORY_SCOPE_AGENT);
  asm volatile("" ::: "memory");
}
template <bool PHI> DI u32 phi2(float a0, float a1, float r0, float r1) {
  const f2 a = {a0, a1}, r = {r0, r1};
  if constexpr (!PHI) return pk2(a * r);
  const f2 t = a * r + 1e-6f, u = a * (r * 1.4426950408889634f);
  return pk2(fmaxf(t[0], 1e-6f) + __builtin_amdgcn_fmed3f(__builtin_amdgcn_exp2f(u[0]), 0.f, 1.f),
             fmaxf(t[1], 1e-6f) + __builtin_amdgcn_fmed3f(__builtin_amdgcn_exp2f(u[1]), 0.f, 1.f));
}
DI float dot2c(u32 a, u32 b, float c) { return __builtin_amdgcn_fdot2_f32_bf16(__builtin_bit_cast(bf2, a), __builtin_bit_cast(bf2, b), c, false); }
DI u4 center8(u4 x, u32 lo, u32 hi, float nm) {
  u4 r; float t0, t1, t2, t3, t4, t5, t6, t7;
  asm("v_dot2_f32_bf16 %4, %12, %16, %18\n\tv_dot2_f32_bf16 %5, %12, %17, %18\n\tv_dot2_f32_bf16 %6, %13, %16, %18\n\tv_dot2_f32_bf16 %7, %13, %17, %18\n\t"
      "v_dot2_f32_bf16 %8, %14, %16, %18\n\tv_dot2_f32_bf16 %9, %14, %17, %18\n\tv_dot2_f32_bf16 %10, %15, %16, %18\n\tv_dot2_f32_bf16 %11, %15, %17, %18\n\t"
      "v_cvt_pk_bf16_f32 %0, %4, %5\n\tv_cvt_pk_bf16_f32 %1, %6, %7\n\tv_cvt_pk_bf16_f32 %2, %8, %9\n\tv_cvt_pk_bf16_f32 %3, %10, %11"
      : "=v"(r.x), "=v"(r.y), "=v"(r.z), "=v"(r.w), "=&v"(t0), "=&v"(t1), "=&v"(t2), "=&v"(t3), "=&v"(t4), "=&v"(t5), "=&v"(t6), "=&v"(t7)
      : "v"(x.x), "v"(x.y), "v"(x.z), "v"(x.w), "v"(lo), "v"(hi), "v"(nm));
  return r;
}
template <int N> DI void waitl() { asm volatile("" ::: "memory"); __builtin_amdgcn_s_waitcnt(0xC07F | (N << 8)); asm volatile("" ::: "memory"); }
DI void st128(u4 v, rsrc_t r, u32 voff, u32 soff) {
  __builtin_amdgcn_raw_buffer_store_b128(v, r, voff, soff, SC1);
  asm volatile("s_nop 1" ::"v"(v) : "memory");
}
DI void st64(u2 v, rsrc_t r, u32 voff, u32 soff) { __builtin_amdgcn_raw_buffer_store_b64(v, r, voff, soff, SC1); }
DI void publish20(const u32* v, rsrc_t r, u32 l16, u32 so, u32 j) {
  if ((j & 1) == 0) {
    st128(u4{v[0], v[1], v[2], v[3]}, r, l16, so);
    st128(u4{v[4], v[5], v[6], v[7]}, r, l16, so + 1024);
    st64(u2{v[8], v[9]}, r, l16 >> 1, so + 2048);
  } else {
    st64(u2{v[0], v[1]}, r, l16 >> 1, so + 2560);
    st128(u4{v[2], v[3], v[4], v[5]}, r, l16, so + 3072);
    st128(u4{v[6], v[7], v[8], v[9]}, r, l16, so + 4096);
  }
}
template <int OFF> DI void ldv2(u2& d, rsrc_t r, u32 voff, u32 soff) {
  asm volatile("buffer_load_dwordx2 %0, %1, %2, %3 offen offset:%4 sc1" : "=v"(d) : "v"(voff), "s"(r), "s"(soff), "n"(OFF) : "memory");
}
DI void mmh(f4& c, u4 a, u2 b0, u2 b1) { mm(c, a, u4{b0.x, b0.y, b1.x, b1.y}); }
template <int V, int L> DI void waitvl() {
  asm volatile("" ::: "memory"); __builtin_amdgcn_s_waitcnt(0x0070 | (V & 15) | ((V >> 4) << 14) | (L << 8)); asm volatile("" ::: "memory");
}

constexpr int d_issued(int u) {
  int k = DG * (u / 4 < 4 ? u / 4 : 4) - DB, e = u + DX0 + (k > 0 ? k : 0);
  if (u <= DLP && e > 16) e = 16;
  return e < 28 ? e : 28;
}
constexpr int d_younger(int u) {
  int n = 0;
  for (int v = u + 1; v < d_issued(u); v++) n += (v >= 16 && (v - 16) % 3 == 1) ? 2 : 1;
  return n;
}

__global__ __launch_bounds__(512, 2) void fused_block(const u16* __restrict__ x, const u16* __restrict__ wqkv, const u16* __restrict__ wo,
                                                      const u16* __restrict__ w1, const u16* __restrict__ b1, const u16* __restrict__ w2,
                                                      const u16* __restrict__ b2, u16* __restrict__ out, char* __restrict__ ws) {
  __shared__ __attribute__((aligned(1024))) char smem[163840];
  asm volatile("" ::"v"((u32)(uintptr_t)(LAS char*)smem));
  const u32 tid = threadIdx.x, lane = tid & 63, lr = lane & 15, g = lane >> 4;
  const u32 w = __builtin_amdgcn_readfirstlane(tid >> 6), mt = w & 3, nh = w >> 2;
  const bool helper = nh != 0;
  const rsrc_t rX = __builtin_amdgcn_make_buffer_rsrc((void*)x, 0, 2048 * 1280, 0x00020000);
  u4 xr[2][10];
  if (!helper) {
    const u32 p_ = ((blockIdx.x & 6) << 2) + (blockIdx.x >> 5);
    const u32 vx = (lane >> 3) * 1280 + (lane & 7) * 16, sx = (p_ * 64 + 16 * mt) * 1280;
    sfor<0, 10>([&](auto I) { ldp<I * 128>(xr[0][I], rX, vx, sx); });
    sfor<0, 10>([&](auto I) { ldp<I * 128>(xr[1][I], rX, vx, sx + 8 * 1280); });
  } else {
    sfor<0, 10>([&](auto I) { undef(xr[0][I]); undef(xr[1][I]); });
  }
  u32 b = blockIdx.x;
  asm volatile("" : "+s"(b) : "s"(wqkv), "s"(wo), "s"(w1), "s"(b1), "s"(w2), "s"(b2), "s"(out), "s"(ws) : "memory");
  const u32 xg = b & 7, rr = b >> 3, p = 8 * (xg >> 1) + (rr >> 2), j = 4 * (xg & 1) + (rr & 3);
  const rsrc_t rW = __builtin_amdgcn_make_buffer_rsrc((void*)ws, 0, WS_END, 0x00020000);
  const u32 l16 = lane * 16, l8 = lane * 8;
  u2 ep;
  u64 tag;
#define LD_EPOCH() asm volatile("buffer_load_dwordx2 %0, off, %1, 0 sc1" : "=v"(ep) : "s"(rW) : "memory")
#define GET_TAG() do { u32 lo_, hi_; asm volatile("v_readfirstlane_b32 %0, %2\n\tv_readfirstlane_b32 %1, %3\n\ts_nop 3" : "=s"(lo_), "=s"(hi_) : "v"(ep.x), "v"(ep.y) : "memory"); \
    tag = (((u64)hi_ << 32) | lo_) + 1; } while (0)
  u32 rot = xg + 3 * (p & 3), rotq = xg + (p & 3);
  rot = rot >= 10 ? rot - 10 : rot; rotq = rotq >= 10 ? rotq - 10 : (rotq >= 5 ? rotq - 5 : rotq);
  const u32 P = 4 * (xg >> 1) + (rr >> 3), c = 8 * (xg & 1) + (rr & 7), rho = c >> 1, odd = c & 1;
  const u32 t0 = rho < 6 ? 14 * rho + 8 * odd : 12 * rho + 12 + 6 * odd, tl = rho < 6 ? 14 * rho + 6 + odd : t0;
  const u32 s0 = w < 6 ? 7 * w : 6 * w + 6;
  const u32 vlast = w >= 6 ? OOB : l16 + 3072, vmid = w >= 6 ? l16 : l16 + 1024, vsp = w >= 6 ? l16 + 2048 : l16, soW2 = WS_W2T + (j * 5 * 54 + s0) * 1024;
  u4 w2f[35];
  auto ldw2 = [&](auto I) {
    constexpr int s = I / 5, n = I % 5;
    if constexpr (s < 2) ldv<s * 1024>(w2f[I], rW, l16, soW2 + n * 54 * 1024);
    else if constexpr (s < 4) ldv<(s - 2) * 1024>(w2f[I], rW, vmid, soW2 + (n * 54 + 3) * 1024);
    else if constexpr (s == 4) ldv<2048>(w2f[I], rW, l16, soW2 + n * 54 * 1024);
    else ldv<0>(w2f[I], rW, s == 5 ? vsp : vlast, soW2 + (n * 54 + 3) * 1024);
  };
#define RT_CHECK() do { waitv<0>(); landed(fr[0]); landed(fr[1]); const u32 tl32 = (u32)tag, th32 = (u32)(tag >> 32); \
    const u32 bad = ((fr[0].x ^ tl32) | (fr[0].z ^ tl32) | (fr[1].x ^ tl32) | (fr[1].z ^ tl32)) | ((fr[0].y ^ th32) | (fr[0].w ^ th32) | (fr[1].y ^ th32) | (fr[1].w ^ th32)); \
    if (__any(bad != 0)) { _Pragma("nounroll") for (u32 q = 0; q < 4; q++) poll(rW, WS_FRT + (64 * q + lane) * 8, tag); } } while (0)
  constexpr u32 L_RS = L_RING + 4096;

  if (!helper) {
    float rsd[2];
    const u32 ONE2 = opaque(0x3F803F80u);
    float dm[2];
    LD_EPOCH();
    const u32 xa0 = opaque(L_X + (lr >> 3) * 1024 + (lr & 7) * 128 + ((g ^ (lr & 7)) << 4));
    const u32 tr0 = opaque(L_RING + (4 * g + (lr >> 2)) * 160 + (lr & 3) * 8);
    const u32 mh = mt & 1, nq = mt >> 1;
    float qrs;
    u4 xq[20];
    {
      f4 acc[2][5];
#pragma unroll
      for (int i = 0; i < 5; i++) { zero(acc[0][i]); zero(acc[1][i]); }
      const u32 bA = opaque(tr0 + nq * 96), bC = opaque(tr0 + 64 + nq * 5120);
      const u32 xs0 = opaque(xa0 + mh * 4096);
      u32 lkv[6];
#pragma unroll
      for (int q = 0; q < 2; q++) {
        const u32 dq = 4 * q + g - mt, e0 = lr - 4 * mt, e1 = e0 + 16, sb = L_RING + mt * 5120;
        lkv[3 * q] = opaque(sb + ((dq < 5 && e0 < 20) ? e0 * 40 + dq * 8 : 896));
        lkv[3 * q + 1] = opaque(sb + ((dq < 5 && e1 < 20) ? e1 * 40 + dq * 8 : 896));
        lkv[3 * q + 2] = opaque(sb + ((dq < 5 && lr == 0) ? 800 + dq * 8 : (dq == 0 && lr == 1) ? 840 : 896));
      }
      {
        const u32 wa = opaque(L_X + mt * 2048 + (lane >> 3) * 128 + (((lane & 7) ^ (lane >> 3)) << 4));
        const u32 lo = opaque(0x00003F80u), hi = opaque(0x3F800000u);
        sfor<0, 2>([&](auto G) {
          waitv<(G == 0 ? 11 : 1)>();
          float s0 = 0.f, s1 = 0.f;
#pragma unroll
          for (int i = 0; i < 10; i++) {
            landed(xr[G][i]);
            s0 = dot2c(xr[G][i].x, ONE2, s0); s1 = dot2c(xr[G][i].y, ONE2, s1); s0 = dot2c(xr[G][i].z, ONE2, s0); s1 = dot2c(xr[G][i].w, ONE2, s1);
          }
          const float m = xsum8(s0 + s1) * (1.f / 640.f), nm = -rbf(m);
          dm[G] = m + nm;
#pragma unroll
          for (int i = 0; i < 10; i++) { xr[G][i] = center8(xr[G][i], lo, hi, nm); sts128(wa + G * 1024 + i * 8192, xr[G][i]); }
        });
      }
      auto half = [&](u4* cur, u4* nxt, u32 xa, u32 a, u32 c, auto KS, auto LD) {
        constexpr int o = KS * 10240;
        mm(acc[0][0], cur[5], cur[0]);
        if constexpr (LD) nxt[5] = lds128(xa);
        mm(acc[1][0], cur[6], cur[0]);
        if constexpr (LD) nxt[6] = lds128(xa + 2048);
        mm(acc[0][1], cur[5], cur[1]);
        if constexpr (LD) nxt[0] = ldstr(a + o);
        mm(acc[1][1], cur[6], cur[1]);
        if constexpr (LD) nxt[1] = ldstr(a + o + 32);
        mm(acc[0][2], cur[5], cur[2]);
        if constexpr (LD) nxt[2] = ldstr(c + o);
        mm(acc[1][2], cur[6], cur[2]);
        if constexpr (LD) nxt[3] = ldstr(a + o + 5120);
        mm(acc[0][3], cur[5], cur[3]);
        if constexpr (LD) nxt[4] = ldstr(a + o + 5152);
        mm(acc[1][3], cur[6], cur[3]);
        mm(acc[0][4], cur[5], cur[4]); mm(acc[1][4], cur[6], cur[4]);
      };
      u4 fa[7], fb[7];
      u32 st = rot, xa = xs0 + st * 8192, a = bA, cc = bC;
      BAR();
      fa[5] = lds128(xa); fa[6] = lds128(xa + 2048); fa[0] = ldstr(a); fa[1] = ldstr(a + 32); fa[2] = ldstr(cc);
      fa[3] = ldstr(a + 5120); fa[4] = ldstr(a + 5152);
      sfor<0, 2>([&](auto G) {
        float s0 = 0.f, s1 = 0.f;
#pragma unroll
        for (int i = 0; i < 10; i++) {
          landed(xr[G][i]);
          const u4 v = xr[G][i];
          s0 = dot2c(v.x, v.x, s0); s1 = dot2c(v.y, v.y, s1); s0 = dot2c(v.z, v.z, s0); s1 = dot2c(v.w, v.w, s1);
        }
        const float var = fmaxf(xsum8(s0 + s1) * (1.f / 640.f) - dm[G] * dm[G], 0.f);
        rsd[G] = __builtin_amdgcn_rcpf(rbf(__builtin_amdgcn_sqrtf(rbf(rbf(var) + 1e-5f))));
        landed(rsd[G]);
      });
      waitv<0>();
      GET_TAG();
#pragma nounroll
      for (u32 i = 1; i < 10; i++) {
        waitl<0>();
        half(fa, fb, xa ^ 64, a, cc, IC<1>{}, IC<1>{});
        st = st + 1 >= 10 ? 0 : st + 1;
        xa = opaque(xs0 + st * 8192); a = opaque(bA + (i & 3) * 20480); cc = opaque(bC + (i & 3) * 20480);
        BAR();
        half(fb, fa, xa, a, cc, IC<0>{}, IC<1>{});
      }
      if ((lane & 7) == 0) { sts32(L_RS + mt * 64 + (lane >> 3) * 4, rsd[0]); sts32(L_RS + mt * 64 + 32 + (lane >> 3) * 4, rsd[1]); }
      waitl<0>();
      half(fa, fb, xa ^ 64, a, cc, IC<1>{}, IC<1>{});
      {
        u32 qs = rotq;
#pragma unroll
        for (int q = 0; q < 5; q++) {
          const u32 xb = xa0 + mt * 2048 + qs * 16384, xc = xb ^ 64;
          xq[4 * q] = lds128(xb); xq[4 * q + 1] = lds128(xc); xq[4 * q + 2] = lds128(xb + 8192); xq[4 * q + 3] = lds128(xc + 8192);
          qs = qs + 1 >= 5 ? 0 : qs + 1;
        }
      }
      BAR();
      const f4 r0 = __builtin_bit_cast(f4, lds128(L_RS + mh * 128 + g * 16)), r1 = __builtin_bit_cast(f4, lds128(L_RS + mh * 128 + 64 + g * 16));
      qrs = *(LAS float*)(L_RS + mt * 64 + lr * 4);
      half(fb, fa, 0, 0, 0, IC<0>{}, IC<0>{});
      MM_DONE();
      const u32 EX = L_RING + 20480;
      const u32 ew = EX + mh * 10240 + nq * 3072 + l16;
      auto put = [&](auto N, auto PHI, u32 a) {
        constexpr int n = N;
        pin(acc[0][n]); pin(acc[1][n]);
        sts128(a, u4{phi2<PHI>(acc[0][n][0], acc[0][n][1], r0[0], r0[1]), phi2<PHI>(acc[0][n][2], acc[0][n][3], r0[2], r0[3]),
                     phi2<PHI>(acc[1][n][0], acc[1][n][1], r1[0], r1[1]), phi2<PHI>(acc[1][n][2], acc[1][n][3], r1[2], r1[3])});
      };
      put(IC<0>{}, IC<1>{}, ew); put(IC<1>{}, IC<1>{}, ew + 1024); put(IC<3>{}, IC<0>{}, ew + 5120); put(IC<4>{}, IC<0>{}, ew + 6144);
      if (nq == 0) put(IC<2>{}, IC<1>{}, ew + 2048); else put(IC<2>{}, IC<0>{}, ew + 4096);
      BAR();
      const u32 ex = EX + mt * 1024 + l16;
      u4 kf[2][2], vf[2][2];
#pragma unroll
      for (int m = 0; m < 2; m++) {
        kf[m][0] = lds128(ex + m * 10240); vf[m][0] = lds128(ex + m * 10240 + 5120);
        vf[m][1] = lds128(ex + m * 10240 + 6144); kf[m][1] = lds128(ex + m * 10240 + 1024);
      }
      const u32 o0 = lr == 0 ? ONE2 : 0u;
      const u4 ONE0 = {o0, o0, o0, o0};
#pragma unroll
      for (int q = 0; q < 2; q++) {
        f4 z4 = {0, 0, 0, 0};
        f4 kv0 = mfma(kf[1][q], vf[1][0], mfma(kf[0][q], vf[0][0], z4)), kv1 = mfma(kf[1][q], vf[1][1], mfma(kf[0][q], vf[0][1], z4));
        f4 zz = mfma(kf[1][q], ONE0, mfma(kf[0][q], ONE0, z4));
        sts64(lkv[3 * q], u2{pk2(kv0[0], kv0[1]), pk2(kv0[2], kv0[3])});
        sts64(lkv[3 * q + 1], u2{pk2(kv1[0], kv1[1]), pk2(kv1[2], kv1[3])});
        sts64(lkv[3 * q + 2], u2{pk2(zz[0], zz[1]), pk2(zz[2], zz[3])});
      }
      asm volatile("" ::: "memory");
      st128(lds128(L_RING + mt * 5120 + l16), rW, lane < 56 ? l16 : OOB, WS_KVP + ((j * 32 + p) * 4 + mt) * 896);
    }
    u32 pq[5][2];
    u2 xres[5];
    {
      const u32 qa0 = opaque(tr0 + 2 * 20480), qa1 = opaque(tr0 - L_RING);
      u4 wf[2][5];
#pragma unroll
      for (int n = 0; n < 5; n++) wf[0][n] = ldstr(qa0 + n * 32);
      waitv<0>();
      setflag(ws, WS_FKV + ((j * 4 + mt) * 32 + p) * 8, tag, lane);
      sfor<0, 5>([&](auto N) { ldp2<N * 32>(xres[N], rX, lr * 1280 + g * 8, (p * 64 + mt * 16) * 1280 + j * 160); });
      f4 qa[5];
      sfor<0, 20>([&](auto S) {
        constexpr int s = S, t = s + 1;
        if constexpr (s == 7) BAR();
        waitl<6>();
        sfor<0, 5>([&](auto N) {
          constexpr int n = N;
          if constexpr (n == 2) waitl<(s < 19 ? 4 : 0)>();
          if constexpr (s == 0) mm0(qa[n], wf[0][n], xq[0]); else mm(qa[n], wf[s & 1][n], xq[s]);
          if constexpr (s < 19) wf[t & 1][n] = ldstr((t < 8 ? qa0 + t * 5120 : qa1 + (t - 8) * 5120) + n * 32);
        });
      });
      MM_DONE();
#pragma unroll
      for (int n = 0; n < 5; n++) {
        pin(qa[n]);
        pq[n][0] = opaque(phi2<1>(qa[n][0], qa[n][1], qrs, qrs)); pq[n][1] = opaque(phi2<1>(qa[n][2], qa[n][3], qrs, qrs));
      }
    }
    __builtin_amdgcn_s_setprio(2);
    BAR();
    {
      float o[20];
#pragma unroll
      for (int h = 0; h < 4; h++) {
        u4 bq = {pq[h][0], pq[h][1], pq[h + 1][0], pq[h + 1][1]};
        f4 z4 = {0, 0, 0, 0};
        f4 d0 = mfma(lds128(L_IMG + h * 2048 + l16), bq, z4);
        f4 d1 = mfma(lds128(L_IMG + h * 2048 + 1024 + l16), bq, z4);
        float rd = __builtin_amdgcn_rcpf(d1[1] + 1e-6f);
#pragma unroll
        for (int i = 0; i < 4; i++) o[5 * h + i] = d0[i] * rd;
        o[5 * h + 4] = d1[0] * rd;
      }
      u32 ov[10];
#pragma unroll
      for (int i = 0; i < 10; i++) ov[i] = pk2(o[2 * i], o[2 * i + 1]);
      publish20(ov, rW, l16, WS_OB + ((p * 4 + mt) * 20 + 5 * (j >> 1)) * 1024, j);
    }
    waitv<0>();
    setflag(ws, WS_FO + ((p * 4 + mt) * 8 + j) * 8, tag, lane);
#pragma unroll
    for (int n = 0; n < 5; n++) landed(xres[n]);
    BAR();
    {
      u4 of[20], wf[2][5];
      u2 oh[4][2];
      const u32 so = WS_OB + (p * 4 + mt) * 20480;
      auto ldo = [&](auto S) {
        constexpr int s = S;
        if constexpr (s % 5 == 2) { ldv2<(s & 3) * 1024>(oh[s / 5][0], rW, l8, so + (s & ~3) * 1024); ldv2<(s & 3) * 1024 + 512>(oh[s / 5][1], rW, l8, so + (s & ~3) * 1024); }
        else ldv<(s & 3) * 1024>(of[s], rW, l16, so + (s & ~3) * 1024);
      };
      const u32 a0 = opaque(L_WO + l16), a1 = opaque(L_WO + 51200 + l16);
#pragma unroll
      for (int q = 0; q < 2; q++) {
#pragma unroll
        for (int n = 0; n < 5; n++) wf[q][n] = lds128(a0 + n * 10240 + q * 1024);
        asm volatile("" ::: "memory");
      }
      poll(rW, WS_FO + ((p * 4 + mt) * 8 + (lane & 7)) * 8, tag);
      sfor<0, OP>(ldo);
      f4 ha[1][5];
      u4 pk_;
      sfor<0, 20>([&](auto S) {
        constexpr int s = S, e = OP + 2 * s < 20 ? OP + 2 * s : 20, f = OP + 2 + 2 * s < 20 ? OP + 2 + 2 * s : 20;
        constexpr int yo = (e - s - 1) + ((e + 2) / 5 - (s + 3) / 5);
        if constexpr (s == 7) pk_ = lpeek(2);
        if constexpr (s == 8) lcheck(pk_, 2);
        if constexpr (s == 10) lset(1, mt);
        waitvl<yo, (s == 7 ? 6 : s < 19 && s != 10 ? 5 : 0)>();
        sfor<0, 5>([&](auto N) {
          constexpr int n = N, t = s + 2;
          if constexpr (s == 0) mm0(ha[0][n], wf[0][n], of[s]);
          else if constexpr (s % 5 == 2) mmh(ha[0][n], wf[s & 1][n], oh[s / 5][0], oh[s / 5][1]);
          else mm(ha[0][n], wf[s & 1][n], of[s]);
          if constexpr (t < 20) wf[s & 1][n] = lds128((t < 10 ? a0 : a1) + n * 10240 + (t % 10) * 1024);
          if constexpr (n == 1 && e < f) ldo(IC<e>{});
          if constexpr (n == 3 && e + 1 < f) ldo(IC<e + 1>{});
        });
      });
      MM_DONE();
#pragma unroll
      for (int n = 0; n < 5; n++) pin(ha[0][n]);
      u32 hres[10];
      float sum = 0.f, sq = 0.f;
#pragma unroll
      for (int n = 0; n < 5; n++) {
        hres[2 * n] = pk2(unp(xres[n].x) + f2{ha[0][n][0], ha[0][n][1]});
        hres[2 * n + 1] = pk2(unp(xres[n].y) + f2{ha[0][n][2], ha[0][n][3]});
      }
      publish20(hres, rW, l16, WS_HB + ((p * 4 + mt) * 20 + 5 * (j >> 1)) * 1024, j);
#pragma unroll
      for (int i = 0; i < 10; i++) { sum = dot2c(hres[i], ONE2, sum); sq = dot2c(hres[i], hres[i], sq); }
      st64(u2{asu(xsum16_32(sum)), asu(xsum16_32(sq))}, rW, g == 0 ? l8 : OOB, WS_ST + ((p * 4 + mt) * 8 + j) * 128);
#pragma unroll
      for (int n = 0; n < 5; n++) sts64(L_HR + (mt * 5 + n) * 512 + l8, u2{hres[2 * n], hres[2 * n + 1]});
      pk_ = lpeek(4);
      waitv<0>();
      setflag(ws, WS_FH + ((p * 4 + mt) * 8 + j) * 8, tag, lane);
      lset(3, mt);
      lcheck(pk_, 4);
    }
    {
      constexpr int HD = HDV;
      const u32 pc = 2 * P + (mt >> 1), mc = 2 * (mt & 1);
      f4 ca[2][7];
      u4 hf[2][20], wf[7];
      u2 hh[2][4][2], s0v, s1v;
      u4 pk_;
      u32 pk1;
      const u32 so = WS_HB + (pc * 4 + mc) * 20480;
      auto ldh = [&](auto S) {
        constexpr int s = S;
        sfor<0, 2>([&](auto M) {
          if constexpr (s % 5 == 2) {
            ldv2<(s & 3) * 1024>(hh[M][s / 5][0], rW, l8, so + M * 20480 + (s & ~3) * 1024);
            ldv2<(s & 3) * 1024 + 512>(hh[M][s / 5][1], rW, l8, so + M * 20480 + (s & ~3) * 1024);
          } else ldv<(s & 3) * 1024>(hf[M][s], rW, l16, so + M * 20480 + (s & ~3) * 1024);
        });
      };
      const u32 a0 = opaque(L_W1A + l16), a1 = opaque(L_W1B + l16), a2 = opaque(L_W1B + 35840 + l16), a3 = opaque(L_W1B + 71680 + l16);
#pragma unroll
      for (int n = 0; n < 7; n++) wf[n] = lds128(a0 + n * 5120);
      poll(rW, WS_FH + ((pc * 4 + mc) * 8 + lr) * 8, tag);
      sfor<0, 3>(ldh);
      sfor<0, 20>([&](auto S) {
        constexpr int s = S, e1 = 3 + 2 * s < s + HD ? 3 + 2 * s : s + HD, e = e1 < 20 ? e1 : 20;
        constexpr int f1 = 5 + 2 * s < s + 1 + HD ? 5 + 2 * s : s + 1 + HD, f = f1 < 20 ? f1 : 20;
        constexpr int yo = 2 * ((e - s - 1) + ((e + 2) / 5 - (s + 3) / 5));
        if constexpr (s == 8) pk_ = lpeek(5);
        if constexpr (s == 9) lcheck(pk_, 5);
        if constexpr (s == 17) pk1 = *(volatile LAS u32*)(L_CB + 1536 + 96 + mt * 4);
        if constexpr (s == 18) { if (__builtin_amdgcn_readfirstlane(pk1) == 0) lwait1(6, mt); }
        if constexpr (s == 19) { s0v = lds64(L_CB + 2048 + mt * 256 + lr * 8); s1v = lds64(L_CB + 2048 + mt * 256 + 128 + lr * 8); }
        waitvl<yo, (s == 8 || s == 17 ? 4 : s == 19 ? 5 : 3)>();
        sfor<0, 7>([&](auto N) {
          constexpr int n = N, t = s + 1;
          if constexpr (n == 4) waitl<(s < 19 ? 4 : 0)>();
          sfor<0, 2>([&](auto M) {
            if constexpr (s == 0) mm0(ca[M][n], wf[n], hf[M][s]);
            else if constexpr (s % 5 == 2) mmh(ca[M][n], wf[n], hh[M][s / 5][0], hh[M][s / 5][1]);
            else mm(ca[M][n], wf[n], hf[M][s]);
          });
          if constexpr (s < 19) wf[n] = lds128((t < 5 ? a0 : t < 10 ? a1 : t < 15 ? a2 : a3) + n * 5120 + (t % 5) * 1024);
          if constexpr (n == 1 && e < f) ldh(IC<e>{});
          if constexpr (n == 4 && e + 1 < f) ldh(IC<e + 1>{});
        });
      });
      MM_DONE();
      lset(7, mt);
      const u32 sh = WS_HID + (pc * 4 + mc) * 54 * 1024, fh = WS_FHID + (((pc * 16 + c) * 2 + (mt & 1)) * 2) * 8;
      const float sc[2] = {asf(s0v.x), asf(s1v.x)}, sm[2] = {asf(s0v.y), asf(s1v.y)};
      u32 hp[2][2][2];
      sfor<0, 7>([&](auto N) {
        constexpr int n = N;
        const f4 c4 = __builtin_bit_cast(f4, lds128(L_CB + n * 64 + g * 16)), b4 = __builtin_bit_cast(f4, lds128(L_CB + 512 + n * 64 + g * 16));
        if constexpr (n == 6) { waitv<2>(); setflag(ws, fh, tag, lane); }
        sfor<0, 2>([&](auto M) {
          pin(ca[M][n]);
#pragma unroll
          for (int i = 0; i < 2; i++) {
            const f2 v = f2{ca[M][n][2 * i], ca[M][n][2 * i + 1]} * sc[M] + (f2{b4[2 * i], b4[2 * i + 1]} - sm[M] * f2{c4[2 * i], c4[2 * i + 1]});
            const f2 u = v * (-2.302208198f - 0.1029432397f * v * v);
            const f2 d = f2{__builtin_amdgcn_exp2f(u[0]), __builtin_amdgcn_exp2f(u[1])} + 1.f;
            hp[M][n & 1][i] = pk2(v * f2{__builtin_amdgcn_rcpf(d[0]), __builtin_amdgcn_rcpf(d[1])});
          }
          if constexpr (n & 1) st128(u4{hp[M][0][0], hp[M][0][1], hp[M][1][0], hp[M][1][1]}, rW, l16, sh + M * 54 * 1024 + ((t0 >> 1) + n / 2) * 1024);
          if constexpr (n == 6) st64(u2{hp[M][0][0], hp[M][0][1]}, rW, rho < 6 ? l8 + odd * 512 : OOB, sh + M * 54 * 1024 + (tl >> 1) * 1024);
        });
      });
      waitv<0>();
      setflag(ws, fh + 8, tag, lane);
      __builtin_amdgcn_s_setprio(0);
      lwait1(8, mt);
      const u32 wa = opaque(mt * 35840 + l16);
#pragma unroll
      for (int i = 0; i < 35; i++) w2f[i] = lds128(wa + i * 1024);
      lset(9, mt);
    }
  } else {
    LD_EPOCH();
    u4 sel[2];
    u32 dst[2];
    auto setup = [&]() {
#pragma unroll
    for (int q = 0; q < 2; q++) {
      const u32 k = lr + 16 * q - 8 * g, one = (k & 1) ? 0x3F800000u : 0x00003F80u;
      sel[q] = u4{k < 2 ? one : 0u, (k >> 1) == 1 ? one : 0u, (k >> 1) == 2 ? one : 0u, (k >> 1) == 3 ? one : 0u};
      landed(sel[q]);
    }
#pragma unroll
    for (int h = 0; h < 2; h++) {
      const u32 qd = 2 * (lr + 16 * (2 * h + (g >> 1))) + (g & 1), e = (qd * 205) >> 10, dq = qd - e * 5;
      const u32 gs = dq + mt, grp = (e * 13) >> 6, ii = e - grp * 5;
      const u32 base = L_IMG + mt * 2048 + (gs & 3) * 256 + (gs >> 2) * 8;
      dst[h] = opaque(e < 20 ? base + (ii < 4 ? (4 * grp + ii) * 16 : 1024 + 4 * grp * 16) : (e == 20 ? base + 1024 + 16 : L_CB + 1792));
    }
    };
    if (mt < 3) {
      const rsrc_t rQ = __builtin_amdgcn_make_buffer_rsrc((void*)((const char*)wqkv - 4096), 0, 640 * 3840 + 4096, 0x00020000);
      u32 voffW[7];
#pragma unroll
      for (int i = 0; i < 7; i++) {
        const u32 pi = i < 5 ? i : (i == 5 ? mt : (mt < 2 ? mt + 3 : 4u));
        const u32 f = 64 * pi + lane, sg = (f * 6554) >> 16, ch = f - sg * 10;
        voffW[i] = (((sg & 12) << 1) + ((sg >> 4) << 2) + (sg & 3)) * 3840 + ch * 16 + 4096 - (i < 4 ? i * 1024 : 0);
      }
      const u32 soW = (mt >> 1) * 32 * 3840 + ((mt & 1) + 1) * 1280 + j * 160, soW3 = 32 * 3840 + 2560 + j * 160;
      const u32 soQ = mt * 32 * 3840 + j * 160, soQ3 = 96 * 3840 + j * 160;
      const u32 l5 = 15360 + mt * 1024, l6 = 15360 + (mt < 2 ? mt + 3 : 4u) * 1024;
      u32 st = rot, qs = rotq;
      auto issueW = [&](u32 so, u32 so3, u32 ld) {
        dma<0>(rQ, voffW[0], so, ld + mt * 5120); dma_c<1024>(rQ, voffW[1], so); dma_c<2048>(rQ, voffW[2], so); dma_c<3072>(rQ, voffW[3], so);
        dma<0>(rQ, voffW[4], so, ld + mt * 5120 + 4096); dma<0>(rQ, voffW[5], so3, ld + l5);
        if (mt < 2) dma<0>(rQ, voffW[6], so3, ld + l6);
      };
      auto issueKV = [&](u32 slot) { issueW(soW + st * 245760, soW3 + st * 245760, L_RING + slot * 20480); st = st + 1 >= 10 ? 0 : st + 1; };
      auto issueQ = [&](u32 ld) { issueW(soQ + qs * 491520, soQ3 + qs * 491520, ld); qs = qs + 1 >= 5 ? 0 : qs + 1; };
      issueKV(0); issueKV(1); issueKV(2);
      waitv<12>();
      BAR();
      issueKV(3);
#pragma nounroll
      for (u32 i = 0; i < 6; i++) { waitv<12>(); asm volatile("s_barrier" ::: "memory"); issueKV(i & 3); }
      auto issueQa = [&](u32 slot) {
        const u32 so = soQ + qs * 491520;
        dma<0>(rQ, voffW[0], so, L_RING + slot * 20480 + mt * 5120); dma_c<1024>(rQ, voffW[1], so); dma_c<2048>(rQ, voffW[2], so);
      };
      auto issueQb = [&](u32 slot) {
        const u32 so = soQ + qs * 491520, so3 = soQ3 + qs * 491520, ld = L_RING + slot * 20480;
        dma<3072>(rQ, voffW[3], so, ld + mt * 5120); dma<0>(rQ, voffW[4], so, ld + mt * 5120 + 4096); dma<0>(rQ, voffW[5], so3, ld + l5);
        if (mt < 2) dma<0>(rQ, voffW[6], so3, ld + l6);
        qs = qs + 1 >= 5 ? 0 : qs + 1;
      };
      waitv<12>(); BAR(); issueQa(2);
      waitv<9>(); BAR(); issueQb(2);
      waitv<6>(); BAR(); issueQa(3);
      BAR();
      issueQb(3);
      issueQ(0);
      waitv<6>();
      BAR();
      GET_TAG();
      setup();
      waitv<TQ>(); issueQ(20480); waitv<TQ>(); issueQ(40960);
    } else {
      const bool m1 = b < 108, mo = b >= 216;
      const u32 v = m1 ? b : (mo ? b - 216 : b - 108), vq = m1 ? (v * 2428) >> 16 : (v * 6554) >> 16;
      const u32 sA = uni((m1 || mo) ? 5 * vq : v >> 1), cA = uni(m1 ? v - 27 * vq : (mo ? v - 10 * vq : 5 * (v & 1))), sQ = uni((m1 || mo) ? 1u : 0u);
      const u32 pitch = uni(m1 ? 3456 : 1280);
      const u64 src = m1 ? (u64)w1 : (mo ? (u64)wo : (u64)w2);
      const rsrc_t rS = __builtin_amdgcn_make_buffer_rsrc((void*)src, 0, mo ? 640 * 1280 : 640 * 3456, 0x00020000);
      u32 vrt[20];
#pragma unroll
      for (int I = 0; I < 20; I++) {
        const u32 q = I >> 2, i = I & 3, ro = 8 * i + (lane >> 3);
        const u32 i40 = 8 * q + 4 * (i >> 1) + (ro & 3), hf = i40 >= 20 ? 1u : 0u, id = i40 - 20 * hf;
        const u32 rp = opaque(80 * hf + 15 * ((id * 13) >> 6) + 5 * ((ro >> 2) & 3) + id);
        vrt[I] = opaque((mo ? rp : 32 * sQ * q + ro) * pitch + (lane & 7) * 16 + (q - sQ * q) * 128);
      }
      u4 ONES = {0x3F803F80u, 0x3F803F80u, 0x3F803F80u, 0x3F803F80u};
      landed(ONES);
      const u32 sc = L_RING + 16384;
      const u32 wa = sc + (lane >> 3) * 128 + (((lane & 7) ^ (((lane >> 4) & 3) << 1)) << 4);
      const u32 r0 = 4 * g + (lr >> 2), ta = opaque(sc + r0 * 128 + (lr & 1) * 8 + ((((lr >> 1) & 1) ^ (((r0 >> 1) & 3) << 1)) << 4));
      const u32 dT = uni(m1 ? 20480u : (mo ? 10240u : 55296u)), dQ = uni(sQ ? 1024u : 4 * 55296u);
      const u32 dA = uni((m1 ? WS_W1T : (mo ? WS_WOT + (sA >= 10 ? 40960u : 0u) : WS_W2T)) + 4 * cA * dT + sA * 1024);
      u32 dJ[4];
#pragma unroll
      for (int n = 0; n < 4; n++) dJ[n] = uni(dA + n * dT + (mo ? (((4 * cA + n) * 52) >> 8) * 51200u : 0u));
      waitv<0>();
      GET_TAG();
      setup();
#pragma nounroll
      for (u32 i = 0; i < 8; i++) BAR();
      u4 f[5][4];
      sfor<0, 7>([&](auto I) { ldp<0>(f[I >> 2][I & 3], rS, vrt[I], 32 * sA * pitch + cA * 128); });
      BAR();
      sfor<7, 14>([&](auto I) { ldp<0>(f[I >> 2][I & 3], rS, vrt[I], 32 * sA * pitch + cA * 128); });
      BAR();
      sfor<14, 20>([&](auto I) { ldp<0>(f[I >> 2][I & 3], rS, vrt[I], 32 * sA * pitch + cA * 128); });
      BAR();
      f4 cs[4];
      sfor<0, 6>([&](auto Q) {
        constexpr int q = Q;
        if constexpr (q < 5) {
          waitv<(q == 0 ? 16 : 12)>();
#pragma unroll
          for (int i = 0; i < 4; i++) { landed(f[q][i]); sts128(wa + i * 1024, f[q][i]); }
          asm volatile("" ::: "memory");
#pragma unroll
          for (int n = 0; n < 4; n++) f[q][n] = ldstr(ta ^ (n << 5), (ta ^ (n << 5)) + 2048);
          asm volatile("" ::: "memory");
        }
        if constexpr (q > 0) {
#pragma unroll
          for (int n = 0; n < 4; n++) {
            st128(f[q - 1][n], rW, l16, dJ[n] + (q - 1) * dQ);
            cs[n] = mfma(f[q - 1][n], ONES, q == 1 ? f4{0, 0, 0, 0} : cs[n]);
          }
        }
      });
#pragma unroll
      for (int n = 0; n < 4; n++) st128(__builtin_bit_cast(u4, cs[n]), rW, (m1 && lr == 0) ? g * 16 : OOB, WS_CS1 + vq * 6912 + (4 * cA + n) * 64);
      BAR();
    }
    if (mt < 3) waitv<0>();
    BAR();
    if (mt == 3) { waitv<0>(); setflag(ws, WS_FRT + b * 8, tag, lane); }
    poll(rW, WS_FKV + ((j * 4 + mt) * 32 + (lane & 31)) * 8, tag);
    {
      const u32 so = WS_KVP + (j * 32 * 4 + mt) * 896;
      const u32 v0 = lane < 53 ? l16 : OOB;
      u4 a[32], fr[2];
      auto lda = [&](auto Q) { ldv<(Q & 1) * 3584>(a[Q], rW, v0, so + (Q & ~1) * 3584); };
      sfor<0, 14>(lda);
      sts128(L_IMG + mt * 2048 + l16, u4{0, 0, 0, 0}); sts128(L_IMG + mt * 2048 + 1024 + l16, u4{0, 0, 0, 0});
      if (lane < 12) sts128(L_CB + 1536 + l16, u4{0, 0, 0, 0});
      const bool zrow = (dst[1] & 0x430) == 0x410 && dst[1] < L_CB;
      f4 s0, s1, s2, s3;
      sfor<0, 16>([&](auto Q) {
        waitv<(Q < 9 ? 12 : 32 - 2 * Q)>();
        if constexpr (Q == 0) { mm0(s0, sel[0], a[0]); mm0(s1, sel[1], a[0]); mm0(s2, sel[0], a[1]); mm0(s3, sel[1], a[1]); }
        else { mm(s0, sel[0], a[2 * Q]); mm(s1, sel[1], a[2 * Q]); mm(s2, sel[0], a[2 * Q + 1]); mm(s3, sel[1], a[2 * Q + 1]); }
        if constexpr (Q < 9) { lda(IC<14 + 2 * Q>{}); lda(IC<15 + 2 * Q>{}); }
        if constexpr (Q == 8) { ldv<0>(fr[0], rW, lane * 32, WS_FRT); ldv<16>(fr[1], rW, lane * 32, WS_FRT); }
      });
      MM_DONE();
      pin(s0); pin(s1); pin(s2); pin(s3);
      s0 += s2; s1 += s3;
      const u2 v1 = {pk2(s1[0], s1[1]), pk2(s1[2], s1[3])};
      sts64(dst[0], u2{pk2(s0[0], s0[1]), pk2(s0[2], s0[3])});
      sts64(dst[1], v1);
      if (zrow) { sts64(dst[1] + 64, v1); sts64(dst[1] + 128, v1); sts64(dst[1] + 192, v1); }
      BAR();
      RT_CHECK();
    }
    auto issueW1 = [&](u32 it, u32 n, u32 ld) {
      const u32 so = WS_W1T + (n < 6 ? t0 + n : tl) * 20480 + it * 5120;
      dma4_sc1(rW, l16, so, ld + n * 5120); dma1_sc1(rW, l16, so + 4096, ld + n * 5120 + 4096);
    };
    const u32 o = 51200 + mt * 12288, o2 = 51200 + 49152 + (mt & 1) * 1024, sw = WS_WOT + j * 102400;
    {
      const u32 p0 = mt * 12288, p2 = 49152 + (mt & 1) * 1024;
      dma4_sc1(rW, l16, sw + p0, L_WO + p0); dma4_sc1(rW, l16, sw + p0 + 4096, L_WO + p0 + 4096); dma4_sc1(rW, l16, sw + p0 + 8192, L_WO + p0 + 8192);
      dma1_sc1(rW, l16, sw + p2, L_WO + p2);
    }
    if (b == 0 && tid == 256) __hip_atomic_store((GAS u64*)ws, tag, __ATOMIC_RELAXED, __HIP_MEMORY_SCOPE_AGENT);
    {
      f4 csum;
      u32 ci = lane + 64 * (mt & 1); ci = ci > 111 ? 111 : ci;
      const u32 n = ci >> 4, col = (n < 6 ? t0 + n : tl) * 16 + (ci & 15);
#pragma unroll
      for (int q = 0; q < 4; q++) csum[q] = asf(__builtin_amdgcn_raw_buffer_load_b32(rW, col * 4, WS_CS1 + q * 6912, SC1));
      const u32 bv = b1[col], b2v = ((const u32*)b2)[40 * j + (lane < 40 ? lane : 0)];
      sts32(L_CB + ci * 4, (csum[0] + csum[1]) + (csum[2] + csum[3]));
      sts32(L_CB + 512 + ci * 4, blo(bv));
      if (lane < 40) *(LAS u32*)(L_CB + 1024 + lane * 4) = b2v;
    }
    waitv<0>();
    BAR();
    auto w1t = [&](u32 it, u32 n, u32 ld) { waitv<TH>(); issueW1(it, n, ld); };
    auto w2t = [&](auto I) { waitv<TH + 3>(); ldw2(I); };
    dma4_sc1(rW, l16, sw + o, L_WO + o); waitv<TH>(); dma4_sc1(rW, l16, sw + o + 4096, L_WO + o + 4096);
    waitv<TH>(); dma4_sc1(rW, l16, sw + o + 8192, L_WO + o + 8192); dma1_sc1(rW, l16, sw + o2, L_WO + o2);
    w1t(0, mt, L_W1A);
    waitv<5>(); lset(2, mt);
    w1t(0, mt < 3 ? mt + 4 : 6, L_W1A);
    sfor<0, HA>(w2t);
    lwait(1);
    w1t(1, mt, L_W1B); w1t(1, mt < 3 ? mt + 4 : 6, L_W1B);
    sfor<HA, HA + 4>(w2t);
    waitv<4>(); lset(4, mt);
    sfor<HA + 4, HB2>(w2t);
    lwait(3);
    w1t(2, mt, L_W1B + 35840); w1t(2, mt < 3 ? mt + 4 : 6, L_W1B + 35840);
    {
      const u32 pc = 2 * P + (mt >> 1), mc = 2 * (mt & 1);
      poll(rW, WS_FH + ((pc * 4 + mc) * 8 + lr) * 8, tag);
      u2 sv[2][2];
      sfor<0, 2>([&](auto M) {
        ldv2<M * 1024>(sv[M][0], rW, g * 256 + lr * 8, WS_ST + (pc * 4 + mc) * 1024); ldv2<M * 1024 + 128>(sv[M][1], rW, g * 256 + lr * 8, WS_ST + (pc * 4 + mc) * 1024);
      });
      issueW1(3, mt, L_W1B + 71680); w1t(3, mt < 3 ? mt + 4 : 6, L_W1B + 71680);
      waitv<0>(); lset(5, mt);
      __builtin_amdgcn_s_setprio(3);
#pragma unroll
      for (int m = 0; m < 2; m++) {
        landed(sv[m][0]); landed(sv[m][1]);
        const float mean = xsum16_32(asf(sv[m][0].x) + asf(sv[m][1].x)) * (1.f / 640.f);
        const float var = fmaxf(xsum16_32(asf(sv[m][0].y) + asf(sv[m][1].y)) * (1.f / 640.f) - mean * mean, 0.f);
        const float m1 = rbf(mean), sd1 = rbf(__builtin_amdgcn_sqrtf(rbf(rbf(var) + 1e-5f))), r1 = __builtin_amdgcn_rcpf(sd1);
        const float m2 = rbf((mean - m1) * r1), sd2 = rbf(__builtin_amdgcn_sqrtf(rbf(rbf(var * r1 * r1) + 1e-5f)));
        const float sc = r1 * __builtin_amdgcn_rcpf(sd2);
        sts64(L_CB + 2048 + mt * 256 + m * 128 + lr * 8, u2{asu(sc), asu(sc * (m1 + m2 * sd1))});
      }
      __builtin_amdgcn_s_setprio(0);
    }
    lset(6, mt);
    sfor<HB2, 35>(w2t);
    lwait(7);
    {
      const u32 so = WS_W2T + (j * 5 * 54 + 7 * mt) * 1024, ld = mt * 35840;
#pragma nounroll
      for (u32 n = 0; n < 5; n++) {
        waitv<TH2>();
        sfor<0, 7>([&](auto S) {
          constexpr int k = S < 2 ? S : (S < 4 ? S + 2 : (S < 6 ? S - 2 : 6));
          dma1_sc1(rW, l16, so + (n * 54 + k) * 1024, ld + (S * 5 + n) * 1024);
        });
      }
    }
    waitv<0>();
    lset(8, mt);
  }
  {
    f4 da[4][5];
    u4 xf[7][4];
    u2 xh[4][2], hr[3];
    const u32 so = WS_HID + (p * 4 * 54 + s0) * 1024;
    const u32 vh0 = w < 6 ? l8 : l16 + 2048, vh1 = w < 6 ? l8 + 512 : l16 + 2056;
    auto ldx = [&](auto U) {
      constexpr int s = U / 4, a = U % 4;
      if constexpr (s < 2) ldv<s * 1024>(xf[s][a], rW, l16, so + a * 54 * 1024);
      else if constexpr (s < 4) ldv<(s - 2) * 1024>(xf[s][a], rW, vmid, so + (a * 54 + 3) * 1024);
      else if constexpr (s == 4) ldv<2048>(xf[s][a], rW, l16, so + a * 54 * 1024);
      else if constexpr (s == 5) { ldv2<0>(xh[a][0], rW, vh0, so + (a * 54 + 3) * 1024); ldv2<0>(xh[a][1], rW, vh1, so + (a * 54 + 3) * 1024); }
      else ldv<0>(xf[s][a], rW, vlast, so + (a * 54 + 3) * 1024);
    };
    const u32 fo = WS_FHID + ((p * 16 + 2 * w) * 2 + (lane & 3)) * 16;
    const u32 rw = opaque(L_RED + (w >> 1) * 1024 + l16 + (w & 1) * 8);
    auto put = [&](auto A, auto N) {
      pin(da[A][N]);
      sts64(rw + (A * 5 + N) * 4096, u2{pk2(da[A][N][0], da[A][N][1]), pk2(da[A][N][2], da[A][N][3])});
    };
    {
      auto ldu = [&](auto V) { constexpr int v = V; ldx(IC<(v < 16 ? v : 16 + 4 * ((v - 16) % 3) + (v - 16) / 3)>{}); };
      poll(rW, fo, tag);
      sfor<0, d_issued(0)>(ldu);
      sfor<0, 28>([&](auto U) {
        constexpr int u = U, s = u < 16 ? u / 4 : 4 + (u - 16) % 3, a = u < 16 ? u % 4 : (u - 16) / 3;
        waitv<d_younger(u)>();
        if constexpr (u == 16) lwait(9);
        sfor<0, 5>([&](auto N) {
          if constexpr (s == 0) mm0(da[a][N], w2f[N], xf[s][a]);
          else if constexpr (s == 5) mmh(da[a][N], w2f[5 * s + N], xh[a][0], xh[a][1]);
          else mm(da[a][N], w2f[5 * s + N], xf[s][a]);
          if constexpr (s == 5 && a > 0 && N < 3) { put(IC<a - 1>{}, N); __builtin_amdgcn_sched_barrier(0); }
          if constexpr (s == 6 && a > 0 && N < 2) { put(IC<a - 1>{}, IC<3 + N>{}); __builtin_amdgcn_sched_barrier(0); }
        });
        if constexpr (u == DLP) poll(rW, fo + 8, tag);
        sfor<d_issued(u), d_issued(u + 1)>(ldu);
      });
    }
    MM_DONE();
    sfor<0, 5>([&](auto N) { put(IC<3>{}, N); });
    const rsrc_t rOut = __builtin_amdgcn_make_buffer_rsrc((void*)out, 0, 2048 * 1280, 0x00020000);
    const u32 LO = opaque(0x00003F80u), HI = opaque(0x3F800000u), n0 = 3 * nh;
    const u32 ra = opaque(L_RED + (mt * 5 + n0) * 4096 + l16);
    u2 bb[3];
#pragma unroll
    for (int i = 0; i < 3; i++) {
      bb[i] = lds64(L_CB + 1024 + (n0 + i) * 32 + g * 8);
      hr[i] = lds64(L_HR + (mt * 5 + n0 + i) * 512 + l8);
    }
    BAR();
    u4 pv[3][4];
#pragma unroll
    for (int i = 0; i < 3; i++)
#pragma unroll
      for (int q = 0; q < 4; q++) pv[i][q] = lds128(ra + i * 4096 + q * 1024);
#pragma unroll
    for (int i = 0; i < 3; i++) {
      float s0 = 0.f, s1 = 0.f, s2 = 0.f, s3 = 0.f;
#pragma unroll
      for (int q = 0; q < 4; q++) {
        s0 = dot2c(pv[i][q].x, LO, s0); s1 = dot2c(pv[i][q].x, HI, s1); s2 = dot2c(pv[i][q].y, LO, s2); s3 = dot2c(pv[i][q].y, HI, s3);
        s0 = dot2c(pv[i][q].z, LO, s0); s1 = dot2c(pv[i][q].z, HI, s1); s2 = dot2c(pv[i][q].w, LO, s2); s3 = dot2c(pv[i][q].w, HI, s3);
      }
      const u32 o0 = pk2(unp(hr[i].x) + unp(pk2(f2{s0, s1} + unp(bb[i].x)))), o1 = pk2(unp(hr[i].y) + unp(pk2(f2{s2, s3} + unp(bb[i].y))));
      __builtin_amdgcn_raw_buffer_store_b64(u2{o0, o1}, rOut, (i == 2 && helper) ? OOB : lr * 1280 + g * 8 + i * 32, (p * 64 + mt * 16) * 1280 + j * 160 + n0 * 32, 0);
    }
  }
}

extern "C" void kernel_launch(void* const* d_in, const int* in_sizes, int n_in, void* d_out, int out_size, void* d_ws, size_t ws_size,
                              hipStream_t stream) {
  fused_block<<<256, 512, 0, stream>>>((const u16*)d_in[0], (const u16*)d_in[1], (const u16*)d_in[2], (const u16*)d_in[4], (const u16*)d_in[5],
                                       (const u16*)d_in[6], (const u16*)d_in[7], (u16*)d_out, (char*)d_ws);
}
```
